```python
import torch

def _softmax(x, axis=-1):
    return torch.softmax(x, dim=axis)
def _gelu(x):
    return torch.nn.functional.gelu(x, approximate="tanh")
def _silu(x):
    return torch.nn.functional.silu(x)
def _sigmoid(x):
    return torch.sigmoid(x)
def _layernorm(x, axis=-1, eps=1e-5):
    m = x.mean(dim=axis, keepdim=True)
    v = x.var(dim=axis, keepdim=True, unbiased=False)
    return (x - m) / torch.sqrt(v + eps)
def _scatter_add(tgt, idx, src, axis=0):
    return tgt.clone().index_add_(axis, idx, src)
def _segment_sum(src, seg, n):
    out = torch.zeros((n,) + src.shape[1:], dtype=src.dtype)
    return out.index_add_(0, seg, src)
def _pad(x, pw):
    flat = tuple(v for pair in reversed(pw) for v in pair)
    return torch.nn.functional.pad(x, flat)

kernel_name = 'sequential_preLN__ssm_conv_s4d__mixer_token_mlp__prompt'

def setup_inputs():

    _w = (lambda a: (lambda t: t.to(torch.bfloat16) if t.is_floating_point() else t)(torch.as_tensor(a)))
    def _A_setup():
        Bsz, L, D, N = 1, 2048, 640, 128
        return {
            "u": _w((lambda *s: torch.randn(*s, dtype=torch.bfloat16) * 0.05)(Bsz, L, D)),
            "log_A_real": _w((lambda *s: torch.randn(*s, dtype=torch.bfloat16) * 6.4)(D, N) * 0.5),
            "A_imag": _w((lambda *s: torch.randn(*s, dtype=torch.bfloat16) * 0.05)(D, N) * 3.14),
            "C_real": _w((lambda *s: torch.randn(*s, dtype=torch.bfloat16) * 0.05)(D, N) / N ** 0.5),
            "C_imag": _w((lambda *s: torch.randn(*s, dtype=torch.bfloat16) * 0.05)(D, N) / N ** 0.5),
            "log_dt": _w((lambda *s: torch.randn(*s, dtype=torch.bfloat16) * 0.05)(D) * 0.5 - 4.0),
            "D_skip": _w((lambda *s: torch.randn(*s, dtype=torch.bfloat16) * 0.05)(D)),
        }
    _A_in = _A_setup()

    def _B_setup():
        B, S, D, F = 1, 2048, 640, 1728
        return {
            "x": _w((lambda *s: torch.randn(*s, dtype=torch.bfloat16) * 0.05)(B, S, D)),
            "w1": _w((lambda *s: torch.randn(*s, dtype=torch.bfloat16) * 0.4)(S, F) * (S ** -0.5)),
            "b1": _w((lambda *s: torch.randn(*s, dtype=torch.bfloat16) * 12.8)(F) * 0.02),
            "w2": _w((lambda *s: torch.randn(*s, dtype=torch.bfloat16) * 0.2)(F, S) * (F ** -0.5)),
            "b2": _w((lambda *s: torch.randn(*s, dtype=torch.bfloat16) * 3.2)(S) * 0.02),
        }
    _B_in = _B_setup()

    return {
        "A_u": _A_in["u"],
        "A_log_A_real": _A_in["log_A_real"],
        "A_A_imag": _A_in["A_imag"],
        "A_C_real": _A_in["C_real"],
        "A_C_imag": _A_in["C_imag"],
        "A_log_dt": _A_in["log_dt"],
        "A_D_skip": _A_in["D_skip"],
        "B_x": _B_in["x"],
        "B_w1": _B_in["w1"],
        "B_b1": _B_in["b1"],
        "B_w2": _B_in["w2"],
        "B_b2": _B_in["b2"],
    }

def reference(A_u, A_log_A_real, A_A_imag, A_C_real, A_C_imag, A_log_dt, A_D_skip, B_x, B_w1, B_b1, B_w2, B_b2):
    def _A_ref(u, log_A_real, A_imag, C_real, C_imag, log_dt, D_skip):
        L = u.shape[1]
        dt = torch.exp(log_dt)
        A = -torch.exp(log_A_real) + 1j * A_imag
        C = (C_real + 1j * C_imag) * dt[:, None]
        pos = (lambda x, dt: x.to(dt))(torch.arange(L), log_dt.dtype)
        P = torch.exp((A * dt[:, None])[:, :, None] * pos[None, None, :])
        K = 2.0 * torch.einsum("dn,dnl->dl", C, P).real
        L2 = 2 * L
        u_t = (lambda x, *d: x.permute(*d))(u, 0, 2, 1)
        Kf = (lambda x, *a, **k: torch.fft.rfft(x.float(), *a, **k))(K, L2)
        Uf = (lambda x, *a, **k: torch.fft.rfft(x.float(), *a, **k))(u_t, L2)
        y = (lambda x, *a, **k: torch.fft.irfft(x, *a, **k).to(torch.bfloat16))(Uf * Kf[None, :, :], L2)[:, :, :L]
        y = (lambda x, *d: x.permute(*d))(y, 0, 2, 1)
        return y + u * D_skip[None, None, :]

    def _B_ref(x, w1, b1, w2, b2):
        B, S, D = x.shape
        h = _layernorm(x, axis=-1)
        u = torch.einsum("bsd,sf->bdf", h, w1) + b1
        u = _gelu(u)
        y = torch.einsum("bdf,fs->bds", u, w2) + b2
        return (lambda x, *d: x.permute(*d))(y, 0, 2, 1)

    x = A_u
    h = x + _A_ref(_layernorm(x, axis=-1), A_log_A_real, A_A_imag, A_C_real, A_C_imag, A_log_dt, A_D_skip)
    h2 = h + _B_ref(_layernorm(h, axis=-1), B_w1, B_b1, B_w2, B_b2)
    return h2
```

```cpp
#include <hip/hip_runtime.h>
#include <hip/hip_bf16.h>
#include <stdint.h>

typedef unsigned short u16;
typedef __bf16 bf16x8 __attribute__((ext_vector_type(8)));
typedef __bf16 bf16x2 __attribute__((ext_vector_type(2)));
typedef short s16x4 __attribute__((ext_vector_type(4)));
typedef float f32x4 __attribute__((ext_vector_type(4)));
typedef float f32x2 __attribute__((ext_vector_type(2)));
typedef unsigned u32x4 __attribute__((ext_vector_type(4)));
typedef unsigned u32x2 __attribute__((ext_vector_type(2)));
#define LDS_AS __attribute__((address_space(3)))

constexpr int L_ = 2048;
constexpr int D_ = 640;
constexpr int N_ = 128;
constexpr int F_ = 1728;
constexpr int KROW = 2064;

__device__ __forceinline__ float bf2f(unsigned v) { return __uint_as_float(v << 16); }
__device__ __forceinline__ float bflo(unsigned v) { return __uint_as_float(v << 16); }
__device__ __forceinline__ float bfhi(unsigned v) { return __uint_as_float(v & 0xffff0000u); }
__device__ __forceinline__ unsigned pk2(float lo, float hi) {
    f32x2 f = {lo, hi};
    bf16x2 b = __builtin_convertvector(f, bf16x2);
    return __builtin_bit_cast(unsigned, b);
}
__device__ __forceinline__ float rb(float x) { return bflo(pk2(x, x)); }

__device__ __forceinline__ f32x4 mfma16(u32x4 a, u32x4 b, f32x4 c) {
    return __builtin_amdgcn_mfma_f32_16x16x32_bf16(__builtin_bit_cast(bf16x8, a), __builtin_bit_cast(bf16x8, b), c, 0, 0, 0);
}
__device__ __forceinline__ u32x2 tr_read(const char* p) {
    s16x4 v = __builtin_amdgcn_ds_read_tr16_b64_v4i16((LDS_AS s16x4*)p);
    return __builtin_bit_cast(u32x2, v);
}
template <int CTRL>
__device__ __forceinline__ float dpp_add(float v) {
    int t = __builtin_amdgcn_update_dpp(0, __float_as_int(v), CTRL, 0xf, 0xf, true);
    return v + __int_as_float(t);
}
__device__ __forceinline__ float row16_sum(float v) {
    v = dpp_add<0x128>(v);
    v = dpp_add<0x124>(v);
    v = dpp_add<0x122>(v);
    v = dpp_add<0x121>(v);
    return v;
}

constexpr int K1_ROWB = 528;
constexpr int K1_LDS = 64 * K1_ROWB + 1312;

__device__ __forceinline__ f32x2 cmul(f32x2 a, f32x2 b) {
    f32x2 r;
    r.x = a.x * b.x - a.y * b.y;
    r.y = a.x * b.y + a.y * b.x;
    return r;
}
__device__ __forceinline__ f32x2 cexp_p(float zr, float zi, float p) {
    float mag = __builtin_amdgcn_exp2f(zr * p * 1.44269504088896341f);
    float rev = zi * p * 0.15915494309189535f;
    rev = rev - __builtin_floorf(rev);
    f32x2 r;
    r.x = mag * __builtin_amdgcn_cosf(rev);
    r.y = mag * __builtin_amdgcn_sinf(rev);
    return r;
}

__global__ __launch_bounds__(128) void k1_ln_kgen(const u16* __restrict__ x, const u16* __restrict__ logA,
                                                  const u16* __restrict__ Aim, const u16* __restrict__ Cre,
                                                  const u16* __restrict__ Cim, const u16* __restrict__ logdt,
                                                  u16* __restrict__ ub, u16* __restrict__ xb, u16* __restrict__ kx) {
    __shared__ __attribute__((aligned(16))) char smem[K1_LDS];
    const int tid = threadIdx.x;
    const int lane = tid & 63;
    if (blockIdx.x < 256) {
        const int rg = blockIdx.x;
        const int r = tid >> 4, i = tid & 15;
        const u32x4* xrow = (const u32x4*)(x + (size_t)(rg * 8 + r) * D_);
        u32x4 v[5];
#pragma unroll
        for (int j = 0; j < 5; ++j) v[j] = xrow[i + 16 * j];
        float s = 0.f;
#pragma unroll
        for (int j = 0; j < 5; ++j)
#pragma unroll
            for (int e = 0; e < 4; ++e) s += bflo(v[j][e]) + bfhi(v[j][e]);
        s = row16_sum(s);
        const float mean = s * (1.0f / D_);
        float q = 0.f;
#pragma unroll
        for (int j = 0; j < 5; ++j)
#pragma unroll
            for (int e = 0; e < 4; ++e) {
                float a = bflo(v[j][e]) - mean, b = bfhi(v[j][e]) - mean;
                q += a * a + b * b;
            }
        q = row16_sum(q);
        const float mb = rb(mean);
        const float den = rb(__builtin_sqrtf(rb(rb(q * (1.0f / D_)) + 1e-5f)));
        const float inv = 1.0f / den;
        constexpr int PITCH = D_ * 2 + 32;
        char* tu = smem;
        char* tx = smem + 8 * PITCH;
#pragma unroll
        for (int j = 0; j < 5; ++j) {
            u32x4 o;
#pragma unroll
            for (int e = 0; e < 4; ++e) {
                unsigned t = pk2(bflo(v[j][e]) - mb, bfhi(v[j][e]) - mb);
                o[e] = pk2(bflo(t) * inv, bfhi(t) * inv);
            }
            *(u32x4*)(tu + r * PITCH + (i + 16 * j) * 16) = o;
            *(u32x4*)(tx + r * PITCH + (i + 16 * j) * 16) = v[j];
        }
        __syncthreads();
        const int q4 = i >> 2, p4 = i & 3;
#pragma unroll
        for (int j = 0; j < 5; ++j) {
            const int c0 = (j * 8 + r) * 16;
            const int off = q4 * PITCH + (c0 + 4 * p4) * 2;
            u32x2 a0 = tr_read(tu + off), a1 = tr_read(tu + off + 4 * PITCH);
            u32x2 b0 = tr_read(tx + off), b1 = tr_read(tx + off + 4 * PITCH);
            u32x4 ou = {a0.x, a0.y, a1.x, a1.y}, ox = {b0.x, b0.y, b1.x, b1.y};
            const size_t g = ((size_t)rg * D_ + c0 + i) * 8;
            *(u32x4*)(ub + g) = ou;
            *(u32x4*)(xb + g) = ox;
        }
        return;
    }
    const int d = blockIdx.x - 256;
    {
        const int n = tid;
        const float dt = rb(expf(bf2f(logdt[d])));
        const float ar = -rb(expf(bf2f(logA[d * N_ + n])));
        const float zr = ar * dt, zi = bf2f(Aim[d * N_ + n]) * dt;
        f32x2 c2 = {2.0f * (bf2f(Cre[d * N_ + n]) * dt), 2.0f * (bf2f(Cim[d * N_ + n]) * dt)};
        const f32x2 E1 = cexp_p(zr, zi, 1.f), E8 = cexp_p(zr, zi, 8.f), E128 = cexp_p(zr, zi, 128.f);
        char* col = smem + n * 4;
        auto putA = [&](int row, f32x2 v) { *(unsigned*)(col + row * K1_ROWB) = pk2(v.x, -v.y); };
        auto putB = [&](int row, f32x2 v) { *(unsigned*)(col + (48 + row) * K1_ROWB) = pk2(v.x, v.y); };
        f32x2 b = {1.f, 0.f};
        putB(0, b);
        b = E8;
        putB(1, b);
#pragma unroll
        for (int c = 2; c < 16; ++c) { b = cmul(b, E8); putB(c, b); }
        f32x2 e[8];
        e[1] = E1;
#pragma unroll
        for (int k = 2; k < 8; ++k) e[k] = cmul(e[k - 1], E1);
        f32x2 a = c2;
#pragma unroll
        for (int c = 0; c < 17; ++c) {
            if (c < 2) {
                putA(8 * c, a);
#pragma unroll
                for (int k = 1; k < 8; ++k) putA(8 * c + k, cmul(a, e[k]));
            } else if (c < 4) {
                putA(16 + 4 * (c - 2), a);
#pragma unroll
                for (int k = 1; k < 4; ++k) putA(16 + 4 * (c - 2) + k, cmul(a, e[2 * k]));
            } else if (c < 8) {
                putA(24 + 2 * (c - 4), a);
                putA(24 + 2 * (c - 4) + 1, cmul(a, e[4]));
            } else {
                putA(32 + (c - 8), a);
            }
            if (c < 16) a = cmul(a, E128);
        }
    }
    __syncthreads();
    u16* kd = (u16*)(smem + 64 * K1_ROWB);
    {
        const int w = tid >> 6, m = lane & 15, hg = lane >> 4;
        const char* fb = smem + m * K1_ROWB + hg * 16;
        f32x4 acc0 = {0.f, 0.f, 0.f, 0.f}, acc1 = {0.f, 0.f, 0.f, 0.f};
#pragma unroll
        for (int ks = 0; ks < 8; ++ks) {
            u32x4 bf = *(const u32x4*)(fb + 48 * K1_ROWB + ks * 64);
            u32x4 a0 = *(const u32x4*)(fb + (w * 16) * K1_ROWB + ks * 64);
            u32x4 a2 = *(const u32x4*)(fb + 32 * K1_ROWB + ks * 64);
            acc0 = mfma16(a0, bf, acc0);
            acc1 = mfma16(a2, bf, acc1);
        }
        const int c = m;
        const unsigned p01 = pk2(acc0[0], acc0[1]), p23 = pk2(acc0[2], acc0[3]);
        if (w == 0) {
            u32x2 o = {p01, p23};
            *(u32x2*)(kd + 128 * (hg >> 1) + 4 * (hg & 1) + 8 * c) = o;
        } else {
            if (hg < 2) {
                u32x2 o = {p01, p23};
                *(u32x2*)(kd + 256 + 64 * hg + 4 * c) = o;
            } else {
                *(unsigned*)(kd + 384 + 32 * (2 * (hg - 2)) + 2 * c) = p01;
                *(unsigned*)(kd + 384 + 32 * (2 * (hg - 2) + 1) + 2 * c) = p23;
            }
            const unsigned q01 = pk2(acc1[0], acc1[1]), q23 = pk2(acc1[2], acc1[3]);
            if (hg < 2) {
                kd[512 + 16 * (4 * hg + 0) + c] = (u16)(q01 & 0xffff);
                kd[512 + 16 * (4 * hg + 1) + c] = (u16)(q01 >> 16);
                kd[512 + 16 * (4 * hg + 2) + c] = (u16)(q23 & 0xffff);
                kd[512 + 16 * (4 * hg + 3) + c] = (u16)(q23 >> 16);
            } else if (hg == 2 && c == 0) {
                kd[640] = (u16)(q01 & 0xffff);
            }
        }
    }
    __syncthreads();
    u16* krow = kx + (size_t)d * KROW;
#pragma unroll
    for (int it = 0; it < 2; ++it) {
        const int j = tid + 128 * it;
        u32x4 o;
#pragma unroll
        for (int e = 0; e < 4; ++e) {
            unsigned hv[2];
#pragma unroll
            for (int h = 0; h < 2; ++h) {
                const int l = 2047 - 8 * j - (2 * e + h);
                const unsigned bits = __float_as_uint((float)l);
                const int b16 = (int)((bits + 0x7fffu + ((bits >> 16) & 1u)) >> 16);
                const int idx = l < 128 ? l : b16 - 17024;
                hv[h] = kd[idx];
            }
            o[e] = hv[0] | (hv[1] << 16);
        }
        *(u32x4*)(krow + 8 * j) = o;
    }
    if (tid < 2) {
        u32x4 z = {0u, 0u, 0u, 0u};
        *(u32x4*)(krow + 2048 + 8 * tid) = z;
    }
}

constexpr int CV_COPY = 4160;
constexpr int CV_KB = 4 * CV_COPY;
constexpr int CV_UB = 4576 + 32;
constexpr int CV_XB = 4096 + 32;
constexpr int CV_CH = CV_KB + CV_UB + CV_XB;

__global__ __launch_bounds__(256) void k2_conv(const u16* __restrict__ ub, const u16* __restrict__ xb,
                                               const u16* __restrict__ kx, const u16* __restrict__ dskip,
                                               u16* __restrict__ hb) {
    __shared__ __attribute__((aligned(16))) char smem[4 * CV_CH];
    const int tid = threadIdx.x, lane = tid & 63;
    const int w = __builtin_amdgcn_readfirstlane(tid >> 6);
    const int grp = (blockIdx.x & 7) * 20 + (blockIdx.x >> 3);
    const int d0 = grp * 4;
    const int d = d0 + w;
    u32x4 vu[4], vx[4];
    {
        const int ch = tid & 3;
#pragma unroll
        for (int i = 0; i < 4; ++i) {
            const int piece = (tid >> 2) + 64 * i;
            const size_t g = ((size_t)piece * D_ + d0 + ch) * 8;
            vu[i] = *(const u32x4*)(ub + g);
            vx[i] = *(const u32x4*)(xb + g);
        }
    }
    u32x4 k4[4];
    u32x2 k2[4];
    {
        const u16* krow = kx + (size_t)d * KROW;
#pragma unroll
        for (int i = 0; i < 4; ++i) {
            const int j = lane + 64 * i;
            k4[i] = *(const u32x4*)(krow + 8 * j);
            k2[i] = *(const u32x2*)(krow + 8 * j + 8);
        }
    }
    const float dsk = bf2f(dskip[d]);
    char* mine = smem + w * CV_CH;
    {
        u32x4 z = {0u, 0u, 0u, 0u};
        if (lane < 30) *(u32x4*)(mine + CV_KB + lane * 16) = z;
        else if (lane >= 32 && lane < 40) *(u32x4*)(mine + ((lane - 32) >> 1) * CV_COPY + 4096 + (lane & 1) * 16) = z;
    }
#pragma unroll
    for (int i = 0; i < 4; ++i) {
        const int j = lane + 64 * i;
        const unsigned r0 = k4[i][0], r1 = k4[i][1], r2 = k4[i][2], r3 = k4[i][3], r4 = k2[i][0], r5 = k2[i][1];
        const unsigned s0 = __builtin_amdgcn_alignbit(r1, r0, 16), s1 = __builtin_amdgcn_alignbit(r2, r1, 16),
                       s2 = __builtin_amdgcn_alignbit(r3, r2, 16), s3 = __builtin_amdgcn_alignbit(r4, r3, 16),
                       s4 = __builtin_amdgcn_alignbit(r5, r4, 16);
        u32x4 c1 = {s0, s1, s2, s3}, c2 = {r1, r2, r3, r4}, c3 = {s1, s2, s3, s4};
        *(u32x4*)(mine + 0 * CV_COPY + j * 16) = k4[i];
        *(u32x4*)(mine + 1 * CV_COPY + j * 16) = c1;
        *(u32x4*)(mine + 2 * CV_COPY + j * 16) = c2;
        *(u32x4*)(mine + 3 * CV_COPY + j * 16) = c3;
    }
    {
        char* cb = smem + (tid & 3) * CV_CH + CV_KB;
#pragma unroll
        for (int i = 0; i < 4; ++i) {
            const int piece = (tid >> 2) + 64 * i;
            *(u32x4*)(cb + 480 + piece * 16) = vu[i];
            *(u32x4*)(cb + CV_UB + piece * 16) = vx[i];
        }
    }
    __syncthreads();
    const int m = lane & 15, hg = lane >> 4;
    const char* abase = mine + (3 - (m & 3)) * CV_COPY + 2 * (2028 - 32 * 63 - 4 * (m >> 2) + 8 * hg);
    const char* bbase = mine + CV_KB + (2 * m + hg) * 16;
    f32x4 acc[8];
#pragma unroll
    for (int q = 0; q < 8; ++q) acc[q] = (f32x4){0.f, 0.f, 0.f, 0.f};
#pragma unroll
    for (int g0 = 0; g0 < 8; ++g0) {
        u32x4 af[8], bf[8];
#pragma unroll
        for (int g1 = 0; g1 < 8; ++g1) {
            const int g = 8 * g1 + g0;
            const u32x2 lo = *(const u32x2*)(abase + (63 - g) * 64);
            const u32x2 hi = *(const u32x2*)(abase + (63 - g) * 64 + 8);
            af[g1] = (u32x4){lo.x, lo.y, hi.x, hi.y};
            bf[g1] = *(const u32x4*)(bbase + (8 * g1 - g0 + 7) * 64);
        }
#pragma unroll
        for (int q = 0; q < 8; ++q)
#pragma unroll
            for (int g1 = 0; g1 <= q; ++g1) acc[q] = mfma16(af[g1], bf[q - g1], acc[q]);
    }
    {
        const char* up = mine + CV_KB + 480 + (16 * m + 4 * hg) * 2;
        char* xp = mine + CV_KB + CV_UB + (16 * m + 4 * hg) * 2;
#pragma unroll
        for (int q = 0; q < 8; ++q) {
            const u32x2 uu = *(const u32x2*)(up + q * 512);
            const u32x2 xx = *(const u32x2*)(xp + q * 512);
            const unsigned y01 = pk2(acc[q][0], acc[q][1]), y23 = pk2(acc[q][2], acc[q][3]);
            const unsigned s01 = pk2(bflo(uu.x) * dsk, bfhi(uu.x) * dsk), s23 = pk2(bflo(uu.y) * dsk, bfhi(uu.y) * dsk);
            const unsigned a01 = pk2(bflo(y01) + bflo(s01), bfhi(y01) + bfhi(s01));
            const unsigned a23 = pk2(bflo(y23) + bflo(s23), bfhi(y23) + bfhi(s23));
            u32x2 o;
            o.x = pk2(bflo(xx.x) + bflo(a01), bfhi(xx.x) + bfhi(a01));
            o.y = pk2(bflo(xx.y) + bflo(a23), bfhi(xx.y) + bfhi(a23));
            *(u32x2*)(xp + q * 512) = o;
        }
    }
    __syncthreads();
    {
        const int ch = tid & 3;
        const char* cb = smem + ch * CV_CH + CV_KB + CV_UB;
#pragma unroll
        for (int i = 0; i < 4; ++i) {
            const int piece = (tid >> 2) + 64 * i;
            *(u32x4*)(hb + ((size_t)piece * D_ + d0 + ch) * 8) = *(const u32x4*)(cb + piece * 16);
        }
    }
}

__device__ __forceinline__ float xor_lane(float v, int mask) { return __shfl_xor(v, mask, 64); }

__device__ __forceinline__ void block_stats(const float (&v)[8], float* red, int tid, float (&mb)[8], float (&inv)[8]) {
    const int lane = tid & 63, w = tid >> 6;
    float a[16];
#pragma unroll
    for (int e = 0; e < 8; ++e) { a[e] = v[e]; a[8 + e] = v[e] * v[e]; }
#pragma unroll
    for (int e = 0; e < 8; ++e) {
        const bool up = lane & 1;
        const float keep = up ? a[8 + e] : a[e], send = up ? a[e] : a[8 + e];
        a[e] = keep + xor_lane(send, 1);
    }
#pragma unroll
    for (int e = 0; e < 4; ++e) {
        const bool up = lane & 2;
        const float keep = up ? a[4 + e] : a[e], send = up ? a[e] : a[4 + e];
        a[e] = keep + xor_lane(send, 2);
    }
#pragma unroll
    for (int e = 0; e < 2; ++e) {
        const bool up = lane & 4;
        const float keep = up ? a[2 + e] : a[e], send = up ? a[e] : a[2 + e];
        a[e] = keep + xor_lane(send, 4);
    }
    {
        const bool up = lane & 8;
        const float keep = up ? a[1] : a[0], send = up ? a[0] : a[1];
        a[0] = keep + xor_lane(send, 8);
    }
    a[0] += xor_lane(a[0], 16);
    a[0] += xor_lane(a[0], 32);
    if (lane < 16) {
        const int row = ((lane >> 1) & 1) * 4 + ((lane >> 2) & 1) * 2 + ((lane >> 3) & 1);
        red[w * 16 + (lane & 1) * 8 + row] = a[0];
    }
    __syncthreads();
    if (tid < 8) {
        float s = 0.f, q = 0.f;
#pragma unroll
        for (int k = 0; k < 10; ++k) { s += red[k * 16 + tid]; q += red[k * 16 + 8 + tid]; }
        const float mean = s * (1.0f / D_);
        const float var = fmaxf(q * (1.0f / D_) - mean * mean, 0.f);
        const float den = rb(__builtin_sqrtf(rb(rb(var) + 1e-5f)));
        red[160 + tid] = rb(mean);
        red[168 + tid] = 1.0f / den;
    }
    __syncthreads();
#pragma unroll
    for (int e = 0; e < 8; ++e) { mb[e] = red[160 + e]; inv[e] = red[168 + e]; }
}

__global__ __launch_bounds__(640) void k3_ln2(const u16* __restrict__ hb, u16* __restrict__ hn) {
    __shared__ __attribute__((aligned(16))) float red[2][176];
    const int tid = threadIdx.x;
    const size_t g = ((size_t)blockIdx.x * D_ + tid) * 8;
    const u32x4 p = *(const u32x4*)(hb + g);
    float v[8], mb[8], inv[8];
#pragma unroll
    for (int e = 0; e < 4; ++e) { v[2 * e] = bflo(p[e]); v[2 * e + 1] = bfhi(p[e]); }
    block_stats(v, red[0], tid, mb, inv);
#pragma unroll
    for (int e = 0; e < 4; ++e) {
        const unsigned t = pk2(v[2 * e] - mb[2 * e], v[2 * e + 1] - mb[2 * e + 1]);
        const unsigned o = pk2(bflo(t) * inv[2 * e], bfhi(t) * inv[2 * e + 1]);
        v[2 * e] = bflo(o);
        v[2 * e + 1] = bfhi(o);
    }
    block_stats(v, red[1], tid, mb, inv);
    u32x4 o;
#pragma unroll
    for (int e = 0; e < 4; ++e) {
        const unsigned t = pk2(v[2 * e] - mb[2 * e], v[2 * e + 1] - mb[2 * e + 1]);
        o[e] = pk2(bflo(t) * inv[2 * e], bfhi(t) * inv[2 * e + 1]);
    }
    *(u32x4*)(hn + g) = o;
}

constexpr int G_STAGE = 9216;
constexpr int G_DEPTH = 4;
constexpr int G_WAVE = G_STAGE * G_DEPTH;

__device__ __forceinline__ void glds16(const void* g, char* l) {
    __builtin_amdgcn_global_load_lds((const __attribute__((address_space(1))) unsigned*)g, (LDS_AS unsigned*)l, 16, 0, 0);
}
__device__ __forceinline__ float gelu_tanh(float x) {
    const float u = 0.7978845608028654f * (x + 0.044715f * x * x * x);
    const float e = __builtin_amdgcn_exp2f(-2.885390081777927f * u);
    return x * __builtin_amdgcn_rcpf(1.0f + e);
}

template <int MODE>
__global__ __launch_bounds__(256) void k45_gemm(const u16* __restrict__ W, const u16* __restrict__ act,
                                                const u16* __restrict__ bias, const u16* __restrict__ hb,
                                                u16* __restrict__ out) {
    constexpr int LDW = MODE == 0 ? F_ : L_;
    constexpr int KSTEPS = MODE == 0 ? L_ / 32 : F_ / 32;
    __shared__ __attribute__((aligned(16))) char smem[4 * G_WAVE];
    const int tid = threadIdx.x, lane = tid & 63;
    const int w = __builtin_amdgcn_readfirstlane(tid >> 6);
    int mt, dt;
    {
        const int xcd = blockIdx.x & 7, idx = blockIdx.x >> 3;
        constexpr int PER = MODE == 0 ? 27 : 32;
        const int t = (xcd >> 1) * PER + idx;
        mt = t >> 2;
        dt = (xcd & 1) * 4 + (t & 3);
    }
    const int m0 = mt * 64, d0 = dt * 80;
    const int ks0 = MODE == 0 ? w * 16 : (w * 14 - (w > 2 ? 1 : 0));
    const int nks = MODE == 0 ? 16 : (w < 2 ? 14 : 13);
    char* ring = smem + w * G_WAVE;
    const int wr = lane >> 3, wslot = lane & 7;
    const u16* wsrc0 = W + (size_t)(ks0 * 32 + wr) * LDW + m0 + ((((wslot >> 1) ^ ((wr >> 1) & 1)) * 2 + (wslot & 1)) * 8);
    const u16* wsrc1 = W + (size_t)(ks0 * 32 + wr + 8) * LDW + m0 + ((((wslot >> 1) ^ (((wr >> 1) & 1) | 2)) * 2 + (wslot & 1)) * 8);
    const int n = lane & 15, hg = lane >> 4;
    const u16* asrc = act + ((size_t)(ks0 * 4 + hg) * D_ + d0 + n) * 8;
    auto issue = [&](int s) {
        char* dst = ring + (s & (G_DEPTH - 1)) * G_STAGE;
        const size_t wo = (size_t)s * 32 * LDW;
        glds16(wsrc0 + wo, dst);
        glds16(wsrc1 + wo, dst + 1024);
        glds16(wsrc0 + wo + 16 * LDW, dst + 2048);
        glds16(wsrc1 + wo + 16 * LDW, dst + 3072);
        const size_t ao = (size_t)s * 4 * D_ * 8;
#pragma unroll
        for (int j = 0; j < 5; ++j) glds16(asrc + ao + j * 128, dst + 4096 + j * 1024);
    };
    f32x4 acc[4][5];
#pragma unroll
    for (int c = 0; c < 4; ++c)
#pragma unroll
        for (int j = 0; j < 5; ++j) acc[c][j] = (f32x4){0.f, 0.f, 0.f, 0.f};
    const int q4 = n >> 2, p4 = n & 3;
    const int fsw = ((q4 >> 1) & 1) | ((hg & 1) << 1);
    const int troff = (8 * hg + q4) * 128 + p4 * 8;
    issue(0);
    issue(1);
    issue(2);
    for (int s = 0; s < nks; ++s) {
        if (s + 3 < nks) {
            issue(s + 3);
            asm volatile("s_waitcnt vmcnt(27)" ::: "memory");
        } else if (s + 2 < nks) {
            asm volatile("s_waitcnt vmcnt(18)" ::: "memory");
        } else if (s + 1 < nks) {
            asm volatile("s_waitcnt vmcnt(9)" ::: "memory");
        } else {
            asm volatile("s_waitcnt vmcnt(0)" ::: "memory");
        }
        const char* st = ring + (s & (G_DEPTH - 1)) * G_STAGE;
        u32x4 wf[4], af[5];
#pragma unroll
        for (int c = 0; c < 4; ++c) {
            const u32x2 lo = tr_read(st + troff + ((c ^ fsw) * 32));
            const u32x2 hi = tr_read(st + troff + 512 + ((c ^ fsw) * 32));
            wf[c] = (u32x4){lo.x, lo.y, hi.x, hi.y};
        }
#pragma unroll
        for (int j = 0; j < 5; ++j) af[j] = *(const u32x4*)(st + 4096 + j * 1024 + lane * 16);
#pragma unroll
        for (int c = 0; c < 4; ++c)
#pragma unroll
            for (int j = 0; j < 5; ++j) acc[c][j] = mfma16(wf[c], af[j], acc[c][j]);
        asm volatile("" ::: "memory");
    }
    __syncthreads();
#pragma unroll
    for (int c = 0; c < 4; ++c)
#pragma unroll
        for (int j = 0; j < 5; ++j)
            if (c != w) *(f32x4*)(smem + (((c * 4 + w) * 5 + j) * 64 + lane) * 16) = acc[c][j];
    __syncthreads();
    f32x4 r[5];
#pragma unroll
    for (int j = 0; j < 5; ++j) {
        r[j] = w == 0 ? acc[0][j] : w == 1 ? acc[1][j] : w == 2 ? acc[2][j] : acc[3][j];
    }
#pragma unroll
    for (int o = 0; o < 4; ++o)
        if (o != w) {
#pragma unroll
            for (int j = 0; j < 5; ++j) r[j] += *(const f32x4*)(smem + (((w * 4 + o) * 5 + j) * 64 + lane) * 16);
        }
    const int mrow = m0 + 16 * w + 4 * hg;
    const u32x2 bb = *(const u32x2*)(bias + mrow);
    const float b0 = bflo(bb.x), b1 = bfhi(bb.x), b2 = bflo(bb.y), b3 = bfhi(bb.y);
    const size_t goff = ((size_t)(mrow >> 3) * D_ + d0 + n) * 8 + (hg & 1) * 4;
    if (MODE == 0) {
#pragma unroll
        for (int j = 0; j < 5; ++j) {
            const unsigned y01 = pk2(r[j][0], r[j][1]), y23 = pk2(r[j][2], r[j][3]);
            const unsigned z01 = pk2(bflo(y01) + b0, bfhi(y01) + b1), z23 = pk2(bflo(y23) + b2, bfhi(y23) + b3);
            u32x2 o;
            o.x = pk2(gelu_tanh(bflo(z01)), gelu_tanh(bfhi(z01)));
            o.y = pk2(gelu_tanh(bflo(z23)), gelu_tanh(bfhi(z23)));
            *(u32x2*)(out + goff + j * 128) = o;
        }
    } else {
        char* img = smem + 81920 + w * 4096;
#pragma unroll
        for (int j = 0; j < 5; ++j) {
            const u32x2 hh = *(const u32x2*)(hb + goff + j * 128);
            const unsigned y01 = pk2(r[j][0], r[j][1]), y23 = pk2(r[j][2], r[j][3]);
            const unsigned z01 = pk2(bflo(y01) + b0, bfhi(y01) + b1), z23 = pk2(bflo(y23) + b2, bfhi(y23) + b3);
            u32x2 o;
            o.x = pk2(bflo(hh.x) + bflo(z01), bfhi(hh.x) + bfhi(z01));
            o.y = pk2(bflo(hh.y) + bflo(z23), bfhi(hh.y) + bfhi(z23));
            *(u32x2*)(img + (16 * j + n) * 40 + hg * 8) = o;
        }
        asm volatile("s_waitcnt lgkmcnt(0)" ::: "memory");
#pragma unroll
        for (int i = 0; i < 3; ++i) {
            int k = hg + 4 * i;
            const bool ok = k < 10;
            k = ok ? k : 0;
            const u32x2 lo = tr_read(img + (8 * k + q4) * 40 + p4 * 8);
            const u32x2 hi = tr_read(img + (8 * k + 4 + q4) * 40 + p4 * 8);
            u32x4 o = {lo.x, lo.y, hi.x, hi.y};
            if (ok) *(u32x4*)(out + (size_t)(m0 + 16 * w + n) * D_ + d0 + 8 * k) = o;
        }
    }
}

extern "C" void kernel_launch(void* const* d_in, const int* in_sizes, int n_in, void* d_out, int out_size,
                              void* d_ws, size_t ws_size, hipStream_t stream) {
    const u16* x = (const u16*)d_in[0];
    const u16* logA = (const u16*)d_in[1];
    const u16* Aim = (const u16*)d_in[2];
    const u16* Cre = (const u16*)d_in[3];
    const u16* Cim = (const u16*)d_in[4];
    const u16* logdt = (const u16*)d_in[5];
    const u16* dskip = (const u16*)d_in[6];
    const u16* w1 = (const u16*)d_in[8];
    const u16* b1 = (const u16*)d_in[9];
    const u16* w2 = (const u16*)d_in[10];
    const u16* b2 = (const u16*)d_in[11];
    char* ws = (char*)d_ws;
    constexpr size_t SZ = (size_t)L_ * D_ * 2;
    u16* ub = (u16*)(ws);
    u16* xb = (u16*)(ws + SZ);
    u16* hb = (u16*)(ws + 2 * SZ);
    u16* hn = (u16*)(ws + 3 * SZ);
    u16* gb = (u16*)(ws + 4 * SZ);
    u16* kx = (u16*)(ws + 5 * SZ);
    k1_ln_kgen<<<256 + D_, 128, 0, stream>>>(x, logA, Aim, Cre, Cim, logdt, ub, xb, kx);
    k2_conv<<<160, 256, 0, stream>>>(ub, xb, kx, dskip, hb);
    k3_ln2<<<256, 640, 0, stream>>>(hb, hn);
    k45_gemm<0><<<216, 256, 0, stream>>>(w1, hn, b1, nullptr, gb);
    k45_gemm<1><<<256, 256, 0, stream>>>(w2, gb, b2, hb, (u16*)d_out);
}
```

```cpp
#include <hip/hip_runtime.h>
#include <hip/hip_bf16.h>
#include <stdint.h>
#pragma clang diagnostic ignored "-Winline-asm"

typedef unsigned short u16;
typedef __bf16 bf16x8 __attribute__((ext_vector_type(8)));
typedef __bf16 bf16x2 __attribute__((ext_vector_type(2)));
typedef short s16x4 __attribute__((ext_vector_type(4)));
typedef float f32x4 __attribute__((ext_vector_type(4)));
typedef float f32x2 __attribute__((ext_vector_type(2)));
typedef unsigned u32x4 __attribute__((ext_vector_type(4)));
typedef unsigned u32x2 __attribute__((ext_vector_type(2)));
#define LDS_AS __attribute__((address_space(3)))

constexpr int L_ = 2048;
constexpr int D_ = 640;
constexpr int N_ = 128;
constexpr int F_ = 1728;
constexpr int KROW = 2064;

__device__ __forceinline__ float bf2f(unsigned v) { return __uint_as_float(v << 16); }
__device__ __forceinline__ float bflo(unsigned v) { return __uint_as_float(v << 16); }
__device__ __forceinline__ float bfhi(unsigned v) { return __uint_as_float(v & 0xffff0000u); }
__device__ __forceinline__ unsigned pk2(float lo, float hi) {
    f32x2 f = {lo, hi};
    bf16x2 b = __builtin_convertvector(f, bf16x2);
    return __builtin_bit_cast(unsigned, b);
}
__device__ __forceinline__ float rb(float x) { return bflo(pk2(x, x)); }

__device__ __forceinline__ f32x4 mfma16(u32x4 a, u32x4 b, f32x4 c) {
    return __builtin_amdgcn_mfma_f32_16x16x32_bf16(__builtin_bit_cast(bf16x8, a), __builtin_bit_cast(bf16x8, b), c, 0, 0, 0);
}
__device__ __forceinline__ u32x2 tr_read(const char* p) {
    s16x4 v = __builtin_amdgcn_ds_read_tr16_b64_v4i16((LDS_AS s16x4*)p);
    return __builtin_bit_cast(u32x2, v);
}
template <int CTRL>
__device__ __forceinline__ float dpp_add(float v) {
    int t = __builtin_amdgcn_update_dpp(0, __float_as_int(v), CTRL, 0xf, 0xf, true);
    return v + __int_as_float(t);
}
__device__ __forceinline__ float row16_sum(float v) {
    v = dpp_add<0x128>(v);
    v = dpp_add<0x124>(v);
    v = dpp_add<0x122>(v);
    v = dpp_add<0x121>(v);
    return v;
}

constexpr int K1_ROWB = 528;
constexpr int K1_LDS = 64 * K1_ROWB + 1312;

__device__ __forceinline__ f32x2 cmul(f32x2 a, f32x2 b) {
    f32x2 r;
    r.x = a.x * b.x - a.y * b.y;
    r.y = a.x * b.y + a.y * b.x;
    return r;
}
__device__ __forceinline__ f32x2 cexp_p(float zr, float zi, float p) {
    float mag = __builtin_amdgcn_exp2f(zr * p * 1.44269504088896341f);
    float rev = zi * p * 0.15915494309189535f;
    rev = rev - __builtin_floorf(rev);
    f32x2 r;
    r.x = mag * __builtin_amdgcn_cosf(rev);
    r.y = mag * __builtin_amdgcn_sinf(rev);
    return r;
}

__global__ __launch_bounds__(128) void k1_ln_kgen(const u16* __restrict__ x, const u16* __restrict__ logA,
                                                  const u16* __restrict__ Aim, const u16* __restrict__ Cre,
                                                  const u16* __restrict__ Cim, const u16* __restrict__ logdt,
                                                  u16* __restrict__ ub, u16* __restrict__ xb, u16* __restrict__ kx) {
    __shared__ __attribute__((aligned(16))) char smem[K1_LDS];
    const int tid = threadIdx.x;
    const int lane = tid & 63;
    if (blockIdx.x < 256) {
        const int rg = blockIdx.x;
        const int r = tid >> 4, i = tid & 15;
        const u32x4* xrow = (const u32x4*)(x + (size_t)(rg * 8 + r) * D_);
        u32x4 v[5];
#pragma unroll
        for (int j = 0; j < 5; ++j) v[j] = xrow[i + 16 * j];
        float s = 0.f;
#pragma unroll
        for (int j = 0; j < 5; ++j)
#pragma unroll
            for (int e = 0; e < 4; ++e) s += bflo(v[j][e]) + bfhi(v[j][e]);
        s = row16_sum(s);
        const float mean = s * (1.0f / D_);
        float q = 0.f;
#pragma unroll
        for (int j = 0; j < 5; ++j)
#pragma unroll
            for (int e = 0; e < 4; ++e) {
                float a = bflo(v[j][e]) - mean, b = bfhi(v[j][e]) - mean;
                q += a * a + b * b;
            }
        q = row16_sum(q);
        const float mb = rb(mean);
        const float den = rb(__builtin_sqrtf(rb(rb(q * (1.0f / D_)) + 1e-5f)));
        const float inv = 1.0f / den;
        constexpr int PITCH = D_ * 2 + 32;
        char* tu = smem;
        char* tx = smem + 8 * PITCH;
#pragma unroll
        for (int j = 0; j < 5; ++j) {
            u32x4 o;
#pragma unroll
            for (int e = 0; e < 4; ++e) {
                unsigned t = pk2(bflo(v[j][e]) - mb, bfhi(v[j][e]) - mb);
                o[e] = pk2(bflo(t) * inv, bfhi(t) * inv);
            }
            *(u32x4*)(tu + r * PITCH + (i + 16 * j) * 16) = o;
            *(u32x4*)(tx + r * PITCH + (i + 16 * j) * 16) = v[j];
        }
        __syncthreads();
        const int q4 = i >> 2, p4 = i & 3;
#pragma unroll
        for (int j = 0; j < 5; ++j) {
            const int c0 = (j * 8 + r) * 16;
            const int off = q4 * PITCH + (c0 + 4 * p4) * 2;
            u32x2 a0 = tr_read(tu + off), a1 = tr_read(tu + off + 4 * PITCH);
            u32x2 b0 = tr_read(tx + off), b1 = tr_read(tx + off + 4 * PITCH);
            u32x4 ou = {a0.x, a0.y, a1.x, a1.y}, ox = {b0.x, b0.y, b1.x, b1.y};
            const size_t g = ((size_t)rg * D_ + c0 + i) * 8;
            *(u32x4*)(ub + g) = ou;
            *(u32x4*)(xb + g) = ox;
        }
        return;
    }
    const int d = blockIdx.x - 256;
    {
        const int n = tid;
        const float dt = rb(expf(bf2f(logdt[d])));
        const float ar = -rb(expf(bf2f(logA[d * N_ + n])));
        const float zr = ar * dt, zi = bf2f(Aim[d * N_ + n]) * dt;
        f32x2 c2 = {2.0f * (bf2f(Cre[d * N_ + n]) * dt), 2.0f * (bf2f(Cim[d * N_ + n]) * dt)};
        const f32x2 E1 = cexp_p(zr, zi, 1.f), E8 = cexp_p(zr, zi, 8.f), E128 = cexp_p(zr, zi, 128.f);
        char* col = smem + n * 4;
        auto putA = [&](int row, f32x2 v) { *(unsigned*)(col + row * K1_ROWB) = pk2(v.x, -v.y); };
        auto putB = [&](int row, f32x2 v) { *(unsigned*)(col + (48 + row) * K1_ROWB) = pk2(v.x, v.y); };
        f32x2 b = {1.f, 0.f};
        putB(0, b);
        b = E8;
        putB(1, b);
#pragma unroll
        for (int c = 2; c < 16; ++c) { b = cmul(b, E8); putB(c, b); }
        f32x2 e[8];
        e[1] = E1;
#pragma unroll
        for (int k = 2; k < 8; ++k) e[k] = cmul(e[k - 1], E1);
        f32x2 a = c2;
#pragma unroll
        for (int c = 0; c < 17; ++c) {
            if (c < 2) {
                putA(8 * c, a);
#pragma unroll
                for (int k = 1; k < 8; ++k) putA(8 * c + k, cmul(a, e[k]));
            } else if (c < 4) {
                putA(16 + 4 * (c - 2), a);
#pragma unroll
                for (int k = 1; k < 4; ++k) putA(16 + 4 * (c - 2) + k, cmul(a, e[2 * k]));
            } else if (c < 8) {
                putA(24 + 2 * (c - 4), a);
                putA(24 + 2 * (c - 4) + 1, cmul(a, e[4]));
            } else {
                putA(32 + (c - 8), a);
            }
            if (c < 16) a = cmul(a, E128);
        }
    }
    __syncthreads();
    u16* kd = (u16*)(smem + 64 * K1_ROWB);
    {
        const int w = tid >> 6, m = lane & 15, hg = lane >> 4;
        const char* fb = smem + m * K1_ROWB + hg * 16;
        f32x4 acc0 = {0.f, 0.f, 0.f, 0.f}, acc1 = {0.f, 0.f, 0.f, 0.f};
#pragma unroll
        for (int ks = 0; ks < 8; ++ks) {
            u32x4 bf = *(const u32x4*)(fb + 48 * K1_ROWB + ks * 64);
            u32x4 a0 = *(const u32x4*)(fb + (w * 16) * K1_ROWB + ks * 64);
            u32x4 a2 = *(const u32x4*)(fb + 32 * K1_ROWB + ks * 64);
            acc0 = mfma16(a0, bf, acc0);
            acc1 = mfma16(a2, bf, acc1);
        }
        const int c = m;
        const unsigned p01 = pk2(acc0[0], acc0[1]), p23 = pk2(acc0[2], acc0[3]);
        if (w == 0) {
            u32x2 o = {p01, p23};
            *(u32x2*)(kd + 128 * (hg >> 1) + 4 * (hg & 1) + 8 * c) = o;
        } else {
            if (hg < 2) {
                u32x2 o = {p01, p23};
                *(u32x2*)(kd + 256 + 64 * hg + 4 * c) = o;
            } else {
                *(unsigned*)(kd + 384 + 32 * (2 * (hg - 2)) + 2 * c) = p01;
                *(unsigned*)(kd + 384 + 32 * (2 * (hg - 2) + 1) + 2 * c) = p23;
            }
            const unsigned q01 = pk2(acc1[0], acc1[1]), q23 = pk2(acc1[2], acc1[3]);
            if (hg < 2) {
                kd[512 + 16 * (4 * hg + 0) + c] = (u16)(q01 & 0xffff);
                kd[512 + 16 * (4 * hg + 1) + c] = (u16)(q01 >> 16);
                kd[512 + 16 * (4 * hg + 2) + c] = (u16)(q23 & 0xffff);
                kd[512 + 16 * (4 * hg + 3) + c] = (u16)(q23 >> 16);
            } else if (hg == 2 && c == 0) {
                kd[640] = (u16)(q01 & 0xffff);
            }
        }
    }
    __syncthreads();
    u16* krow = kx + (size_t)d * KROW;
#pragma unroll
    for (int it = 0; it < 2; ++it) {
        const int j = tid + 128 * it;
        u32x4 o;
#pragma unroll
        for (int e = 0; e < 4; ++e) {
            unsigned hv[2];
#pragma unroll
            for (int h = 0; h < 2; ++h) {
                const int l = 2047 - 8 * j - (2 * e + h);
                const unsigned bits = __float_as_uint((float)l);
                const int b16 = (int)((bits + 0x7fffu + ((bits >> 16) & 1u)) >> 16);
                const int idx = l < 128 ? l : b16 - 17024;
                hv[h] = kd[idx];
            }
            o[e] = hv[0] | (hv[1] << 16);
        }
        *(u32x4*)(krow + 8 * j) = o;
    }
    if (tid < 2) {
        u32x4 z = {0u, 0u, 0u, 0u};
        *(u32x4*)(krow + 2048 + 8 * tid) = z;
    }
}

constexpr int CV_COPY = 4160;
constexpr int CV_KB = 4 * CV_COPY;
constexpr int CV_UB = 4576 + 32;
constexpr int CV_XB = 4096 + 32;
constexpr int CV_CH = CV_KB + CV_UB + CV_XB;

__global__ __launch_bounds__(256) void k2_conv(const u16* __restrict__ ub, const u16* __restrict__ xb,
                                               const u16* __restrict__ kx, const u16* __restrict__ dskip,
                                               u16* __restrict__ hb) {
    __shared__ __attribute__((aligned(16))) char smem[4 * CV_CH];
    const int tid = threadIdx.x, lane = tid & 63;
    const int w = __builtin_amdgcn_readfirstlane(tid >> 6);
    const int grp = (blockIdx.x & 7) * 20 + (blockIdx.x >> 3);
    const int d0 = grp * 4;
    const int d = d0 + w;
    u32x4 vu[4], vx[4];
    {
        const int ch = tid & 3;
#pragma unroll
        for (int i = 0; i < 4; ++i) {
            const int piece = (tid >> 2) + 64 * i;
            const size_t g = ((size_t)piece * D_ + d0 + ch) * 8;
            vu[i] = *(const u32x4*)(ub + g);
            vx[i] = *(const u32x4*)(xb + g);
        }
    }
    u32x4 k4[4];
    u32x2 k2[4];
    {
        const u16* krow = kx + (size_t)d * KROW;
#pragma unroll
        for (int i = 0; i < 4; ++i) {
            const int j = lane + 64 * i;
            k4[i] = *(const u32x4*)(krow + 8 * j);
            k2[i] = *(const u32x2*)(krow + 8 * j + 8);
        }
    }
    const float dsk = bf2f(dskip[d]);
    char* mine = smem + w * CV_CH;
    {
        u32x4 z = {0u, 0u, 0u, 0u};
        if (lane < 30) *(u32x4*)(mine + CV_KB + lane * 16) = z;
        else if (lane >= 32 && lane < 40) *(u32x4*)(mine + ((lane - 32) >> 1) * CV_COPY + 4096 + (lane & 1) * 16) = z;
    }
#pragma unroll
    for (int i = 0; i < 4; ++i) {
        const int j = lane + 64 * i;
        const unsigned r0 = k4[i][0], r1 = k4[i][1], r2 = k4[i][2], r3 = k4[i][3], r4 = k2[i][0], r5 = k2[i][1];
        const unsigned s0 = __builtin_amdgcn_alignbit(r1, r0, 16), s1 = __builtin_amdgcn_alignbit(r2, r1, 16),
                       s2 = __builtin_amdgcn_alignbit(r3, r2, 16), s3 = __builtin_amdgcn_alignbit(r4, r3, 16),
                       s4 = __builtin_amdgcn_alignbit(r5, r4, 16);
        u32x4 c1 = {s0, s1, s2, s3}, c2 = {r1, r2, r3, r4}, c3 = {s1, s2, s3, s4};
        *(u32x4*)(mine + 0 * CV_COPY + j * 16) = k4[i];
        *(u32x4*)(mine + 1 * CV_COPY + j * 16) = c1;
        *(u32x4*)(mine + 2 * CV_COPY + j * 16) = c2;
        *(u32x4*)(mine + 3 * CV_COPY + j * 16) = c3;
    }
    {
        char* cb = smem + (tid & 3) * CV_CH + CV_KB;
#pragma unroll
        for (int i = 0; i < 4; ++i) {
            const int piece = (tid >> 2) + 64 * i;
            *(u32x4*)(cb + 480 + piece * 16) = vu[i];
            *(u32x4*)(cb + CV_UB + piece * 16) = vx[i];
        }
    }
    __syncthreads();
    const int m = lane & 15, hg = lane >> 4;
    const char* abase = mine + (3 - (m & 3)) * CV_COPY + 2 * (2028 - 32 * 63 - 4 * (m >> 2) + 8 * hg);
    const char* bbase = mine + CV_KB + (2 * m + hg) * 16;
    f32x4 acc[8];
#pragma unroll
    for (int q = 0; q < 8; ++q) acc[q] = (f32x4){0.f, 0.f, 0.f, 0.f};
#pragma unroll
    for (int g0 = 0; g0 < 8; ++g0) {
        u32x4 af[8], bf[8];
#pragma unroll
        for (int g1 = 0; g1 < 8; ++g1) {
            const int g = 8 * g1 + g0;
            const u32x2 lo = *(const u32x2*)(abase + (63 - g) * 64);
            const u32x2 hi = *(const u32x2*)(abase + (63 - g) * 64 + 8);
            af[g1] = (u32x4){lo.x, lo.y, hi.x, hi.y};
            bf[g1] = *(const u32x4*)(bbase + (8 * g1 - g0 + 7) * 64);
        }
#pragma unroll
        for (int q = 0; q < 8; ++q)
#pragma unroll
            for (int g1 = 0; g1 <= q; ++g1) acc[q] = mfma16(af[g1], bf[q - g1], acc[q]);
    }
    {
        const char* up = mine + CV_KB + 480 + (16 * m + 4 * hg) * 2;
        char* xp = mine + CV_KB + CV_UB + (16 * m + 4 * hg) * 2;
#pragma unroll
        for (int q = 0; q < 8; ++q) {
            const u32x2 uu = *(const u32x2*)(up + q * 512);
            const u32x2 xx = *(const u32x2*)(xp + q * 512);
            const unsigned y01 = pk2(acc[q][0], acc[q][1]), y23 = pk2(acc[q][2], acc[q][3]);
            const unsigned s01 = pk2(bflo(uu.x) * dsk, bfhi(uu.x) * dsk), s23 = pk2(bflo(uu.y) * dsk, bfhi(uu.y) * dsk);
            const unsigned a01 = pk2(bflo(y01) + bflo(s01), bfhi(y01) + bfhi(s01));
            const unsigned a23 = pk2(bflo(y23) + bflo(s23), bfhi(y23) + bfhi(s23));
            u32x2 o;
            o.x = pk2(bflo(xx.x) + bflo(a01), bfhi(xx.x) + bfhi(a01));
            o.y = pk2(bflo(xx.y) + bflo(a23), bfhi(xx.y) + bfhi(a23));
            *(u32x2*)(xp + q * 512) = o;
        }
    }
    __syncthreads();
    {
        const int ch = tid & 3;
        const char* cb = smem + ch * CV_CH + CV_KB + CV_UB;
#pragma unroll
        for (int i = 0; i < 4; ++i) {
            const int piece = (tid >> 2) + 64 * i;
            *(u32x4*)(hb + ((size_t)piece * D_ + d0 + ch) * 8) = *(const u32x4*)(cb + piece * 16);
        }
    }
}

__device__ __forceinline__ float xor_lane(float v, int mask) { return __shfl_xor(v, mask, 64); }

__device__ __forceinline__ void block_stats(const float (&v)[8], float* red, int tid, float (&mb)[8], float (&inv)[8]) {
    const int lane = tid & 63, w = tid >> 6;
    float a[16];
#pragma unroll
    for (int e = 0; e < 8; ++e) { a[e] = v[e]; a[8 + e] = v[e] * v[e]; }
#pragma unroll
    for (int e = 0; e < 8; ++e) {
        const bool up = lane & 1;
        const float keep = up ? a[8 + e] : a[e], send = up ? a[e] : a[8 + e];
        a[e] = keep + xor_lane(send, 1);
    }
#pragma unroll
    for (int e = 0; e < 4; ++e) {
        const bool up = lane & 2;
        const float keep = up ? a[4 + e] : a[e], send = up ? a[e] : a[4 + e];
        a[e] = keep + xor_lane(send, 2);
    }
#pragma unroll
    for (int e = 0; e < 2; ++e) {
        const bool up = lane & 4;
        const float keep = up ? a[2 + e] : a[e], send = up ? a[e] : a[2 + e];
        a[e] = keep + xor_lane(send, 4);
    }
    {
        const bool up = lane & 8;
        const float keep = up ? a[1] : a[0], send = up ? a[0] : a[1];
        a[0] = keep + xor_lane(send, 8);
    }
    a[0] += xor_lane(a[0], 16);
    a[0] += xor_lane(a[0], 32);
    if (lane < 16) {
        const int row = ((lane >> 1) & 1) * 4 + ((lane >> 2) & 1) * 2 + ((lane >> 3) & 1);
        red[w * 16 + (lane & 1) * 8 + row] = a[0];
    }
    __syncthreads();
    if (tid < 8) {
        float s = 0.f, q = 0.f;
#pragma unroll
        for (int k = 0; k < 10; ++k) { s += red[k * 16 + tid]; q += red[k * 16 + 8 + tid]; }
        const float mean = s * (1.0f / D_);
        const float var = fmaxf(q * (1.0f / D_) - mean * mean, 0.f);
        const float den = rb(__builtin_sqrtf(rb(rb(var) + 1e-5f)));
        red[160 + tid] = rb(mean);
        red[168 + tid] = 1.0f / den;
    }
    __syncthreads();
#pragma unroll
    for (int e = 0; e < 8; ++e) { mb[e] = red[160 + e]; inv[e] = red[168 + e]; }
}

__global__ __launch_bounds__(640) void k3_ln2(const u16* __restrict__ hb, u16* __restrict__ hn) {
    __shared__ __attribute__((aligned(16))) float red[2][176];
    const int tid = threadIdx.x;
    const size_t g = ((size_t)blockIdx.x * D_ + tid) * 8;
    const u32x4 p = *(const u32x4*)(hb + g);
    float v[8], mb[8], inv[8];
#pragma unroll
    for (int e = 0; e < 4; ++e) { v[2 * e] = bflo(p[e]); v[2 * e + 1] = bfhi(p[e]); }
    block_stats(v, red[0], tid, mb, inv);
#pragma unroll
    for (int e = 0; e < 4; ++e) {
        const unsigned t = pk2(v[2 * e] - mb[2 * e], v[2 * e + 1] - mb[2 * e + 1]);
        const unsigned o = pk2(bflo(t) * inv[2 * e], bfhi(t) * inv[2 * e + 1]);
        v[2 * e] = bflo(o);
        v[2 * e + 1] = bfhi(o);
    }
    block_stats(v, red[1], tid, mb, inv);
    u32x4 o;
#pragma unroll
    for (int e = 0; e < 4; ++e) {
        const unsigned t = pk2(v[2 * e] - mb[2 * e], v[2 * e + 1] - mb[2 * e + 1]);
        o[e] = pk2(bflo(t) * inv[2 * e], bfhi(t) * inv[2 * e + 1]);
    }
    *(u32x4*)(hn + g) = o;
}

constexpr int G_STAGE = 9216;
constexpr int G_DEPTH = 4;
constexpr int G_WAVE = G_STAGE * G_DEPTH;

__device__ __forceinline__ void dma16(__amdgpu_buffer_rsrc_t srd, unsigned voff, unsigned soff, unsigned m0v) {
    asm volatile("s_mov_b32 m0, %0\n\ts_nop 0\n\tbuffer_load_dwordx4 %1, %2, %3 offen lds"
                 :: "s"(m0v), "v"(voff), "s"(srd), "s"(soff) : "m0", "memory");
}
__device__ __forceinline__ float gelu_tanh(float x) {
    const float u = 0.7978845608028654f * (x + 0.044715f * x * x * x);
    const float e = __builtin_amdgcn_exp2f(-2.885390081777927f * u);
    return x * __builtin_amdgcn_rcpf(1.0f + e);
}

template <int MODE>
__global__ __launch_bounds__(256) void k45_gemm(const u16* __restrict__ W, const u16* __restrict__ act,
                                                const u16* __restrict__ bias, const u16* __restrict__ hb,
                                                u16* __restrict__ out) {
    constexpr int LDW = MODE == 0 ? F_ : L_;
    constexpr int KSTEPS = MODE == 0 ? L_ / 32 : F_ / 32;
    __shared__ __attribute__((aligned(16))) char smem[4 * G_WAVE];
    const int tid = threadIdx.x, lane = tid & 63;
    const int w = __builtin_amdgcn_readfirstlane(tid >> 6);
    int mt, dt;
    {
        const int xcd = blockIdx.x & 7, idx = blockIdx.x >> 3;
        constexpr int PER = MODE == 0 ? 27 : 32;
        const int t = (xcd >> 1) * PER + idx;
        mt = t >> 2;
        dt = (xcd & 1) * 4 + (t & 3);
    }
    const int m0 = mt * 64, d0 = dt * 80;
    const int ks0 = MODE == 0 ? w * 16 : (w * 14 - (w > 2 ? 1 : 0));
    const int nks = MODE == 0 ? 16 : (w < 2 ? 14 : 13);
    char* ring = smem + w * G_WAVE;
    const unsigned ring_a = (unsigned)(size_t)(LDS_AS char*)ring;
    const __amdgpu_buffer_rsrc_t srdW = __builtin_amdgcn_make_buffer_rsrc((void*)W, (short)0, L_ * F_ * 2, 0x00020000);
    const __amdgpu_buffer_rsrc_t srdA = __builtin_amdgcn_make_buffer_rsrc((void*)act, (short)0, L_ * D_ * 2, 0x00020000);
    const int wr = lane >> 3, wslot = lane & 7;
    const unsigned wv0 = (wr * LDW + ((((wslot >> 1) ^ ((wr >> 1) & 1)) * 2 + (wslot & 1)) * 8)) * 2;
    const unsigned wv1 = ((wr + 8) * LDW + ((((wslot >> 1) ^ (((wr >> 1) & 1) | 2)) * 2 + (wslot & 1)) * 8)) * 2;
    const int n = lane & 15, hg = lane >> 4;
    const unsigned av = (hg * D_ + n) * 16;
    const unsigned ws0 = (ks0 * 32 * LDW + m0) * 2, as0 = (ks0 * 4 * D_ + d0) * 16;
    auto issue = [&](int s) {
        const unsigned dst = ring_a + (s & (G_DEPTH - 1)) * G_STAGE;
        const unsigned wo = ws0 + s * (32 * LDW * 2);
        dma16(srdW, wv0, wo, dst);
        dma16(srdW, wv1, wo, dst + 1024);
        dma16(srdW, wv0, wo + 16 * LDW * 2, dst + 2048);
        dma16(srdW, wv1, wo + 16 * LDW * 2, dst + 3072);
        const unsigned ao = as0 + s * (4 * D_ * 16);
#pragma unroll
        for (int j = 0; j < 5; ++j) dma16(srdA, av, ao + j * 256, dst + 4096 + j * 1024);
    };
    f32x4 acc[4][5];
#pragma unroll
    for (int c = 0; c < 4; ++c)
#pragma unroll
        for (int j = 0; j < 5; ++j) acc[c][j] = (f32x4){0.f, 0.f, 0.f, 0.f};
    const int q4 = n >> 2, p4 = n & 3;
    const int fsw = ((q4 >> 1) & 1) | ((hg & 1) << 1);
    const int troff = (8 * hg + q4) * 128 + p4 * 8;
    issue(0);
    issue(1);
    issue(2);
    for (int s = 0; s < nks; ++s) {
        if (s + 3 < nks) {
            issue(s + 3);
            asm volatile("s_waitcnt vmcnt(27)" ::: "memory");
        } else if (s + 2 < nks) {
            asm volatile("s_waitcnt vmcnt(18)" ::: "memory");
        } else if (s + 1 < nks) {
            asm volatile("s_waitcnt vmcnt(9)" ::: "memory");
        } else {
            asm volatile("s_waitcnt vmcnt(0)" ::: "memory");
        }
        const char* st = ring + (s & (G_DEPTH - 1)) * G_STAGE;
        u32x4 wf[4], af[5];
#pragma unroll
        for (int c = 0; c < 4; ++c) {
            const u32x2 lo = tr_read(st + troff + ((c ^ fsw) * 32));
            const u32x2 hi = tr_read(st + troff + 512 + ((c ^ fsw) * 32));
            wf[c] = (u32x4){lo.x, lo.y, hi.x, hi.y};
        }
#pragma unroll
        for (int j = 0; j < 5; ++j) af[j] = *(const u32x4*)(st + 4096 + j * 1024 + lane * 16);
#pragma unroll
        for (int c = 0; c < 4; ++c)
#pragma unroll
            for (int j = 0; j < 5; ++j) acc[c][j] = mfma16(wf[c], af[j], acc[c][j]);
        asm volatile("" ::: "memory");
    }
    __syncthreads();
#pragma unroll
    for (int c = 0; c < 4; ++c)
#pragma unroll
        for (int j = 0; j < 5; ++j)
            if (c != w) *(f32x4*)(smem + (((c * 4 + w) * 5 + j) * 64 + lane) * 16) = acc[c][j];
    __syncthreads();
    f32x4 r[5];
#pragma unroll
    for (int j = 0; j < 5; ++j) {
        r[j] = w == 0 ? acc[0][j] : w == 1 ? acc[1][j] : w == 2 ? acc[2][j] : acc[3][j];
    }
#pragma unroll
    for (int o = 0; o < 4; ++o)
        if (o != w) {
#pragma unroll
            for (int j = 0; j < 5; ++j) r[j] += *(const f32x4*)(smem + (((w * 4 + o) * 5 + j) * 64 + lane) * 16);
        }
    const int mrow = m0 + 16 * w + 4 * hg;
    const u32x2 bb = *(const u32x2*)(bias + mrow);
    const float b0 = bflo(bb.x), b1 = bfhi(bb.x), b2 = bflo(bb.y), b3 = bfhi(bb.y);
    const size_t goff = ((size_t)(mrow >> 3) * D_ + d0 + n) * 8 + (hg & 1) * 4;
    if (MODE == 0) {
#pragma unroll
        for (int j = 0; j < 5; ++j) {
            const unsigned y01 = pk2(r[j][0], r[j][1]), y23 = pk2(r[j][2], r[j][3]);
            const unsigned z01 = pk2(bflo(y01) + b0, bfhi(y01) + b1), z23 = pk2(bflo(y23) + b2, bfhi(y23) + b3);
            u32x2 o;
            o.x = pk2(gelu_tanh(bflo(z01)), gelu_tanh(bfhi(z01)));
            o.y = pk2(gelu_tanh(bflo(z23)), gelu_tanh(bfhi(z23)));
            *(u32x2*)(out + goff + j * 128) = o;
        }
    } else {
        char* img = smem + 81920 + w * 4096;
#pragma unroll
        for (int j = 0; j < 5; ++j) {
            const u32x2 hh = *(const u32x2*)(hb + goff + j * 128);
            const unsigned y01 = pk2(r[j][0], r[j][1]), y23 = pk2(r[j][2], r[j][3]);
            const unsigned z01 = pk2(bflo(y01) + b0, bfhi(y01) + b1), z23 = pk2(bflo(y23) + b2, bfhi(y23) + b3);
            u32x2 o;
            o.x = pk2(bflo(hh.x) + bflo(z01), bfhi(hh.x) + bfhi(z01));
            o.y = pk2(bflo(hh.y) + bflo(z23), bfhi(hh.y) + bfhi(z23));
            *(u32x2*)(img + (16 * j + n) * 40 + hg * 8) = o;
        }
        asm volatile("s_waitcnt lgkmcnt(0)" ::: "memory");
#pragma unroll
        for (int i = 0; i < 3; ++i) {
            int k = hg + 4 * i;
            const bool ok = k < 10;
            k = ok ? k : 0;
            const u32x2 lo = tr_read(img + (8 * k + q4) * 40 + p4 * 8);
            const u32x2 hi = tr_read(img + (8 * k + 4 + q4) * 40 + p4 * 8);
            u32x4 o = {lo.x, lo.y, hi.x, hi.y};
            if (ok) *(u32x4*)(out + (size_t)(m0 + 16 * w + n) * D_ + d0 + 8 * k) = o;
        }
    }
}

extern "C" void kernel_launch(void* const* d_in, const int* in_sizes, int n_in, void* d_out, int out_size,
                              void* d_ws, size_t ws_size, hipStream_t stream) {
    const u16* x = (const u16*)d_in[0];
    const u16* logA = (const u16*)d_in[1];
    const u16* Aim = (const u16*)d_in[2];
    const u16* Cre = (const u16*)d_in[3];
    const u16* Cim = (const u16*)d_in[4];
    const u16* logdt = (const u16*)d_in[5];
    const u16* dskip = (const u16*)d_in[6];
    const u16* w1 = (const u16*)d_in[8];
    const u16* b1 = (const u16*)d_in[9];
    const u16* w2 = (const u16*)d_in[10];
    const u16* b2 = (const u16*)d_in[11];
    char* ws = (char*)d_ws;
    constexpr size_t SZ = (size_t)L_ * D_ * 2;
    u16* ub = (u16*)(ws);
    u16* xb = (u16*)(ws + SZ);
    u16* hb = (u16*)(ws + 2 * SZ);
    u16* hn = (u16*)(ws + 3 * SZ);
    u16* gb = (u16*)(ws + 4 * SZ);
    u16* kx = (u16*)(ws + 5 * SZ);
    k1_ln_kgen<<<256 + D_, 128, 0, stream>>>(x, logA, Aim, Cre, Cim, logdt, ub, xb, kx);
    k2_conv<<<160, 256, 0, stream>>>(ub, xb, kx, dskip, hb);
    k3_ln2<<<256, 640, 0, stream>>>(hb, hn);
    k45_gemm<0><<<216, 256, 0, stream>>>(w1, hn, b1, nullptr, gb);
    k45_gemm<1><<<256, 256, 0, stream>>>(w2, gb, b2, hb, (u16*)d_out);
}
```

```cpp
#define GDEPTH 4
#include <hip/hip_runtime.h>
#include <hip/hip_bf16.h>
#include <stdint.h>
#pragma clang diagnostic ignored "-Winline-asm"

typedef unsigned short u16;
typedef __bf16 bf16x8 __attribute__((ext_vector_type(8)));
typedef __bf16 bf16x2 __attribute__((ext_vector_type(2)));
typedef short s16x4 __attribute__((ext_vector_type(4)));
typedef float f32x4 __attribute__((ext_vector_type(4)));
typedef float f32x2 __attribute__((ext_vector_type(2)));
typedef unsigned u32x4 __attribute__((ext_vector_type(4)));
typedef unsigned u32x2 __attribute__((ext_vector_type(2)));
#define LDS_AS __attribute__((address_space(3)))

#define STAMP(k, i) do { } while (0)

constexpr int L_ = 2048;
constexpr int D_ = 640;
constexpr int N_ = 128;
constexpr int F_ = 1728;
constexpr int KROW = 2064;
constexpr unsigned SZ_ = L_ * D_ * 2;
constexpr unsigned WS_HB = 0, WS_HN = SZ_, WS_GB = 2 * SZ_, WS_END = 3 * SZ_;
constexpr unsigned WS_UB = 3 * SZ_, WS_XB = 4 * SZ_, WS_KX = 5 * SZ_, WS_ID = 7 * SZ_;

__device__ __forceinline__ float bf2f(unsigned v) { return __uint_as_float(v << 16); }
__device__ __forceinline__ float bflo(unsigned v) { return __uint_as_float(v << 16); }
__device__ __forceinline__ float bfhi(unsigned v) { return __uint_as_float(v & 0xffff0000u); }
__device__ __forceinline__ unsigned pk2(float lo, float hi) {
    f32x2 f = {lo, hi};
    bf16x2 b = __builtin_convertvector(f, bf16x2);
    return __builtin_bit_cast(unsigned, b);
}
__device__ __forceinline__ float rb(float x) { return bflo(pk2(x, x)); }

__device__ __forceinline__ f32x4 mfma16(u32x4 a, u32x4 b, f32x4 c) {
    return __builtin_amdgcn_mfma_f32_16x16x32_bf16(__builtin_bit_cast(bf16x8, a), __builtin_bit_cast(bf16x8, b), c, 0, 0, 0);
}
__device__ __forceinline__ u32x2 tr_read(const char* p) {
    s16x4 v = __builtin_amdgcn_ds_read_tr16_b64_v4i16((LDS_AS s16x4*)p);
    return __builtin_bit_cast(u32x2, v);
}
template <int CTRL>
__device__ __forceinline__ float dpp_add(float v) {
    int t = __builtin_amdgcn_update_dpp(0, __float_as_int(v), CTRL, 0xf, 0xf, true);
    return v + __int_as_float(t);
}
__device__ __forceinline__ float row16_sum(float v) {
    v = dpp_add<0x128>(v);
    v = dpp_add<0x124>(v);
    v = dpp_add<0x122>(v);
    v = dpp_add<0x121>(v);
    return v;
}

template <int NT>
__device__ __forceinline__ void touch4(__amdgpu_buffer_rsrc_t srd, unsigned voff, unsigned soff, unsigned m0v) {
    if (NT) asm volatile("s_mov_b32 m0, %0\n\ts_nop 0\n\tbuffer_load_dword %1, %2, %3 offen nt lds"
                 :: "s"(m0v), "v"(voff), "s"(srd), "s"(soff) : "m0", "memory");
    else asm volatile("s_mov_b32 m0, %0\n\ts_nop 0\n\tbuffer_load_dword %1, %2, %3 offen lds"
                 :: "s"(m0v), "v"(voff), "s"(srd), "s"(soff) : "m0", "memory");
}
template <int MODE>
__device__ __forceinline__ void warm_w(const u16* W, int xcd, int gw, int nw, unsigned lds, int lane) {
    constexpr int LDW = MODE == 0 ? F_ : L_;
    const __amdgpu_buffer_rsrc_t srdW = __builtin_amdgcn_make_buffer_rsrc((void*)W, (short)0, L_ * F_ * 2, 0x00020000);
    const int mt0 = MODE == 0 ? ((xcd >> 1) * 27) >> 2 : (xcd >> 1) * 8;
    const unsigned voff = ((lane >> 3) * LDW + (lane & 7) * 64) * 2;
    for (int j = gw; j < (MODE == 0 ? 256 : 224); j += nw) {
        const int s = j >> 4, w = (j >> 2) & 3;
        const int ks = MODE == 0 ? w * 16 + s : w * 14 + s;
        if (MODE == 1 && ks >= 54) continue;
        touch4<0>(srdW, voff, ((ks * 32 + (j & 3) * 8) * LDW + mt0 * 64) * 2, lds);
    }
}

constexpr int K1_ROWB = 528;
constexpr int K1_LDS = 64 * K1_ROWB + 1312;

__device__ __forceinline__ f32x2 cmul(f32x2 a, f32x2 b) {
    f32x2 r;
    r.x = a.x * b.x - a.y * b.y;
    r.y = a.x * b.y + a.y * b.x;
    return r;
}
__device__ __forceinline__ f32x2 cexp_p(float zr, float zi, float p) {
    float mag = __builtin_amdgcn_exp2f(zr * p * 1.44269504088896341f);
    float rev = zi * p * 0.15915494309189535f;
    rev = rev - __builtin_floorf(rev);
    f32x2 r;
    r.x = mag * __builtin_amdgcn_cosf(rev);
    r.y = mag * __builtin_amdgcn_sinf(rev);
    return r;
}

__global__ __launch_bounds__(128) void k1_ln_kgen(const u16* __restrict__ x, const u16* __restrict__ logA,
                                                  const u16* __restrict__ Aim, const u16* __restrict__ Cre,
                                                  const u16* __restrict__ Cim, const u16* __restrict__ logdt,
                                                  u16* __restrict__ ub, u16* __restrict__ xb, u16* __restrict__ kx) {
    __shared__ __attribute__((aligned(16))) char smem[K1_LDS];
    const int tid = threadIdx.x;
    const int lane = tid & 63;
    STAMP(0, 0);
    if (blockIdx.x < 256) {
        const int rg = blockIdx.x;
        const int r = tid >> 4, i = tid & 15;
        const u32x4* xrow = (const u32x4*)(x + (size_t)(rg * 8 + r) * D_);
        u32x4 v[5];
#pragma unroll
        for (int j = 0; j < 5; ++j) v[j] = xrow[i + 16 * j];
        float s = 0.f;
#pragma unroll
        for (int j = 0; j < 5; ++j)
#pragma unroll
            for (int e = 0; e < 4; ++e) s += bflo(v[j][e]) + bfhi(v[j][e]);
        s = row16_sum(s);
        const float mean = s * (1.0f / D_);
        STAMP(0, 1);
        float q = 0.f;
#pragma unroll
        for (int j = 0; j < 5; ++j)
#pragma unroll
            for (int e = 0; e < 4; ++e) {
                float a = bflo(v[j][e]) - mean, b = bfhi(v[j][e]) - mean;
                q += a * a + b * b;
            }
        q = row16_sum(q);
        const float mb = rb(mean);
        const float den = rb(__builtin_sqrtf(rb(rb(q * (1.0f / D_)) + 1e-5f)));
        const float inv = 1.0f / den;
        constexpr int PITCH = D_ * 2 + 32;
        char* tu = smem;
        char* tx = smem + 8 * PITCH;
#pragma unroll
        for (int j = 0; j < 5; ++j) {
            u32x4 o;
#pragma unroll
            for (int e = 0; e < 4; ++e) {
                unsigned t = pk2(bflo(v[j][e]) - mb, bfhi(v[j][e]) - mb);
                o[e] = pk2(bflo(t) * inv, bfhi(t) * inv);
            }
            *(u32x4*)(tu + r * PITCH + (i + 16 * j) * 16) = o;
            *(u32x4*)(tx + r * PITCH + (i + 16 * j) * 16) = v[j];
        }
        __syncthreads();
        const int q4 = i >> 2, p4 = i & 3;
#pragma unroll
        for (int j = 0; j < 5; ++j) {
            const int c0 = (j * 8 + r) * 16;
            const int off = q4 * PITCH + (c0 + 4 * p4) * 2;
            u32x2 a0 = tr_read(tu + off), a1 = tr_read(tu + off + 4 * PITCH);
            u32x2 b0 = tr_read(tx + off), b1 = tr_read(tx + off + 4 * PITCH);
            u32x4 ou = {a0.x, a0.y, a1.x, a1.y}, ox = {b0.x, b0.y, b1.x, b1.y};
            const size_t g = ((size_t)rg * D_ + c0 + i) * 8;
            *(u32x4*)(ub + g) = ou;
            *(u32x4*)(xb + g) = ox;
        }
        STAMP(0, 2);
        return;
    }
    const int d = blockIdx.x - 256;
    {
        const int n = tid;
        const float dt = rb(expf(bf2f(logdt[d])));
        const float ar = -rb(expf(bf2f(logA[d * N_ + n])));
        const float zr = ar * dt, zi = bf2f(Aim[d * N_ + n]) * dt;
        f32x2 c2 = {2.0f * (bf2f(Cre[d * N_ + n]) * dt), 2.0f * (bf2f(Cim[d * N_ + n]) * dt)};
        const f32x2 E1 = cexp_p(zr, zi, 1.f), E8 = cexp_p(zr, zi, 8.f), E128 = cexp_p(zr, zi, 128.f);
        STAMP(0, 3);
        char* col = smem + n * 4;
        auto putA = [&](int row, f32x2 v) { *(unsigned*)(col + row * K1_ROWB) = pk2(v.x, -v.y); };
        auto putB = [&](int row, f32x2 v) { *(unsigned*)(col + (48 + row) * K1_ROWB) = pk2(v.x, v.y); };
        f32x2 b = {1.f, 0.f};
        putB(0, b);
        b = E8;
        putB(1, b);
#pragma unroll
        for (int c = 2; c < 16; ++c) { b = cmul(b, E8); putB(c, b); }
        f32x2 e[8];
        e[1] = E1;
#pragma unroll
        for (int k = 2; k < 8; ++k) e[k] = cmul(e[k - 1], E1);
        f32x2 a = c2;
#pragma unroll
        for (int c = 0; c < 17; ++c) {
            if (c < 2) {
                putA(8 * c, a);
#pragma unroll
                for (int k = 1; k < 8; ++k) putA(8 * c + k, cmul(a, e[k]));
            } else if (c < 4) {
                putA(16 + 4 * (c - 2), a);
#pragma unroll
                for (int k = 1; k < 4; ++k) putA(16 + 4 * (c - 2) + k, cmul(a, e[2 * k]));
            } else if (c < 8) {
                putA(24 + 2 * (c - 4), a);
                putA(24 + 2 * (c - 4) + 1, cmul(a, e[4]));
            } else {
                putA(32 + (c - 8), a);
            }
            if (c < 16) a = cmul(a, E128);
        }
    }
    __syncthreads();
    STAMP(0, 4);
    u16* kd = (u16*)(smem + 64 * K1_ROWB);
    {
        const int w = tid >> 6, m = lane & 15, hg = lane >> 4;
        const char* fb = smem + m * K1_ROWB + hg * 16;
        f32x4 acc0 = {0.f, 0.f, 0.f, 0.f}, acc1 = {0.f, 0.f, 0.f, 0.f};
#pragma unroll
        for (int ks = 0; ks < 8; ++ks) {
            u32x4 bf = *(const u32x4*)(fb + 48 * K1_ROWB + ks * 64);
            u32x4 a0 = *(const u32x4*)(fb + (w * 16) * K1_ROWB + ks * 64);
            u32x4 a2 = *(const u32x4*)(fb + 32 * K1_ROWB + ks * 64);
            acc0 = mfma16(a0, bf, acc0);
            acc1 = mfma16(a2, bf, acc1);
        }
        const int c = m;
        const unsigned p01 = pk2(acc0[0], acc0[1]), p23 = pk2(acc0[2], acc0[3]);
        if (w == 0) {
            u32x2 o = {p01, p23};
            *(u32x2*)(kd + 128 * (hg >> 1) + 4 * (hg & 1) + 8 * c) = o;
        } else {
            if (hg < 2) {
                u32x2 o = {p01, p23};
                *(u32x2*)(kd + 256 + 64 * hg + 4 * c) = o;
            } else {
                *(unsigned*)(kd + 384 + 32 * (2 * (hg - 2)) + 2 * c) = p01;
                *(unsigned*)(kd + 384 + 32 * (2 * (hg - 2) + 1) + 2 * c) = p23;
            }
            const unsigned q01 = pk2(acc1[0], acc1[1]), q23 = pk2(acc1[2], acc1[3]);
            if (hg < 2) {
                kd[512 + 16 * (4 * hg + 0) + c] = (u16)(q01 & 0xffff);
                kd[512 + 16 * (4 * hg + 1) + c] = (u16)(q01 >> 16);
                kd[512 + 16 * (4 * hg + 2) + c] = (u16)(q23 & 0xffff);
                kd[512 + 16 * (4 * hg + 3) + c] = (u16)(q23 >> 16);
            } else if (hg == 2 && c == 0) {
                kd[640] = (u16)(q01 & 0xffff);
            }
        }
    }
    __syncthreads();
    STAMP(0, 5);
    u16* krow = kx + (size_t)d * KROW;
#pragma unroll
    for (int it = 0; it < 2; ++it) {
        const int j = tid + 128 * it;
        u32x4 o;
#pragma unroll
        for (int e = 0; e < 4; ++e) {
            unsigned hv[2];
#pragma unroll
            for (int h = 0; h < 2; ++h) {
                const int l = 2047 - 8 * j - (2 * e + h);
                const unsigned bits = __float_as_uint((float)l);
                const int b16 = (int)((bits + 0x7fffu + ((bits >> 16) & 1u)) >> 16);
                const int idx = l < 128 ? l : b16 - 17024;
                hv[h] = kd[idx];
            }
            o[e] = hv[0] | (hv[1] << 16);
        }
        *(u32x4*)(krow + 8 * j) = o;
    }
    if (tid < 2) {
        u32x4 z = {0u, 0u, 0u, 0u};
        *(u32x4*)(krow + 2048 + 8 * tid) = z;
    }
    STAMP(0, 6);
}

constexpr int CV_COPY = 4160;
constexpr int CV_KB = 4 * CV_COPY;
constexpr int CV_UB = 4576 + 32;
constexpr int CV_XB = 4096 + 32;
constexpr int CV_CH = CV_KB + CV_UB + CV_XB;

__global__ __launch_bounds__(256) void k2_conv(const u16* __restrict__ ub, const u16* __restrict__ xb,
                                               const u16* __restrict__ kx, const u16* __restrict__ dskip,
                                               u16* __restrict__ hb, const u16* __restrict__ w1, const u16* __restrict__ w2) {
    __shared__ __attribute__((aligned(16))) char smem[4 * CV_CH];
    const int tid = threadIdx.x, lane = tid & 63;
    const int w = __builtin_amdgcn_readfirstlane(tid >> 6);
    STAMP(1, 0);
    if (blockIdx.x >= 160) {
        const unsigned lds = (unsigned)(size_t)(LDS_AS char*)(smem + w * 1024);
        const int gw = ((blockIdx.x - 160) >> 3) * 4 + w;
        warm_w<0>(w1, blockIdx.x & 7, gw, 48, lds, lane);
        asm volatile("s_waitcnt vmcnt(0)" ::: "memory");
        STAMP(1, 4);
        return;
    }
    const int grp = (blockIdx.x & 7) * 20 + (blockIdx.x >> 3);
    const int d0 = grp * 4;
    const int d = d0 + w;
    u32x4 vu[4], vx[4];
    {
        const int ch = tid & 3;
#pragma unroll
        for (int i = 0; i < 4; ++i) {
            const int piece = (tid >> 2) + 64 * i;
            const size_t g = ((size_t)piece * D_ + d0 + ch) * 8;
            vu[i] = *(const u32x4*)(ub + g);
            vx[i] = *(const u32x4*)(xb + g);
        }
    }
    u32x4 k4[4];
    u32x2 k2[4];
    {
        const u16* krow = kx + (size_t)d * KROW;
#pragma unroll
        for (int i = 0; i < 4; ++i) {
            const int j = lane + 64 * i;
            k4[i] = *(const u32x4*)(krow + 8 * j);
            k2[i] = *(const u32x2*)(krow + 8 * j + 8);
        }
    }
    const float dsk = bf2f(dskip[d]);
    char* mine = smem + w * CV_CH;
    {
        u32x4 z = {0u, 0u, 0u, 0u};
        if (lane < 30) *(u32x4*)(mine + CV_KB + lane * 16) = z;
        else if (lane >= 32 && lane < 40) *(u32x4*)(mine + ((lane - 32) >> 1) * CV_COPY + 4096 + (lane & 1) * 16) = z;
    }
#pragma unroll
    for (int i = 0; i < 4; ++i) {
        const int j = lane + 64 * i;
        const unsigned r0 = k4[i][0], r1 = k4[i][1], r2 = k4[i][2], r3 = k4[i][3], r4 = k2[i][0], r5 = k2[i][1];
        const unsigned s0 = __builtin_amdgcn_alignbit(r1, r0, 16), s1 = __builtin_amdgcn_alignbit(r2, r1, 16),
                       s2 = __builtin_amdgcn_alignbit(r3, r2, 16), s3 = __builtin_amdgcn_alignbit(r4, r3, 16),
                       s4 = __builtin_amdgcn_alignbit(r5, r4, 16);
        u32x4 c1 = {s0, s1, s2, s3}, c2 = {r1, r2, r3, r4}, c3 = {s1, s2, s3, s4};
        *(u32x4*)(mine + 0 * CV_COPY + j * 16) = k4[i];
        *(u32x4*)(mine + 1 * CV_COPY + j * 16) = c1;
        *(u32x4*)(mine + 2 * CV_COPY + j * 16) = c2;
        *(u32x4*)(mine + 3 * CV_COPY + j * 16) = c3;
    }
    {
        char* cb = smem + (tid & 3) * CV_CH + CV_KB;
#pragma unroll
        for (int i = 0; i < 4; ++i) {
            const int piece = (tid >> 2) + 64 * i;
            *(u32x4*)(cb + 480 + piece * 16) = vu[i];
            *(u32x4*)(cb + CV_UB + piece * 16) = vx[i];
        }
    }
    __syncthreads();
    STAMP(1, 1);
    const int m = lane & 15, hg = lane >> 4;
    const char* abase = mine + (3 - (m & 3)) * CV_COPY + 2 * (2028 - 32 * 63 - 4 * (m >> 2) + 8 * hg);
    const char* bbase = mine + CV_KB + (2 * m + hg) * 16;
    f32x4 acc[8];
#pragma unroll
    for (int q = 0; q < 8; ++q) acc[q] = (f32x4){0.f, 0.f, 0.f, 0.f};
#pragma unroll
    for (int g0 = 0; g0 < 8; ++g0) {
        u32x4 af[8], bf[8];
#pragma unroll
        for (int g1 = 0; g1 < 8; ++g1) {
            const int g = 8 * g1 + g0;
            const u32x2 lo = *(const u32x2*)(abase + (63 - g) * 64);
            const u32x2 hi = *(const u32x2*)(abase + (63 - g) * 64 + 8);
            af[g1] = (u32x4){lo.x, lo.y, hi.x, hi.y};
            bf[g1] = *(const u32x4*)(bbase + (8 * g1 - g0 + 7) * 64);
        }
#pragma unroll
        for (int q = 0; q < 8; ++q)
#pragma unroll
            for (int g1 = 0; g1 <= q; ++g1) acc[q] = mfma16(af[g1], bf[q - g1], acc[q]);
    }
    STAMP(1, 2);
    {
        const char* up = mine + CV_KB + 480 + (16 * m + 4 * hg) * 2;
        char* xp = mine + CV_KB + CV_UB + (16 * m + 4 * hg) * 2;
#pragma unroll
        for (int q = 0; q < 8; ++q) {
            const u32x2 uu = *(const u32x2*)(up + q * 512);
            const u32x2 xx = *(const u32x2*)(xp + q * 512);
            const unsigned y01 = pk2(acc[q][0], acc[q][1]), y23 = pk2(acc[q][2], acc[q][3]);
            const unsigned s01 = pk2(bflo(uu.x) * dsk, bfhi(uu.x) * dsk), s23 = pk2(bflo(uu.y) * dsk, bfhi(uu.y) * dsk);
            const unsigned a01 = pk2(bflo(y01) + bflo(s01), bfhi(y01) + bfhi(s01));
            const unsigned a23 = pk2(bflo(y23) + bflo(s23), bfhi(y23) + bfhi(s23));
            u32x2 o;
            o.x = pk2(bflo(xx.x) + bflo(a01), bfhi(xx.x) + bfhi(a01));
            o.y = pk2(bflo(xx.y) + bflo(a23), bfhi(xx.y) + bfhi(a23));
            *(u32x2*)(xp + q * 512) = o;
        }
    }
    __syncthreads();
    {
        const int ch = tid & 3;
        const char* cb = smem + ch * CV_CH + CV_KB + CV_UB;
        const int dt = grp / 20, rem = grp % 20, mq = rem / 5, j = rem % 5;
#pragma unroll
        for (int i = 0; i < 4; ++i) {
            const int piece = (tid >> 2) + 64 * i;
            const int unit = ((piece >> 2) * 40 + dt * 5 + j) * 64 + (piece & 3) * 16 + 4 * mq + ch;
            *(u32x4*)(hb + (size_t)unit * 8) = *(const u32x4*)(cb + piece * 16);
        }
    }
    STAMP(1, 3);
}

__device__ __forceinline__ float xor_lane(float v, int mask) { return __shfl_xor(v, mask, 64); }

__device__ __forceinline__ void block_stats(const float (&v)[8], float* red, int tid, float (&mb)[8], float (&inv)[8]) {
    const int lane = tid & 63, w = tid >> 6;
    float a[16];
#pragma unroll
    for (int e = 0; e < 8; ++e) { a[e] = v[e]; a[8 + e] = v[e] * v[e]; }
#pragma unroll
    for (int e = 0; e < 8; ++e) {
        const bool up = lane & 1;
        const float keep = up ? a[8 + e] : a[e], send = up ? a[e] : a[8 + e];
        a[e] = keep + xor_lane(send, 1);
    }
#pragma unroll
    for (int e = 0; e < 4; ++e) {
        const bool up = lane & 2;
        const float keep = up ? a[4 + e] : a[e], send = up ? a[e] : a[4 + e];
        a[e] = keep + xor_lane(send, 2);
    }
#pragma unroll
    for (int e = 0; e < 2; ++e) {
        const bool up = lane & 4;
        const float keep = up ? a[2 + e] : a[e], send = up ? a[e] : a[2 + e];
        a[e] = keep + xor_lane(send, 4);
    }
    {
        const bool up = lane & 8;
        const float keep = up ? a[1] : a[0], send = up ? a[0] : a[1];
        a[0] = keep + xor_lane(send, 8);
    }
    a[0] += xor_lane(a[0], 16);
    a[0] += xor_lane(a[0], 32);
    if (lane < 16) {
        const int row = ((lane >> 1) & 1) * 4 + ((lane >> 2) & 1) * 2 + ((lane >> 3) & 1);
        red[w * 16 + (lane & 1) * 8 + row] = a[0];
    }
    __syncthreads();
    if (tid < 8) {
        float s = 0.f, q = 0.f;
#pragma unroll
        for (int k = 0; k < 10; ++k) { s += red[k * 16 + tid]; q += red[k * 16 + 8 + tid]; }
        const float mean = s * (1.0f / D_);
        const float var = fmaxf(q * (1.0f / D_) - mean * mean, 0.f);
        const float den = rb(__builtin_sqrtf(rb(rb(var) + 1e-5f)));
        red[160 + tid] = rb(mean);
        red[168 + tid] = 1.0f / den;
    }
    __syncthreads();
#pragma unroll
    for (int e = 0; e < 8; ++e) { mb[e] = red[160 + e]; inv[e] = red[168 + e]; }
}

__global__ __launch_bounds__(640) void k3_ln2(const u16* __restrict__ hb, u16* __restrict__ hn, u16* __restrict__ ident) {
    __shared__ __attribute__((aligned(16))) float red[2][176];
    const int tid = threadIdx.x;
    STAMP(2, 0);
    const size_t g = ((size_t)((blockIdx.x >> 2) * 40 + (tid >> 4)) * 64 + (blockIdx.x & 3) * 16 + (tid & 15)) * 8;
    if (blockIdx.x < 64 && tid < 32) {
        const int r = blockIdx.x;
        *(unsigned*)(ident + r * L_ + 2 * tid) = (r >> 1) == tid ? ((r & 1) ? 0x3f800000u : 0x3f80u) : 0u;
    }
    const u32x4 p = *(const u32x4*)(hb + g);
    float v[8], mb[8], inv[8];
#pragma unroll
    for (int e = 0; e < 4; ++e) { v[2 * e] = bflo(p[e]); v[2 * e + 1] = bfhi(p[e]); }
    if (v[0] == 12345.f) STAMP(2, 7);
    STAMP(2, 1);
    block_stats(v, red[0], tid, mb, inv);
    STAMP(2, 2);
#pragma unroll
    for (int e = 0; e < 4; ++e) {
        const unsigned t = pk2(v[2 * e] - mb[2 * e], v[2 * e + 1] - mb[2 * e + 1]);
        const unsigned o = pk2(bflo(t) * inv[2 * e], bfhi(t) * inv[2 * e + 1]);
        v[2 * e] = bflo(o);
        v[2 * e + 1] = bfhi(o);
    }
    block_stats(v, red[1], tid, mb, inv);
    u32x4 o;
#pragma unroll
    for (int e = 0; e < 4; ++e) {
        const unsigned t = pk2(v[2 * e] - mb[2 * e], v[2 * e + 1] - mb[2 * e + 1]);
        o[e] = pk2(bflo(t) * inv[2 * e], bfhi(t) * inv[2 * e + 1]);
    }
    *(u32x4*)(hn + g) = o;
    STAMP(2, 3);
}

constexpr int G_D = GDEPTH;
constexpr int G_NS = G_D + 1;
constexpr int G_RING = G_NS * 4096;
constexpr int G_RED = 4 * G_RING;

__device__ __forceinline__ float gelu_tanh(float x) {
    const float p = __builtin_fmaf(x * x, -2.302208198f * 0.044715f, -2.302208198f);
    const float e = __builtin_amdgcn_exp2f(p * x);
    return x * __builtin_amdgcn_rcpf(1.0f + e);
}
__device__ __forceinline__ void g_issue(u32x4 (&a)[5], __amdgpu_buffer_rsrc_t sw, unsigned wo, unsigned m0v,
                                        unsigned wv0, unsigned wv1, unsigned wv2, unsigned wv3,
                                        __amdgpu_buffer_rsrc_t sa, unsigned ao, unsigned av0, unsigned av1) {
    asm volatile("s_mov_b32 m0, %5\n\ts_nop 0\n\t"
                 "buffer_load_dwordx4 %8, %6, %7 offen lds\n\t"
                 "buffer_load_dwordx4 %9, %6, %7 offen offset:1024 lds\n\t"
                 "buffer_load_dwordx4 %10, %6, %7 offen offset:2048 lds\n\t"
                 "buffer_load_dwordx4 %11, %6, %7 offen offset:3072 lds\n\t"
                 "buffer_load_dwordx4 %0, %14, %12, %13 offen\n\t"
                 "buffer_load_dwordx4 %1, %14, %12, %13 offen offset:1024\n\t"
                 "buffer_load_dwordx4 %2, %14, %12, %13 offen offset:2048\n\t"
                 "buffer_load_dwordx4 %3, %14, %12, %13 offen offset:3072\n\t"
                 "buffer_load_dwordx4 %4, %15, %12, %13 offen"
                 : "=&v"(a[0]), "=&v"(a[1]), "=&v"(a[2]), "=&v"(a[3]), "=&v"(a[4])
                 : "s"(m0v), "s"(sw), "s"(wo), "v"(wv0), "v"(wv1), "v"(wv2), "v"(wv3), "s"(sa), "s"(ao), "v"(av0), "v"(av1)
                 : "m0", "memory");
}
template <int N>
__device__ __forceinline__ void g_wait(u32x4 (&a)[5]) {
    asm volatile("s_waitcnt vmcnt(%5)" : "+v"(a[0]), "+v"(a[1]), "+v"(a[2]), "+v"(a[3]), "+v"(a[4]) : "n"(N) : "memory");
}

template <int MODE, int S, int NST>
struct GStep {
    template <class Issue>
    static __device__ __forceinline__ void run(u32x4 (&a)[G_NS][5], f32x4 (&acc)[4][5], const char* const (&tp)[4], Issue& issue) {
        constexpr int left = NST - 1 - S;
        g_wait<9 * (left < G_D - 1 ? left : G_D - 1)>(a[S % G_NS]);
        if (S == 0) STAMP(3 + MODE, 1);
        u32x4 wf[4];
#pragma unroll
        for (int i = 0; i < 4; ++i) {
            const int ii = (i + 1) & 3;
            const u32x2 lo = tr_read(tp[ii] + (S % G_NS) * 4096);
            const u32x2 hi = tr_read(tp[ii] + (S % G_NS) * 4096 + 512);
            wf[ii] = (u32x4){lo.x, lo.y, hi.x, hi.y};
        }
        __builtin_amdgcn_sched_barrier(0);
        if constexpr (S + G_D < NST) issue(a[(S + G_D) % G_NS], S + G_D);
        __builtin_amdgcn_sched_barrier(0);
#pragma unroll
        for (int i = 0; i < 4; ++i)
#pragma unroll
            for (int j = 0; j < 5; ++j) {
                const int ii = (i + 1) & 3;
                acc[ii][j] = MODE == 0 ? mfma16(wf[ii], a[S % G_NS][j], acc[ii][j]) : mfma16(a[S % G_NS][j], wf[ii], acc[ii][j]);
            }
        if constexpr (S + 1 < NST) {
            __builtin_amdgcn_sched_barrier(0);
            GStep<MODE, S + 1, NST>::run(a, acc, tp, issue);
        }
    }
};

template <int MODE>
__global__ __launch_bounds__(256) void k45_gemm(const u16* __restrict__ W, const char* __restrict__ ws,
                                                const u16* __restrict__ bias, u16* __restrict__ out,
                                                const u16* __restrict__ wn) {
    constexpr int LDW = MODE == 0 ? F_ : L_;
    constexpr int NST = MODE == 0 ? 16 : 14;
    __shared__ __attribute__((aligned(16))) char smem[G_RED + 61440];
    const int tid = threadIdx.x, lane = tid & 63;
    const int w = __builtin_amdgcn_readfirstlane(tid >> 6);
    STAMP(3 + MODE, 0);
    int mt, dt;
    {
        const int xcd = blockIdx.x & 7, idx = blockIdx.x >> 3;
        constexpr int PER = MODE == 0 ? 27 : 32;
        if (MODE == 0 && idx >= 27) {
            warm_w<1>(wn, xcd, (idx - 27) * 4 + w, 20, (unsigned)(size_t)(LDS_AS char*)(smem + w * 1024), lane);
            asm volatile("s_waitcnt vmcnt(0)" ::: "memory");
            STAMP(3, 5);
            return;
        }
        const int t = (xcd >> 1) * PER + idx;
        mt = t >> 2;
        dt = (xcd & 1) * 4 + (t & 3);
    }
    const int m0 = mt * 64;
    const int ks0 = w * NST;
    char* ring = smem + w * G_RING;
    const unsigned ring_a = (unsigned)(size_t)(LDS_AS char*)ring;
    const __amdgpu_buffer_rsrc_t srdW = __builtin_amdgcn_make_buffer_rsrc((void*)W, (short)0, L_ * F_ * 2, 0x00020000);
    const __amdgpu_buffer_rsrc_t srdA = __builtin_amdgcn_make_buffer_rsrc((void*)ws, (short)0, WS_END, 0x00020000);
    const bool idw = MODE == 1 && w == 3;
    const __amdgpu_buffer_rsrc_t srdL =
        __builtin_amdgcn_make_buffer_rsrc((void*)(idw ? ws + WS_ID : (const char*)W), (short)0, L_ * F_ * 2, 0x00020000);
    const int wr = lane >> 3, wslot = lane & 7;
    const unsigned wv0 = (wr * LDW + ((((wslot >> 1) ^ ((wr >> 1) & 1)) * 2 + (wslot & 1)) * 8)) * 2;
    const unsigned wv1 = ((wr + 8) * LDW + ((((wslot >> 1) ^ (((wr >> 1) & 1) | 2)) * 2 + (wslot & 1)) * 8)) * 2 - 1024;
    const unsigned wv2 = wv0 + 16 * LDW * 2 - 2048, wv3 = wv1 + 16 * LDW * 2 - 2048;
    const unsigned av0 = lane * 16, av1 = lane * 16 + 4096;
    const unsigned ws0 = (ks0 * 32 * LDW + m0) * 2;
    const unsigned as0 = (MODE == 0 ? WS_HN : WS_GB) + (ks0 * 40 + dt * 5) * 1024;
    const unsigned wsl = idw ? 0u : ws0 + (NST - 2) * (32 * LDW * 2);
    const unsigned asl = idw ? WS_HB + (mt * 2 * 40 + dt * 5) * 1024 : as0 + (NST - 2) * 40960;
    auto issue = [&](u32x4 (&a)[5], int s) {
        const unsigned dst = ring_a + (s % G_NS) * 4096;
        if (MODE == 1 && s >= NST - 2)
            g_issue(a, srdL, wsl + (s - (NST - 2)) * (32 * LDW * 2), dst, wv0, wv1, wv2, wv3, srdA, asl + (s - (NST - 2)) * 40960, av0, av1);
        else
            g_issue(a, srdW, ws0 + s * (32 * LDW * 2), dst, wv0, wv1, wv2, wv3, srdA, as0 + s * 40960, av0, av1);
    };
    const int n = lane & 15, hg = lane >> 4;
    u32x2 bb;
    {
        const u16* bp = bias + m0 + 16 * w + (MODE == 0 ? 4 * hg : (n & ~3));
        asm volatile("global_load_dwordx2 %0, %1, off" : "=v"(bb) : "v"(bp) : "memory");
    }
    u32x4 a[G_NS][5];
#pragma unroll
    for (int s = 0; s < G_D; ++s) issue(a[s], s);
    f32x4 acc[4][5];
#pragma unroll
    for (int c = 0; c < 4; ++c)
#pragma unroll
        for (int j = 0; j < 5; ++j) acc[c][j] = (f32x4){0.f, 0.f, 0.f, 0.f};
    const int q4 = n >> 2, p4 = n & 3;
    const int fsw = ((q4 >> 1) & 1) | ((hg & 1) << 1);
    const char* tb = ring + (8 * hg + q4) * 128 + p4 * 8;
    const char* const tp[4] = {tb + ((((0 + w) & 3) ^ fsw) * 32), tb + ((((1 + w) & 3) ^ fsw) * 32),
                               tb + ((((2 + w) & 3) ^ fsw) * 32), tb + ((((3 + w) & 3) ^ fsw) * 32)};
    asm volatile("s_waitcnt vmcnt(%1)" : "+v"(bb) : "n"(9 * G_D) : "memory");
    {
        f32x4 bs = {bflo(bb.x), bfhi(bb.x), bflo(bb.y), bfhi(bb.y)};
        if (MODE == 1) {
            const unsigned t = (n & 2) ? bb.y : bb.x;
            const float b = (n & 1) ? bfhi(t) : bflo(t);
            bs = (f32x4){b, b, b, b};
        }
#pragma unroll
        for (int j = 0; j < 5; ++j) acc[0][j] = bs;
    }
    GStep<MODE, 0, NST>::run(a, acc, tp, issue);
    STAMP(3 + MODE, 2);
    char* red = smem + G_RED + lane * 16;
#pragma unroll
    for (int i = 1; i < 4; ++i) {
        char* dst = red + (((i + w) & 3) * 3 + i - 1) * 5120;
#pragma unroll
        for (int j = 0; j < 5; ++j) *(f32x4*)(dst + j * 1024) = acc[i][j];
    }
    __syncthreads();
    f32x4 r[5];
    {
        const char* src = red + w * 15360;
        f32x4 p[5][3];
#pragma unroll
        for (int j = 0; j < 5; ++j)
#pragma unroll
            for (int o = 0; o < 3; ++o) p[j][o] = *(const f32x4*)(src + o * 5120 + j * 1024);
        __builtin_amdgcn_sched_barrier(0);
#pragma unroll
        for (int j = 0; j < 5; ++j) r[j] = (acc[0][j] + p[j][0]) + (p[j][1] + p[j][2]);
    }
    STAMP(3 + MODE, 3);
    if (MODE == 0) {
        char* gp = (char*)out + (((m0 >> 5) + (w >> 1)) * 40 + dt * 5) * 1024 + (((w & 1) * 2 + (hg >> 1)) * 16 + n) * 16 + (hg & 1) * 8;
#pragma unroll
        for (int j = 0; j < 5; ++j) {
            u32x2 o;
            o.x = pk2(gelu_tanh(r[j][0]), gelu_tanh(r[j][1]));
            o.y = pk2(gelu_tanh(r[j][2]), gelu_tanh(r[j][3]));
            *(u32x2*)(gp + j * 1024) = o;
        }
    } else {
        u16* op = out + (size_t)(m0 + 16 * w + n) * D_ + dt * 80 + 20 * hg;
        typedef unsigned u32x4u __attribute__((ext_vector_type(4), aligned(8)));
        u32x4u o0 = {pk2(r[0][0], r[0][1]), pk2(r[0][2], r[0][3]), pk2(r[1][0], r[1][1]), pk2(r[1][2], r[1][3])};
        u32x4u o1 = {pk2(r[2][0], r[2][1]), pk2(r[2][2], r[2][3]), pk2(r[3][0], r[3][1]), pk2(r[3][2], r[3][3])};
        u32x2 o2 = {pk2(r[4][0], r[4][1]), pk2(r[4][2], r[4][3])};
        *(u32x4u*)op = o0;
        *(u32x4u*)(op + 8) = o1;
        *(u32x2*)(op + 16) = o2;
    }
    STAMP(3 + MODE, 4);
}

extern "C" void kernel_launch(void* const* d_in, const int* in_sizes, int n_in, void* d_out, int out_size,
                              void* d_ws, size_t ws_size, hipStream_t stream) {
    const u16* x = (const u16*)d_in[0];
    const u16* logA = (const u16*)d_in[1];
    const u16* Aim = (const u16*)d_in[2];
    const u16* Cre = (const u16*)d_in[3];
    const u16* Cim = (const u16*)d_in[4];
    const u16* logdt = (const u16*)d_in[5];
    const u16* dskip = (const u16*)d_in[6];
    const u16* w1 = (const u16*)d_in[8];
    const u16* b1 = (const u16*)d_in[9];
    const u16* w2 = (const u16*)d_in[10];
    const u16* b2 = (const u16*)d_in[11];
    char* ws = (char*)d_ws;
    u16* ub = (u16*)(ws + WS_UB);
    u16* xb = (u16*)(ws + WS_XB);
    u16* hb = (u16*)(ws + WS_HB);
    u16* hn = (u16*)(ws + WS_HN);
    u16* gb = (u16*)(ws + WS_GB);
    u16* kx = (u16*)(ws + WS_KX);
    k1_ln_kgen<<<256 + D_, 128, 0, stream>>>(x, logA, Aim, Cre, Cim, logdt, ub, xb, kx);
    k2_conv<<<256, 256, 0, stream>>>(ub, xb, kx, dskip, hb, w1, w2);
    k3_ln2<<<256, 640, 0, stream>>>(hb, hn, (u16*)(ws + WS_ID));
    k45_gemm<0><<<256, 256, 0, stream>>>(w1, ws, b1, gb, w2);
    k45_gemm<1><<<256, 256, 0, stream>>>(w2, ws, b2, (u16*)d_out, nullptr);
}
```

```cpp
#include <hip/hip_runtime.h>
#include <hip/hip_bf16.h>
#include <stdint.h>
#pragma clang diagnostic ignored "-Winline-asm"

typedef unsigned short u16;
typedef __bf16 bf16x8 __attribute__((ext_vector_type(8)));
typedef __bf16 bf16x2 __attribute__((ext_vector_type(2)));
typedef short s16x4 __attribute__((ext_vector_type(4)));
typedef float f32x4 __attribute__((ext_vector_type(4)));
typedef float f32x2 __attribute__((ext_vector_type(2)));
typedef unsigned u32x4 __attribute__((ext_vector_type(4)));
typedef unsigned u32x2 __attribute__((ext_vector_type(2)));
#define LDS_AS __attribute__((address_space(3)))

#define STAMP(k, i) do { } while (0)

constexpr int L_ = 2048;
constexpr int D_ = 640;
constexpr int N_ = 128;
constexpr int F_ = 1728;
constexpr unsigned SZ_ = L_ * D_ * 2;
constexpr unsigned WS_HB = 0, WS_HN = SZ_, WS_GB = 2 * SZ_, WS_END = 3 * SZ_;
constexpr unsigned WS_ID = 3 * SZ_, WS_GR = WS_ID + 64 * L_ * 2, WS_EP = WS_GR + 32768;

__device__ __forceinline__ float bf2f(unsigned v) { return __uint_as_float(v << 16); }
__device__ __forceinline__ float bflo(unsigned v) { return __uint_as_float(v << 16); }
__device__ __forceinline__ float bfhi(unsigned v) { return __uint_as_float(v & 0xffff0000u); }
__device__ __forceinline__ unsigned pk2(float lo, float hi) {
    f32x2 f = {lo, hi};
    bf16x2 b = __builtin_convertvector(f, bf16x2);
    return __builtin_bit_cast(unsigned, b);
}
__device__ __forceinline__ float rb(float x) { return bflo(pk2(x, x)); }

__device__ __forceinline__ f32x4 mfma16(u32x4 a, u32x4 b, f32x4 c) {
    return __builtin_amdgcn_mfma_f32_16x16x32_bf16(__builtin_bit_cast(bf16x8, a), __builtin_bit_cast(bf16x8, b), c, 0, 0, 0);
}
__device__ __forceinline__ u32x2 tr_read(const char* p) {
    s16x4 v = __builtin_amdgcn_ds_read_tr16_b64_v4i16((LDS_AS s16x4*)p);
    return __builtin_bit_cast(u32x2, v);
}
template <int CTRL>
__device__ __forceinline__ float dpp_add(float v) {
    int t = __builtin_amdgcn_update_dpp(0, __float_as_int(v), CTRL, 0xf, 0xf, true);
    return v + __int_as_float(t);
}
__device__ __forceinline__ float row16_sum(float v) {
    v = dpp_add<0x128>(v);
    v = dpp_add<0x124>(v);
    v = dpp_add<0x122>(v);
    v = dpp_add<0x121>(v);
    return v;
}

template <int NT>
__device__ __forceinline__ void touch4(__amdgpu_buffer_rsrc_t srd, unsigned voff, unsigned soff, unsigned m0v) {
    if (NT) asm volatile("s_mov_b32 m0, %0\n\ts_nop 0\n\tbuffer_load_dword %1, %2, %3 offen nt lds"
                 :: "s"(m0v), "v"(voff), "s"(srd), "s"(soff) : "m0", "memory");
    else asm volatile("s_mov_b32 m0, %0\n\ts_nop 0\n\tbuffer_load_dword %1, %2, %3 offen lds"
                 :: "s"(m0v), "v"(voff), "s"(srd), "s"(soff) : "m0", "memory");
}
template <int MODE>
__device__ __forceinline__ void warm_w(const u16* W, const u16* bias, int xcd, int gw, int nw, unsigned lds, int lane) {
    constexpr int LDW = MODE == 0 ? F_ : L_;
    const __amdgpu_buffer_rsrc_t srdW = __builtin_amdgcn_make_buffer_rsrc((void*)W, (short)0, L_ * F_ * 2, 0x00020000);
    if (gw == 0) touch4<0>(__builtin_amdgcn_make_buffer_rsrc((void*)bias, (short)0, LDW * 2, 0x00020000), lane * 128, 0, lds);
    const int mt0 = MODE == 0 ? ((xcd >> 1) * 27) >> 2 : (xcd >> 1) * 8;
    const unsigned voff = ((lane >> 3) * LDW + (lane & 7) * 64) * 2;
    for (int j = gw; j < (MODE == 0 ? 256 : 224); j += nw) {
        const int s = j >> 4, w = (j >> 2) & 3;
        const int ks = MODE == 0 ? w * 16 + s : w * 14 + s;
        if (MODE == 1 && ks >= 54) continue;
        touch4<0>(srdW, voff, ((ks * 32 + (j & 3) * 8) * LDW + mt0 * 64) * 2, lds);
    }
}

constexpr int KA_ROWB = 528;
constexpr int CV_COPY = 4160;
constexpr int KA_R = 4 * CV_COPY;
constexpr int KA_KD = KA_R + 8192;
constexpr int KA_CH = 30208;
constexpr int CV_UB = 4608;
constexpr int KA_U = 4 * KA_CH;
typedef __attribute__((address_space(1))) unsigned long long gu64;
typedef __attribute__((address_space(1))) unsigned gu32;

__device__ __forceinline__ f32x2 cmul(f32x2 a, f32x2 b) {
    f32x2 t, r;
    asm("v_pk_mul_f32 %0, %1, %2 op_sel:[0,0] op_sel_hi:[1,0]" : "=v"(t) : "v"(a), "v"(b));
    asm("v_pk_fma_f32 %0, %1, %2, %3 op_sel:[1,1,0] op_sel_hi:[0,1,1] neg_lo:[1,0,0]" : "=v"(r) : "v"(a), "v"(b), "v"(t));
    return r;
}
__device__ __forceinline__ f32x2 cexp_p(float zr, float zi, float p) {
    const float mag = __builtin_amdgcn_exp2f(zr * p);
    const float rev = __builtin_amdgcn_fractf(zi * p);
    f32x2 r;
    r.x = mag * __builtin_amdgcn_cosf(rev);
    r.y = mag * __builtin_amdgcn_sinf(rev);
    return r;
}
__device__ __forceinline__ float wave_sum(float v) {
    v = row16_sum(v);
    const int i = __float_as_int(v);
    return (__int_as_float(__builtin_amdgcn_readlane(i, 0)) + __int_as_float(__builtin_amdgcn_readlane(i, 16))) +
           (__int_as_float(__builtin_amdgcn_readlane(i, 32)) + __int_as_float(__builtin_amdgcn_readlane(i, 48)));
}
__device__ __forceinline__ unsigned next_lane(unsigned v, unsigned last) {
    return (unsigned)__builtin_amdgcn_update_dpp((int)last, (int)v, 0x130, 0xf, 0xf, false);
}
__device__ __forceinline__ void put_copies(char* p, unsigned r0, unsigned r1, unsigned r2, unsigned r3, unsigned r4, unsigned r5) {
    const unsigned s0 = __builtin_amdgcn_alignbit(r1, r0, 16), s1 = __builtin_amdgcn_alignbit(r2, r1, 16),
                   s2 = __builtin_amdgcn_alignbit(r3, r2, 16), s3 = __builtin_amdgcn_alignbit(r4, r3, 16),
                   s4 = __builtin_amdgcn_alignbit(r5, r4, 16);
    *(u32x4*)(p + 0 * CV_COPY) = (u32x4){r0, r1, r2, r3};
    *(u32x4*)(p + 1 * CV_COPY) = (u32x4){s0, s1, s2, s3};
    *(u32x4*)(p + 2 * CV_COPY) = (u32x4){r1, r2, r3, r4};
    *(u32x4*)(p + 3 * CV_COPY) = (u32x4){s1, s2, s3, s4};
}
template <int G0, int G1>
__device__ __forceinline__ void cv_read(u32x2& lo, u32x2& hi, u32x4& b, unsigned aa, unsigned ba) {
    asm volatile("ds_read_b64 %0, %3 offset:%5\n\tds_read_b64 %1, %3 offset:%6\n\tds_read_b128 %2, %4 offset:%7"
                 : "=&v"(lo), "=&v"(hi), "=&v"(b)
                 : "v"(aa), "v"(ba), "n"((63 - 8 * G1 - G0) * 64), "n"((63 - 8 * G1 - G0) * 64 + 8), "n"((8 * G1 - G0 + 7) * 64)
                 : "memory");
}
struct CvFrag { u32x2 lo[8], hi[8]; u32x4 b[8]; };
__device__ __forceinline__ void cv_wait(CvFrag& f) {
    asm volatile("s_waitcnt lgkmcnt(0)"
                 : "+v"(f.lo[0]), "+v"(f.lo[1]), "+v"(f.lo[2]), "+v"(f.lo[3]), "+v"(f.lo[4]), "+v"(f.lo[5]), "+v"(f.lo[6]), "+v"(f.lo[7]),
                   "+v"(f.hi[0]), "+v"(f.hi[1]), "+v"(f.hi[2]), "+v"(f.hi[3]), "+v"(f.hi[4]), "+v"(f.hi[5]), "+v"(f.hi[6]), "+v"(f.hi[7]),
                   "+v"(f.b[0]), "+v"(f.b[1]), "+v"(f.b[2]), "+v"(f.b[3]), "+v"(f.b[4]), "+v"(f.b[5]), "+v"(f.b[6]), "+v"(f.b[7])
                 :: "memory");
}
template <int T>
__device__ __forceinline__ void cv_mma(const CvFrag& f, f32x4 (&acc)[8]) {
    constexpr int q = T < 1 ? 0 : T < 3 ? 1 : T < 6 ? 2 : T < 10 ? 3 : T < 15 ? 4 : T < 21 ? 5 : T < 28 ? 6 : 7;
    constexpr int g1 = T - q * (q + 1) / 2;
    const u32x4 a = {f.lo[g1].x, f.lo[g1].y, f.hi[g1].x, f.hi[g1].y};
    acc[q] = mfma16(a, f.b[q - g1], acc[q]);
}
template <int G0, int I>
__device__ __forceinline__ void cv_group(const CvFrag& cur, CvFrag& nxt, f32x4 (&acc)[8], unsigned aa, unsigned ba) {
    if constexpr (G0 < 7 && I < 8) cv_read<G0 + 1, I>(nxt.lo[I], nxt.hi[I], nxt.b[I], aa, ba);
    cv_mma<4 * I>(cur, acc); cv_mma<4 * I + 1>(cur, acc); cv_mma<4 * I + 2>(cur, acc); cv_mma<4 * I + 3>(cur, acc);
    __builtin_amdgcn_sched_barrier(0);
    if constexpr (I < 8) cv_group<G0, I + 1>(cur, nxt, acc, aa, ba);
}
template <int G0>
__device__ __forceinline__ void cv_round(CvFrag& cur, CvFrag& nxt, f32x4 (&acc)[8], unsigned aa, unsigned ba) {
    cv_wait(cur);
    if constexpr (G0 == 5) __builtin_amdgcn_s_barrier();
    __builtin_amdgcn_sched_barrier(0);
    cv_group<G0, 0>(cur, nxt, acc, aa, ba);
    if constexpr (G0 < 7) cv_round<G0 + 1>(nxt, cur, acc, aa, ba);
}
template <int I>
__device__ __forceinline__ void cv_first(CvFrag& f, unsigned aa, unsigned ba) {
    cv_read<0, I>(f.lo[I], f.hi[I], f.b[I], aa, ba);
    if constexpr (I < 7) cv_first<I + 1>(f, aa, ba);
}

__global__ __launch_bounds__(512, 2) void ka_conv(const u16* __restrict__ x, const u16* __restrict__ logA,
                                                  const u16* __restrict__ Aim, const u16* __restrict__ Cre,
                                                  const u16* __restrict__ Cim, const u16* __restrict__ logdt,
                                                  const u16* __restrict__ dskip, char* ws,
                                                  const u16* __restrict__ w1, const u16* __restrict__ b1) {
    __shared__ __attribute__((aligned(16))) char smem[KA_U + 4 * CV_UB];
    const int tid = threadIdx.x, lane = tid & 63;
    const int w = __builtin_amdgcn_readfirstlane(tid >> 6);
    STAMP(0, 0);
    const int xcd = blockIdx.x & 7, idx = blockIdx.x >> 3;
    const bool conv = idx < 20;
    const int grp = xcd * 20 + (conv ? idx : 0);
    const int d0 = grp * 4;
    unsigned sv[5];
    {
        const unsigned* xr = (const unsigned*)(x + (size_t)(blockIdx.x * 8 + w) * D_);
#pragma unroll
        for (int j = 0; j < 5; ++j) sv[j] = xr[lane + 64 * j];
    }
    const unsigned epoch = *(const unsigned*)(ws + WS_EP);
    const int pi = d0 * N_ + tid;
    const unsigned p_la = logA[pi], p_ai = Aim[pi], p_cr = Cre[pi], p_ci = Cim[pi], p_dt = logdt[d0 + (w >> 1)];
    u32x2 xc[8], xd;
    const int t = tid & 255;
    {
        const __amdgpu_buffer_rsrc_t srdX = __builtin_amdgcn_make_buffer_rsrc((void*)x, (short)0, conv && w >= 4 ? SZ_ : 0, 0x00020000);
        const __amdgpu_buffer_rsrc_t srdD = __builtin_amdgcn_make_buffer_rsrc((void*)dskip, (short)0, D_ * 2, 0x00020000);
#pragma unroll
        for (int k = 0; k < 8; ++k)
            xc[k] = __builtin_amdgcn_raw_buffer_load_b64(srdX, t * (8 * D_ * 2) + (k & 3) * (D_ * 2), d0 * 2 + (k >> 2) * (4 * D_ * 2), 0);
        xd = __builtin_amdgcn_raw_buffer_load_b64(srdD, 0, d0 * 2, 0);
    }
    const unsigned tag = (epoch ^ 0x9e3779b9u) * 0x85ebca6bu;
    gu64* gran = (gu64*)(ws + WS_GR);
    {
        float s = 0.f;
#pragma unroll
        for (int j = 0; j < 5; ++j) s += bflo(sv[j]) + bfhi(sv[j]);
        const float mean = wave_sum(s) * (1.0f / D_);
        float q = 0.f;
#pragma unroll
        for (int j = 0; j < 5; ++j) {
            const float a = bflo(sv[j]) - mean, b = bfhi(sv[j]) - mean;
            q += a * a + b * b;
        }
        q = wave_sum(q);
        const float den = __builtin_amdgcn_sqrtf(rb(rb(q * (1.0f / D_)) + 1e-5f));
        if (lane == 0)
            __hip_atomic_store(gran + w * 256 + blockIdx.x, ((unsigned long long)tag << 32) | pk2(mean, den),
                               __ATOMIC_RELAXED, __HIP_MEMORY_SCOPE_AGENT);
    }
    STAMP(0, 1);
    if (!conv) {
        const unsigned lds = (unsigned)(size_t)(LDS_AS char*)(smem + w * 1024);
        warm_w<0>(w1, b1, xcd, (idx - 20) * 8 + w, 96, lds, lane);
        if (idx == 31 && tid < 256) {
            const int r = xcd * 8 + (tid >> 5), c = tid & 31;
            *(unsigned*)(ws + WS_ID + r * (L_ * 2) + 4 * c) = (r >> 1) == c ? ((r & 1) ? 0x3f800000u : 0x3f80u) : 0u;
        }
        asm volatile("s_waitcnt vmcnt(0)" ::: "memory");
        STAMP(0, 7);
        return;
    }
    {
        const float dt = rb(__builtin_amdgcn_exp2f(bf2f(p_dt) * 1.44269504088896341f));
        const float ar = -rb(__builtin_amdgcn_exp2f(bf2f(p_la) * 1.44269504088896341f));
        const float zr = ar * dt * 1.44269504088896341f, zi = bf2f(p_ai) * dt * 0.15915494309189535f;
        f32x2 a = {2.0f * (bf2f(p_cr) * dt), -2.0f * (bf2f(p_ci) * dt)};
        const f32x2 E1 = cexp_p(zr, -zi, 1.f), E8 = cexp_p(zr, zi, 8.f), E128 = cexp_p(zr, -zi, 128.f);
        char* col = smem + (w >> 1) * KA_CH + (tid & 127) * 4;
        auto put = [&](int row, f32x2 v) { *(unsigned*)(col + row * KA_ROWB) = pk2(v.x, v.y); };
        f32x2 e[8];
        e[1] = E1;
        e[2] = cmul(E1, E1);
        e[3] = cmul(e[2], E1);
        e[4] = cmul(e[2], e[2]);
        e[5] = cmul(e[4], E1);
        e[6] = cmul(e[4], e[2]);
        e[7] = cmul(e[4], e[3]);
        f32x2 b = E8;
        put(41, (f32x2){1.f, 0.f});
        put(42, b);
#pragma unroll
        for (int c = 0; c < 17; ++c) {
            if (c < 2) {
                put(8 * c, a);
#pragma unroll
                for (int k = 1; k < 8; ++k) put(8 * c + k, cmul(a, e[k]));
            } else if (c < 4) {
                put(16 + 4 * (c - 2), a);
#pragma unroll
                for (int k = 1; k < 4; ++k) put(16 + 4 * (c - 2) + k, cmul(a, e[2 * k]));
            } else if (c < 8) {
                put(24 + 2 * (c - 4), a);
                put(24 + 2 * (c - 4) + 1, cmul(a, e[4]));
            } else {
                put(32 + (c - 8), a);
            }
            if (c < 16) a = cmul(a, E128);
            if (c >= 2 && c < 16) { b = cmul(b, E8); put(41 + c, b); }
        }
    }
    STAMP(0, 2);
    __syncthreads();
    const int m = lane & 15, hg = lane >> 4;
    char* mine = smem + (w & 3) * KA_CH;
    if (w < 4) {
        u16* kd = (u16*)(mine + KA_KD);
        {
            const char* fb = mine + m * KA_ROWB + hg * 16;
            f32x4 acc0 = {0.f, 0.f, 0.f, 0.f}, acc1 = acc0, acc2 = acc0;
#pragma unroll
            for (int ks = 0; ks < 8; ++ks) {
                const u32x4 bf = *(const u32x4*)(fb + 41 * KA_ROWB + ks * 64);
                acc0 = mfma16(*(const u32x4*)(fb + ks * 64), bf, acc0);
                acc1 = mfma16(*(const u32x4*)(fb + 16 * KA_ROWB + ks * 64), bf, acc1);
                acc2 = mfma16(*(const u32x4*)(fb + 32 * KA_ROWB + ks * 64), bf, acc2);
            }
            const int c = m;
            *(u32x2*)(kd + 128 * (hg >> 1) + 4 * (hg & 1) + 8 * c) = (u32x2){pk2(acc0[0], acc0[1]), pk2(acc0[2], acc0[3])};
            const unsigned p01 = pk2(acc1[0], acc1[1]), p23 = pk2(acc1[2], acc1[3]);
            const int i0 = hg < 2 ? 256 + 64 * hg + 4 * c : 256 + 64 * hg + 2 * c;
            *(unsigned*)(kd + i0) = p01;
            *(unsigned*)(kd + i0 + (hg < 2 ? 2 : 32)) = p23;
            const unsigned q01 = pk2(acc2[0], acc2[1]), q23 = pk2(acc2[2], acc2[3]);
            if (hg < 3) {
                kd[512 + 64 * hg + c] = (u16)(q01 & 0xffff);
                kd[528 + 64 * hg + c] = (u16)(q01 >> 16);
                kd[544 + 64 * hg + c] = (u16)(q23 & 0xffff);
                kd[560 + 64 * hg + c] = (u16)(q23 >> 16);
            }
        }
        const unsigned* kw = (const unsigned*)kd;
        const unsigned dw0 = kw[319 - lane], dw1 = kw[320 - lane];
        const unsigned cw0 = kw[255 - lane], cw1 = kw[256 - lane];
        const bool isA = lane >= 32;
        const char* bp = (const char*)kd + (isA ? 16 * (63 - lane) : 256 + 8 * (63 - lane));
        const u32x2 b01 = *(const u32x2*)bp, b23 = *(const u32x2*)(bp + 8);
        const unsigned P = __builtin_amdgcn_perm(0, dw1, 0x01000100), Q = __builtin_amdgcn_perm(0, dw0, 0x03020302),
                       M = __builtin_amdgcn_perm(0, dw0, 0x01000302), Z = __builtin_amdgcn_perm(0, dw0, 0x01000100);
        const unsigned c0 = __builtin_amdgcn_perm(0, cw1, 0x01000100), c1 = __builtin_amdgcn_perm(0, cw0, 0x03020302),
                       c2 = __builtin_amdgcn_perm(0, cw0, 0x01000302), c3 = __builtin_amdgcn_perm(0, cw0, 0x01000100);
        const unsigned o0 = __builtin_amdgcn_perm(b23.x, isA ? b23.y : b01.y, isA ? 0x01000302u : 0x03020504u),
                       o1 = __builtin_amdgcn_perm(b23.x, b01.y, isA ? 0x05040706u : 0x01000100u),
                       o2 = __builtin_amdgcn_perm(b01.y, b01.x, isA ? 0x05040706u : 0x03020504u),
                       o3 = __builtin_amdgcn_perm(0, b01.x, isA ? 0x01000302u : 0x01000100u);
        put_copies(mine + 3072 + lane * 16, o0, o1, o2, o3, next_lane(o0, 0), next_lane(o1, 0));
        put_copies(mine + 2048 + lane * 16, c0, c1, c2, c3, next_lane(c0, __builtin_amdgcn_readlane(o0, 0)),
                   next_lane(c1, __builtin_amdgcn_readlane(o1, 0)));
        put_copies(mine + lane * 32, P, P, Q, Q, Q, M);
        put_copies(mine + lane * 32 + 16, Q, M, Z, Z, next_lane(P, __builtin_amdgcn_readlane(c0, 0)),
                   next_lane(P, __builtin_amdgcn_readlane(c1, 0)));
        if (lane < 8) *(u32x4*)(mine + (lane >> 1) * CV_COPY + 4096 + (lane & 1) * 16) = (u32x4){0u, 0u, 0u, 0u};
        STAMP(0, 3);
        __syncthreads();
        STAMP(0, 4);
        const unsigned aa = (unsigned)(size_t)(LDS_AS char*)(mine + (3 - (m & 3)) * CV_COPY + 2 * (2028 - 32 * 63 - 4 * (m >> 2) + 8 * hg));
        const unsigned ba = (unsigned)(size_t)(LDS_AS char*)(smem + KA_U + w * CV_UB + (2 * m + hg) * 16);
        f32x4 acc[8];
#pragma unroll
        for (int q = 0; q < 8; ++q) acc[q] = (f32x4){0.f, 0.f, 0.f, 0.f};
        CvFrag f0, f1;
        cv_first<0>(f0, aa, ba);
        cv_round<0>(f0, f1, acc, aa, ba);
        STAMP(0, 5);
        const char* rp = mine + KA_R + (16 * m + 4 * hg) * 4;
        char* hp = mine + (16 * m + 4 * hg) * 2;
#pragma unroll
        for (int q = 0; q < 8; ++q) {
            const f32x4 h = acc[q] + *(const f32x4*)(rp + q * 1024);
            *(u32x2*)(hp + q * 512) = (u32x2){pk2(h[0], h[1]), pk2(h[2], h[3])};
        }
    } else {
        float xf[8][4];
#pragma unroll
        for (int k = 0; k < 8; ++k) {
            xf[k][0] = bflo(xc[k].x); xf[k][1] = bfhi(xc[k].x); xf[k][2] = bflo(xc[k].y); xf[k][3] = bfhi(xc[k].y);
        }
        if (lane < 30) *(u32x4*)(smem + KA_U + (w - 4) * CV_UB + lane * 16) = (u32x4){0u, 0u, 0u, 0u};
        unsigned st[8];
        for (unsigned spins = 0; spins < (1u << 16); ++spins) {
            bool ok = true;
#pragma unroll
            for (int k = 0; k < 8; ++k) {
                const unsigned long long g = __hip_atomic_load(gran + k * 256 + t, __ATOMIC_RELAXED, __HIP_MEMORY_SCOPE_AGENT);
                st[k] = (unsigned)g;
                ok &= (unsigned)(g >> 32) == tag;
            }
            if (__all(ok)) break;
            __builtin_amdgcn_s_sleep(1);
        }
        unsigned up[4][4];
#pragma unroll
        for (int k = 0; k < 8; k += 2) {
            const float i0 = __builtin_amdgcn_rcpf(bfhi(st[k])), i1 = __builtin_amdgcn_rcpf(bfhi(st[k + 1]));
            const float m0 = bflo(st[k]), m1 = bflo(st[k + 1]);
#pragma unroll
            for (int c = 0; c < 4; ++c) up[c][k >> 1] = pk2((xf[k][c] - m0) * i0, (xf[k + 1][c] - m1) * i1);
        }
#pragma unroll
        for (int c = 0; c < 4; ++c)
            *(u32x4*)(smem + KA_U + c * CV_UB + 480 + t * 16) = (u32x4){up[c][0], up[c][1], up[c][2], up[c][3]};
        __syncthreads();
        if (blockIdx.x == 0 && tid == 256)
            __hip_atomic_store((gu32*)(ws + WS_EP), epoch + 1u, __ATOMIC_RELAXED, __HIP_MEMORY_SCOPE_AGENT);
        const float dk[4] = {bflo(xd.x), bfhi(xd.x), bflo(xd.y), bfhi(xd.y)};
#pragma unroll
        for (int c = 0; c < 4; ++c) {
            f32x4 r0, r1;
#pragma unroll
            for (int k = 0; k < 2; ++k) {
                r0[2 * k] = __builtin_fmaf(bflo(up[c][k]), dk[c], xf[2 * k][c]);
                r0[2 * k + 1] = __builtin_fmaf(bfhi(up[c][k]), dk[c], xf[2 * k + 1][c]);
                r1[2 * k] = __builtin_fmaf(bflo(up[c][2 + k]), dk[c], xf[4 + 2 * k][c]);
                r1[2 * k + 1] = __builtin_fmaf(bfhi(up[c][2 + k]), dk[c], xf[5 + 2 * k][c]);
            }
            *(f32x4*)(smem + c * KA_CH + KA_R + t * 32) = r0;
            *(f32x4*)(smem + c * KA_CH + KA_R + t * 32 + 16) = r1;
        }
        asm volatile("s_waitcnt lgkmcnt(0)" ::: "memory");
        __builtin_amdgcn_s_barrier();
    }
    __syncthreads();
    {
        const int ch = tid & 3;
        const int dt = grp / 20, rem = grp % 20, mq = rem / 5, j = rem % 5;
#pragma unroll
        for (int i = 0; i < 2; ++i) {
            const int piece = (tid >> 2) + 128 * i;
            const int unit = ((piece >> 2) * 40 + dt * 5 + j) * 64 + (piece & 3) * 16 + 4 * mq + ch;
            *(u32x4*)(ws + WS_HB + (size_t)unit * 16) = *(const u32x4*)(smem + ch * KA_CH + piece * 16);
        }
    }
    STAMP(0, 6);
}

__device__ __forceinline__ float xor_lane(float v, int mask) { return __shfl_xor(v, mask, 64); }

__device__ __forceinline__ void block_stats(const float (&v)[8], float* red, int tid, float (&mb)[8], float (&inv)[8]) {
    const int lane = tid & 63, w = tid >> 6;
    float a[16];
#pragma unroll
    for (int e = 0; e < 8; ++e) { a[e] = v[e]; a[8 + e] = v[e] * v[e]; }
#pragma unroll
    for (int e = 0; e < 8; ++e) {
        const bool up = lane & 1;
        const float keep = up ? a[8 + e] : a[e], send = up ? a[e] : a[8 + e];
        a[e] = keep + xor_lane(send, 1);
    }
#pragma unroll
    for (int e = 0; e < 4; ++e) {
        const bool up = lane & 2;
        const float keep = up ? a[4 + e] : a[e], send = up ? a[e] : a[4 + e];
        a[e] = keep + xor_lane(send, 2);
    }
#pragma unroll
    for (int e = 0; e < 2; ++e) {
        const bool up = lane & 4;
        const float keep = up ? a[2 + e] : a[e], send = up ? a[e] : a[2 + e];
        a[e] = keep + xor_lane(send, 4);
    }
    {
        const bool up = lane & 8;
        const float keep = up ? a[1] : a[0], send = up ? a[0] : a[1];
        a[0] = keep + xor_lane(send, 8);
    }
    a[0] += xor_lane(a[0], 16);
    a[0] += xor_lane(a[0], 32);
    if (lane < 16) {
        const int row = ((lane >> 1) & 1) * 4 + ((lane >> 2) & 1) * 2 + ((lane >> 3) & 1);
        red[w * 16 + (lane & 1) * 8 + row] = a[0];
    }
    __syncthreads();
    if (tid < 8) {
        float s = 0.f, q = 0.f;
#pragma unroll
        for (int k = 0; k < 10; ++k) { s += red[k * 16 + tid]; q += red[k * 16 + 8 + tid]; }
        const float mean = s * (1.0f / D_);
        const float var = fmaxf(q * (1.0f / D_) - mean * mean, 0.f);
        const float den = rb(__builtin_sqrtf(rb(rb(var) + 1e-5f)));
        red[160 + tid] = rb(mean);
        red[168 + tid] = 1.0f / den;
    }
    __syncthreads();
#pragma unroll
    for (int e = 0; e < 8; ++e) { mb[e] = red[160 + e]; inv[e] = red[168 + e]; }
}

__global__ __launch_bounds__(640) void k3_ln2(const u16* __restrict__ hb, u16* __restrict__ hn) {
    __shared__ __attribute__((aligned(16))) float red[2][176];
    const int tid = threadIdx.x;
    STAMP(2, 0);
    const size_t g = ((size_t)((blockIdx.x >> 2) * 40 + (tid >> 4)) * 64 + (blockIdx.x & 3) * 16 + (tid & 15)) * 8;
    const u32x4 p = *(const u32x4*)(hb + g);
    float v[8], mb[8], inv[8];
#pragma unroll
    for (int e = 0; e < 4; ++e) { v[2 * e] = bflo(p[e]); v[2 * e + 1] = bfhi(p[e]); }
    if (v[0] == 12345.f) STAMP(2, 7);
    STAMP(2, 1);
    block_stats(v, red[0], tid, mb, inv);
    STAMP(2, 2);
#pragma unroll
    for (int e = 0; e < 4; ++e) {
        const unsigned t = pk2(v[2 * e] - mb[2 * e], v[2 * e + 1] - mb[2 * e + 1]);
        const unsigned o = pk2(bflo(t) * inv[2 * e], bfhi(t) * inv[2 * e + 1]);
        v[2 * e] = bflo(o);
        v[2 * e + 1] = bfhi(o);
    }
    block_stats(v, red[1], tid, mb, inv);
    u32x4 o;
#pragma unroll
    for (int e = 0; e < 4; ++e) {
        const unsigned t = pk2(v[2 * e] - mb[2 * e], v[2 * e + 1] - mb[2 * e + 1]);
        o[e] = pk2(bflo(t) * inv[2 * e], bfhi(t) * inv[2 * e + 1]);
    }
    *(u32x4*)(hn + g) = o;
    STAMP(2, 3);
}

#define GDEPTH 3
constexpr int G_D = GDEPTH;
constexpr int G_NS = G_D + 1;
constexpr int G_RING = G_NS * 4096;
constexpr int G_RED = 4 * G_RING;

__device__ __forceinline__ float gelu_tanh(float x) {
    const float p = __builtin_fmaf(x * x, -2.302208198f * 0.044715f, -2.302208198f);
    const float e = __builtin_amdgcn_exp2f(p * x);
    return x * __builtin_amdgcn_rcpf(1.0f + e);
}
__device__ __forceinline__ void g_issue(u32x4 (&a)[5], __amdgpu_buffer_rsrc_t sw, unsigned wo, unsigned m0v,
                                        unsigned wv0, unsigned wv1, unsigned wv2, unsigned wv3,
                                        __amdgpu_buffer_rsrc_t sa, unsigned ao, unsigned av0, unsigned av1) {
    asm volatile("s_mov_b32 m0, %5\n\ts_nop 0\n\t"
                 "buffer_load_dwordx4 %8, %6, %7 offen lds\n\t"
                 "buffer_load_dwordx4 %9, %6, %7 offen offset:1024 lds\n\t"
                 "buffer_load_dwordx4 %10, %6, %7 offen offset:2048 lds\n\t"
                 "buffer_load_dwordx4 %11, %6, %7 offen offset:3072 lds\n\t"
                 "buffer_load_dwordx4 %0, %14, %12, %13 offen\n\t"
                 "buffer_load_dwordx4 %1, %14, %12, %13 offen offset:1024\n\t"
                 "buffer_load_dwordx4 %2, %14, %12, %13 offen offset:2048\n\t"
                 "buffer_load_dwordx4 %3, %14, %12, %13 offen offset:3072\n\t"
                 "buffer_load_dwordx4 %4, %15, %12, %13 offen"
                 : "=&v"(a[0]), "=&v"(a[1]), "=&v"(a[2]), "=&v"(a[3]), "=&v"(a[4])
                 : "s"(m0v), "s"(sw), "s"(wo), "v"(wv0), "v"(wv1), "v"(wv2), "v"(wv3), "s"(sa), "s"(ao), "v"(av0), "v"(av1)
                 : "m0", "memory");
}
template <int N>
__device__ __forceinline__ void g_wait(u32x4 (&a)[5]) {
    asm volatile("s_waitcnt vmcnt(%5)" : "+v"(a[0]), "+v"(a[1]), "+v"(a[2]), "+v"(a[3]), "+v"(a[4]) : "n"(N) : "memory");
}

template <int MODE, int S, int NST>
struct GStep {
    template <class Issue>
    static __device__ __forceinline__ void run(u32x4 (&a)[G_NS][5], f32x4 (&acc)[4][5], const char* const (&tp)[4], Issue& issue) {
        constexpr int left = NST - 1 - S;
        g_wait<9 * (left < G_D - 1 ? left : G_D - 1)>(a[S % G_NS]);
        if (S == 0) STAMP(3 + MODE, 1);
        u32x4 wf[4];
#pragma unroll
        for (int i = 0; i < 4; ++i) {
            const int ii = (i + 1) & 3;
            const u32x2 lo = tr_read(tp[ii] + (S % G_NS) * 4096);
            const u32x2 hi = tr_read(tp[ii] + (S % G_NS) * 4096 + 512);
            wf[ii] = (u32x4){lo.x, lo.y, hi.x, hi.y};
        }
        __builtin_amdgcn_sched_barrier(0);
        if constexpr (S + G_D < NST) issue(a[(S + G_D) % G_NS], S + G_D);
        __builtin_amdgcn_sched_barrier(0);
#pragma unroll
        for (int i = 0; i < 4; ++i)
#pragma unroll
            for (int j = 0; j < 5; ++j) {
                const int ii = (i + 1) & 3;
                acc[ii][j] = MODE == 0 ? mfma16(wf[ii], a[S % G_NS][j], acc[ii][j]) : mfma16(a[S % G_NS][j], wf[ii], acc[ii][j]);
            }
        if constexpr (S + 1 < NST) {
            __builtin_amdgcn_sched_barrier(0);
            GStep<MODE, S + 1, NST>::run(a, acc, tp, issue);
        }
    }
};

template <int MODE>
__global__ __launch_bounds__(256) void k45_gemm(const u16* __restrict__ W, const char* __restrict__ ws,
                                                const u16* __restrict__ bias, u16* __restrict__ out,
                                                const u16* __restrict__ wn, const u16* __restrict__ bn) {
    constexpr int LDW = MODE == 0 ? F_ : L_;
    constexpr int NST = MODE == 0 ? 16 : 14;
    __shared__ __attribute__((aligned(16))) char smem[G_RED + 61440];
    const int tid = threadIdx.x, lane = tid & 63;
    const int w = __builtin_amdgcn_readfirstlane(tid >> 6);
    STAMP(3 + MODE, 0);
    int mt, dt;
    {
        const int xcd = blockIdx.x & 7, idx = blockIdx.x >> 3;
        constexpr int PER = MODE == 0 ? 27 : 32;
        if (MODE == 0 && idx >= 27) {
            warm_w<1>(wn, bn, xcd, (idx - 27) * 4 + w, 20, (unsigned)(size_t)(LDS_AS char*)(smem + w * 1024), lane);
            asm volatile("s_waitcnt vmcnt(0)" ::: "memory");
            STAMP(3, 5);
            return;
        }
        const int t = (xcd >> 1) * PER + idx;
        mt = t >> 2;
        dt = (xcd & 1) * 4 + (t & 3);
    }
    const int m0 = mt * 64;
    const int ks0 = w * NST;
    char* ring = smem + w * G_RING;
    const unsigned ring_a = (unsigned)(size_t)(LDS_AS char*)ring;
    const __amdgpu_buffer_rsrc_t srdW = __builtin_amdgcn_make_buffer_rsrc((void*)W, (short)0, L_ * F_ * 2, 0x00020000);
    const __amdgpu_buffer_rsrc_t srdA = __builtin_amdgcn_make_buffer_rsrc((void*)ws, (short)0, WS_END, 0x00020000);
    const bool idw = MODE == 1 && w == 3;
    const __amdgpu_buffer_rsrc_t srdL =
        __builtin_amdgcn_make_buffer_rsrc((void*)(idw ? ws + WS_ID : (const char*)W), (short)0, L_ * F_ * 2, 0x00020000);
    const int wr = lane >> 3, wslot = lane & 7;
    const unsigned wv0 = (wr * LDW + ((((wslot >> 1) ^ ((wr >> 1) & 1)) * 2 + (wslot & 1)) * 8)) * 2;
    const unsigned wv1 = ((wr + 8) * LDW + ((((wslot >> 1) ^ (((wr >> 1) & 1) | 2)) * 2 + (wslot & 1)) * 8)) * 2 - 1024;
    const unsigned wv2 = wv0 + 16 * LDW * 2 - 2048, wv3 = wv1 + 16 * LDW * 2 - 2048;
    const unsigned av0 = lane * 16, av1 = lane * 16 + 4096;
    const unsigned ws0 = (ks0 * 32 * LDW + m0) * 2;
    const unsigned as0 = (MODE == 0 ? WS_HN : WS_GB) + (ks0 * 40 + dt * 5) * 1024;
    const unsigned wsl = idw ? 0u : ws0 + (NST - 2) * (32 * LDW * 2);
    const unsigned asl = idw ? WS_HB + (mt * 2 * 40 + dt * 5) * 1024 : as0 + (NST - 2) * 40960;
    auto issue = [&](u32x4 (&a)[5], int s) {
        const unsigned dst = ring_a + (s % G_NS) * 4096;
        if (MODE == 1 && s >= NST - 2)
            g_issue(a, srdL, wsl + (s - (NST - 2)) * (32 * LDW * 2), dst, wv0, wv1, wv2, wv3, srdA, asl + (s - (NST - 2)) * 40960, av0, av1);
        else
            g_issue(a, srdW, ws0 + s * (32 * LDW * 2), dst, wv0, wv1, wv2, wv3, srdA, as0 + s * 40960, av0, av1);
    };
    const int n = lane & 15, hg = lane >> 4;
    u32x2 bb;
    {
        const u16* bp = bias + m0 + 16 * w + (MODE == 0 ? 4 * hg : (n & ~3));
        asm volatile("global_load_dwordx2 %0, %1, off" : "=v"(bb) : "v"(bp) : "memory");
    }
    u32x4 a[G_NS][5];
#pragma unroll
    for (int s = 0; s < G_D; ++s) issue(a[s], s);
    f32x4 acc[4][5];
#pragma unroll
    for (int c = 0; c < 4; ++c)
#pragma unroll
        for (int j = 0; j < 5; ++j) acc[c][j] = (f32x4){0.f, 0.f, 0.f, 0.f};
    const int q4 = n >> 2, p4 = n & 3;
    const int fsw = ((q4 >> 1) & 1) | ((hg & 1) << 1);
    const char* tb = ring + (8 * hg + q4) * 128 + p4 * 8;
    const char* const tp[4] = {tb + ((((0 + w) & 3) ^ fsw) * 32), tb + ((((1 + w) & 3) ^ fsw) * 32),
                               tb + ((((2 + w) & 3) ^ fsw) * 32), tb + ((((3 + w) & 3) ^ fsw) * 32)};
    asm volatile("s_waitcnt vmcnt(%1)" : "+v"(bb) : "n"(9 * G_D) : "memory");
    {
        f32x4 bs = {bflo(bb.x), bfhi(bb.x), bflo(bb.y), bfhi(bb.y)};
        if (MODE == 1) {
            const unsigned t = (n & 2) ? bb.y : bb.x;
            const float b = (n & 1) ? bfhi(t) : bflo(t);
            bs = (f32x4){b, b, b, b};
        }
#pragma unroll
        for (int j = 0; j < 5; ++j) acc[0][j] = bs;
    }
    GStep<MODE, 0, NST>::run(a, acc, tp, issue);
    STAMP(3 + MODE, 2);
    char* red = smem + G_RED + lane * 16;
#pragma unroll
    for (int i = 1; i < 4; ++i) {
        char* dst = red + (((i + w) & 3) * 3 + i - 1) * 5120;
#pragma unroll
        for (int j = 0; j < 5; ++j) *(f32x4*)(dst + j * 1024) = acc[i][j];
    }
    __syncthreads();
    f32x4 r[5];
    {
        const char* src = red + w * 15360;
        f32x4 p[5][3];
#pragma unroll
        for (int j = 0; j < 5; ++j)
#pragma unroll
            for (int o = 0; o < 3; ++o) p[j][o] = *(const f32x4*)(src + o * 5120 + j * 1024);
        __builtin_amdgcn_sched_barrier(0);
#pragma unroll
        for (int j = 0; j < 5; ++j) r[j] = (acc[0][j] + p[j][0]) + (p[j][1] + p[j][2]);
    }
    STAMP(3 + MODE, 3);
    if (MODE == 0) {
        char* gp = (char*)out + (((m0 >> 5) + (w >> 1)) * 40 + dt * 5) * 1024 + (((w & 1) * 2 + (hg >> 1)) * 16 + n) * 16 + (hg & 1) * 8;
#pragma unroll
        for (int j = 0; j < 5; ++j) {
            u32x2 o;
            o.x = pk2(gelu_tanh(r[j][0]), gelu_tanh(r[j][1]));
            o.y = pk2(gelu_tanh(r[j][2]), gelu_tanh(r[j][3]));
            *(u32x2*)(gp + j * 1024) = o;
        }
    } else {
        u16* op = out + (size_t)(m0 + 16 * w + n) * D_ + dt * 80 + 20 * hg;
        typedef unsigned u32x4u __attribute__((ext_vector_type(4), aligned(8)));
        u32x4u o0 = {pk2(r[0][0], r[0][1]), pk2(r[0][2], r[0][3]), pk2(r[1][0], r[1][1]), pk2(r[1][2], r[1][3])};
        u32x4u o1 = {pk2(r[2][0], r[2][1]), pk2(r[2][2], r[2][3]), pk2(r[3][0], r[3][1]), pk2(r[3][2], r[3][3])};
        u32x2 o2 = {pk2(r[4][0], r[4][1]), pk2(r[4][2], r[4][3])};
        *(u32x4u*)op = o0;
        *(u32x4u*)(op + 8) = o1;
        *(u32x2*)(op + 16) = o2;
    }
    STAMP(3 + MODE, 4);
}

extern "C" void kernel_launch(void* const* d_in, const int* in_sizes, int n_in, void* d_out, int out_size,
                              void* d_ws, size_t ws_size, hipStream_t stream) {
    const u16* x = (const u16*)d_in[0];
    const u16* logA = (const u16*)d_in[1];
    const u16* Aim = (const u16*)d_in[2];
    const u16* Cre = (const u16*)d_in[3];
    const u16* Cim = (const u16*)d_in[4];
    const u16* logdt = (const u16*)d_in[5];
    const u16* dskip = (const u16*)d_in[6];
    const u16* w1 = (const u16*)d_in[8];
    const u16* b1 = (const u16*)d_in[9];
    const u16* w2 = (const u16*)d_in[10];
    const u16* b2 = (const u16*)d_in[11];
    char* ws = (char*)d_ws;
    u16* hb = (u16*)(ws + WS_HB);
    u16* hn = (u16*)(ws + WS_HN);
    u16* gb = (u16*)(ws + WS_GB);
    ka_conv<<<256, 512, 0, stream>>>(x, logA, Aim, Cre, Cim, logdt, dskip, ws, w1, b1);
    k3_ln2<<<256, 640, 0, stream>>>(hb, hn);
    k45_gemm<0><<<256, 256, 0, stream>>>(w1, ws, b1, gb, w2, b2);
    k45_gemm<1><<<256, 256, 0, stream>>>(w2, ws, b2, (u16*)d_out, nullptr, nullptr);
}
```

```cpp
#include <hip/hip_runtime.h>
#include <hip/hip_bf16.h>
#include <stdint.h>
#pragma clang diagnostic ignored "-Winline-asm"

typedef unsigned short u16;
typedef __bf16 bf16x8 __attribute__((ext_vector_type(8)));
typedef __bf16 bf16x2 __attribute__((ext_vector_type(2)));
typedef short s16x4 __attribute__((ext_vector_type(4)));
typedef float f32x4 __attribute__((ext_vector_type(4)));
typedef float f32x2 __attribute__((ext_vector_type(2)));
typedef unsigned u32x4 __attribute__((ext_vector_type(4)));
typedef unsigned u32x2 __attribute__((ext_vector_type(2)));
#define LDS_AS __attribute__((address_space(3)))

#define STAMP(k, i) do { } while (0)
#define STAMPT(k, i, T) do { } while (0)

constexpr int L_ = 2048;
constexpr int D_ = 640;
constexpr int N_ = 128;
constexpr int F_ = 1728;
constexpr unsigned SZ_ = L_ * D_ * 2;
constexpr unsigned WS_HB = 0, WS_HN = SZ_, WS_GB = 2 * SZ_, WS_END = 3 * SZ_;
constexpr unsigned WS_ID = 3 * SZ_, WS_GR = WS_ID + 64 * L_ * 2, WS_EP = WS_GR + 32768;

__device__ __forceinline__ float bf2f(unsigned v) { return __uint_as_float(v << 16); }
__device__ __forceinline__ float bflo(unsigned v) { return __uint_as_float(v << 16); }
__device__ __forceinline__ float bfhi(unsigned v) { return __uint_as_float(v & 0xffff0000u); }
__device__ __forceinline__ unsigned pk2(float lo, float hi) {
    f32x2 f = {lo, hi};
    bf16x2 b = __builtin_convertvector(f, bf16x2);
    return __builtin_bit_cast(unsigned, b);
}
__device__ __forceinline__ float rb(float x) { return bflo(pk2(x, x)); }

__device__ __forceinline__ f32x4 mfma16(u32x4 a, u32x4 b, f32x4 c) {
    return __builtin_amdgcn_mfma_f32_16x16x32_bf16(__builtin_bit_cast(bf16x8, a), __builtin_bit_cast(bf16x8, b), c, 0, 0, 0);
}
__device__ __forceinline__ u32x2 tr_read(const char* p) {
    s16x4 v = __builtin_amdgcn_ds_read_tr16_b64_v4i16((LDS_AS s16x4*)p);
    return __builtin_bit_cast(u32x2, v);
}
template <int CTRL>
__device__ __forceinline__ float dpp_add(float v) {
    int t = __builtin_amdgcn_update_dpp(0, __float_as_int(v), CTRL, 0xf, 0xf, true);
    return v + __int_as_float(t);
}
__device__ __forceinline__ float row16_sum(float v) {
    v = dpp_add<0x128>(v);
    v = dpp_add<0x124>(v);
    v = dpp_add<0x122>(v);
    v = dpp_add<0x121>(v);
    return v;
}

template <int NT>
__device__ __forceinline__ void touch4(__amdgpu_buffer_rsrc_t srd, unsigned voff, unsigned soff, unsigned m0v) {
    if (NT) asm volatile("s_mov_b32 m0, %0\n\ts_nop 0\n\tbuffer_load_dword %1, %2, %3 offen nt lds"
                 :: "s"(m0v), "v"(voff), "s"(srd), "s"(soff) : "m0", "memory");
    else asm volatile("s_mov_b32 m0, %0\n\ts_nop 0\n\tbuffer_load_dword %1, %2, %3 offen lds"
                 :: "s"(m0v), "v"(voff), "s"(srd), "s"(soff) : "m0", "memory");
}
template <int MODE>
__device__ __forceinline__ void warm_w(const u16* W, const u16* bias, int xcd, int gw, int nw, unsigned lds, int lane) {
    constexpr int LDW = MODE == 0 ? F_ : L_;
    const __amdgpu_buffer_rsrc_t srdW = __builtin_amdgcn_make_buffer_rsrc((void*)W, (short)0, L_ * F_ * 2, 0x00020000);
    if (gw == 0) touch4<0>(__builtin_amdgcn_make_buffer_rsrc((void*)bias, (short)0, LDW * 2, 0x00020000), lane * 128, 0, lds);
    const int mt0 = MODE == 0 ? ((xcd >> 1) * 27) >> 2 : (xcd >> 1) * 8;
    const unsigned voff = ((lane >> 3) * LDW + (lane & 7) * 64) * 2;
    for (int j = gw; j < (MODE == 0 ? 256 : 224); j += nw) {
        const int s = j >> 4, w = (j >> 2) & 3;
        const int ks = MODE == 0 ? w * 16 + s : w * 14 + s;
        if (MODE == 1 && ks >= 54) continue;
        touch4<0>(srdW, voff, ((ks * 32 + (j & 3) * 8) * LDW + mt0 * 64) * 2, lds);
    }
}

constexpr int KA_ROWB = 528;
constexpr int KA_LO = 32 * KA_ROWB;
constexpr int KA_HI0 = 4 * KA_LO;
constexpr int KA_HI = 13312;
constexpr int KA_MB = 20000;
constexpr int CV_COPY = 4160;
constexpr int CV_UB = 4608;
constexpr int KA_U = KA_HI0 + 4 * KA_HI;
#define XC1 4
#define XCS 3
#define GFIRST " sc1"
typedef __attribute__((address_space(1))) unsigned gu32;

__device__ __forceinline__ void cm4(f32x2& r0, f32x2 a0, f32x2 b0, f32x2& r1, f32x2 a1, f32x2 b1,
                                    f32x2& r2, f32x2 a2, f32x2 b2, f32x2& r3, f32x2 a3, f32x2 b3) {
    asm("v_pk_mul_f32 %0, %4, %5 op_sel:[0,0] op_sel_hi:[1,0]\n\t"
        "v_pk_mul_f32 %1, %6, %7 op_sel:[0,0] op_sel_hi:[1,0]\n\t"
        "v_pk_mul_f32 %2, %8, %9 op_sel:[0,0] op_sel_hi:[1,0]\n\t"
        "v_pk_mul_f32 %3, %10, %11 op_sel:[0,0] op_sel_hi:[1,0]\n\t"
        "v_pk_fma_f32 %0, %4, %5, %0 op_sel:[1,1,0] op_sel_hi:[0,1,1] neg_lo:[1,0,0]\n\t"
        "v_pk_fma_f32 %1, %6, %7, %1 op_sel:[1,1,0] op_sel_hi:[0,1,1] neg_lo:[1,0,0]\n\t"
        "v_pk_fma_f32 %2, %8, %9, %2 op_sel:[1,1,0] op_sel_hi:[0,1,1] neg_lo:[1,0,0]\n\t"
        "v_pk_fma_f32 %3, %10, %11, %3 op_sel:[1,1,0] op_sel_hi:[0,1,1] neg_lo:[1,0,0]"
        : "=&v"(r0), "=&v"(r1), "=&v"(r2), "=&v"(r3)
        : "v"(a0), "v"(b0), "v"(a1), "v"(b1), "v"(a2), "v"(b2), "v"(a3), "v"(b3));
}
constexpr int ka_off(int r) { return r < 32 ? r * KA_ROWB : KA_HI0 - KA_MB + (r - 32) * KA_ROWB; }
template <int R0, int R1, int R2, int R3>
__device__ __forceinline__ void put4(const unsigned (&m0v)[2], f32x2 v0, f32x2 v1, f32x2 v2, f32x2 v3) {
    static_assert((R0 < 32) == (R1 < 32) && (R0 < 32) == (R2 < 32) && (R0 < 32) == (R3 < 32), "");
    unsigned t0, t1, t2, t3;
    asm volatile("s_mov_b32 m0, %4\n\t"
                 "v_cvt_pk_bf16_f32 %0, %5, %6\n\tv_cvt_pk_bf16_f32 %1, %7, %8\n\t"
                 "v_cvt_pk_bf16_f32 %2, %9, %10\n\tv_cvt_pk_bf16_f32 %3, %11, %12\n\t"
                 "ds_write_addtid_b32 %0 offset:%13\n\tds_write_addtid_b32 %1 offset:%14\n\t"
                 "ds_write_addtid_b32 %2 offset:%15\n\tds_write_addtid_b32 %3 offset:%16"
                 : "=&v"(t0), "=&v"(t1), "=&v"(t2), "=&v"(t3)
                 : "s"(m0v[R0 >= 32]), "v"(v0.x), "v"(v0.y), "v"(v1.x), "v"(v1.y), "v"(v2.x), "v"(v2.y), "v"(v3.x), "v"(v3.y),
                   "n"(ka_off(R0)), "n"(ka_off(R1)), "n"(ka_off(R2)), "n"(ka_off(R3))
                 : "m0", "memory");
}
__device__ __forceinline__ f32x2 cexp_p(float zr, float zi, float p) {
    const float mag = __builtin_amdgcn_exp2f(zr * p);
    const float rev = __builtin_amdgcn_fractf(zi * p);
    f32x2 r;
    r.x = mag * __builtin_amdgcn_cosf(rev);
    r.y = mag * __builtin_amdgcn_sinf(rev);
    return r;
}
__device__ __forceinline__ float wave_sum(float v) {
    v = row16_sum(v);
    const int i = __float_as_int(v);
    return (__int_as_float(__builtin_amdgcn_readlane(i, 0)) + __int_as_float(__builtin_amdgcn_readlane(i, 16))) +
           (__int_as_float(__builtin_amdgcn_readlane(i, 32)) + __int_as_float(__builtin_amdgcn_readlane(i, 48)));
}
__device__ __forceinline__ unsigned next_lane(unsigned v, unsigned last) {
    return (unsigned)__builtin_amdgcn_update_dpp((int)last, (int)v, 0x130, 0xf, 0xf, false);
}
__device__ __forceinline__ void put_copies(char* p, unsigned r0, unsigned r1, unsigned r2, unsigned r3, unsigned r4, unsigned r5) {
    const unsigned s0 = __builtin_amdgcn_alignbit(r1, r0, 16), s1 = __builtin_amdgcn_alignbit(r2, r1, 16),
                   s2 = __builtin_amdgcn_alignbit(r3, r2, 16), s3 = __builtin_amdgcn_alignbit(r4, r3, 16),
                   s4 = __builtin_amdgcn_alignbit(r5, r4, 16);
    *(u32x4*)(p + 0 * CV_COPY) = (u32x4){r0, r1, r2, r3};
    *(u32x4*)(p + 1 * CV_COPY) = (u32x4){s0, s1, s2, s3};
    *(u32x4*)(p + 2 * CV_COPY) = (u32x4){r1, r2, r3, r4};
    *(u32x4*)(p + 3 * CV_COPY) = (u32x4){s1, s2, s3, s4};
}
template <int O> __device__ __forceinline__ void rd64(u32x2& v, unsigned a) {
    asm volatile("ds_read_b64 %0, %1 offset:%2" : "=v"(v) : "v"(a), "n"(O) : "memory");
}
template <int O> __device__ __forceinline__ void rd128(u32x4& v, unsigned a) {
    asm volatile("ds_read_b128 %0, %1 offset:%2" : "=v"(v) : "v"(a), "n"(O) : "memory");
}
struct CvFrag { u32x2 lo[8], hi[8]; u32x4 b[8]; };
__device__ __forceinline__ void cv_wait(CvFrag& f) {
    asm volatile("s_waitcnt lgkmcnt(0)"
                 : "+v"(f.lo[0]), "+v"(f.lo[1]), "+v"(f.lo[2]), "+v"(f.lo[3]), "+v"(f.lo[4]), "+v"(f.lo[5]), "+v"(f.lo[6]), "+v"(f.lo[7]),
                   "+v"(f.hi[0]), "+v"(f.hi[1]), "+v"(f.hi[2]), "+v"(f.hi[3]), "+v"(f.hi[4]), "+v"(f.hi[5]), "+v"(f.hi[6]), "+v"(f.hi[7]),
                   "+v"(f.b[0]), "+v"(f.b[1]), "+v"(f.b[2]), "+v"(f.b[3]), "+v"(f.b[4]), "+v"(f.b[5]), "+v"(f.b[6]), "+v"(f.b[7])
                 :: "memory");
}
template <int T>
__device__ __forceinline__ void cv_mma(const CvFrag& f, f32x4 (&acc)[8]) {
    constexpr int q = T < 1 ? 0 : T < 3 ? 1 : T < 6 ? 2 : T < 10 ? 3 : T < 15 ? 4 : T < 21 ? 5 : T < 28 ? 6 : 7;
    constexpr int g1 = T - q * (q + 1) / 2;
    const u32x4 a = {f.lo[g1].x, f.lo[g1].y, f.hi[g1].x, f.hi[g1].y};
    acc[q] = mfma16(a, f.b[q - g1], acc[q]);
}
template <int G0, int R>
__device__ __forceinline__ void cv_read(CvFrag& f, unsigned aa, unsigned ba) {
    constexpr int G1 = R / 3;
    if constexpr (R % 3 == 0) rd64<(63 - 8 * G1 - G0) * 64>(f.lo[G1], aa);
    else if constexpr (R % 3 == 1) rd64<(63 - 8 * G1 - G0) * 64 + 8>(f.hi[G1], aa);
    else rd128<(8 * G1 - G0 + 7) * 64>(f.b[G1], ba);
}
template <int G0, int T>
__device__ __forceinline__ void cv_group(const CvFrag& cur, CvFrag& nxt, f32x4 (&acc)[8], unsigned aa, unsigned ba) {
    cv_mma<T>(cur, acc);
    __builtin_amdgcn_sched_barrier(0);
    if constexpr (G0 < 7 && T < 30 && T % 5 != 4) {
        cv_read<G0 + 1, T - T / 5>(nxt, aa, ba);
        __builtin_amdgcn_sched_barrier(0);
    }
    if constexpr (T < 35) cv_group<G0, T + 1>(cur, nxt, acc, aa, ba);
}
template <int G0>
__device__ __forceinline__ void cv_round(CvFrag& cur, CvFrag& nxt, f32x4 (&acc)[8], unsigned aa, unsigned ba) {
    cv_wait(cur);
    if constexpr (G0 == 5) __builtin_amdgcn_s_barrier();
    __builtin_amdgcn_sched_barrier(0);
    cv_group<G0, 0>(cur, nxt, acc, aa, ba);
    if constexpr (G0 < 7) cv_round<G0 + 1>(nxt, cur, acc, aa, ba);
}
template <int I>
__device__ __forceinline__ void cv_first(CvFrag& f, unsigned aa, unsigned ba) {
    cv_read<0, I>(f, aa, ba);
    if constexpr (I < 23) cv_first<I + 1>(f, aa, ba);
}

template <class Hook>
__device__ __forceinline__ void ka_chain(const unsigned (&m0v)[2], float p_la, float p_ai, float p_cr, float p_ci, float p_dt, Hook hook) {
    const float dt = rb(__builtin_amdgcn_exp2f(p_dt * 1.44269504088896341f));
    const float ar = -rb(__builtin_amdgcn_exp2f(p_la * 1.44269504088896341f));
    const float zr = ar * dt * 1.44269504088896341f, zi = p_ai * dt * 0.15915494309189535f;
    f32x2 a[17], b[16], e[8], F1, F2, F4, F8, r0, r1, r2, r3;
    a[0] = (f32x2){2.0f * (p_cr * dt), -2.0f * (p_ci * dt)};
    e[1] = cexp_p(zr, -zi, 1.f);
    b[1] = cexp_p(zr, zi, 8.f);
    F1 = cexp_p(zr, -zi, 128.f);
    cm4(e[2], e[1], e[1], b[2], b[1], b[1], F2, F1, F1, a[1], a[0], F1);
    cm4(e[4], e[2], e[2], b[4], b[2], b[2], F4, F2, F2, e[3], e[2], e[1]);
    put4<41, 42, 43, 45>(m0v, (f32x2){1.f, 0.f}, b[1], b[2], b[4]);
    hook(0);
    cm4(a[2], a[0], F2, a[3], a[1], F2, b[3], b[2], b[1], r0, a[0], e[1]);
    cm4(F8, F4, F4, b[8], b[4], b[4], r1, a[1], e[1], r2, a[0], e[2]);
    put4<16, 20, 1, 9>(m0v, a[2], a[3], r0, r1);
    hook(1);
    cm4(a[4], a[0], F4, a[5], a[1], F4, a[6], a[2], F4, a[7], a[3], F4);
    put4<24, 26, 28, 30>(m0v, a[4], a[5], a[6], a[7]);
    hook(2);
    cm4(e[5], e[4], e[1], e[6], e[4], e[2], e[7], e[4], e[3], r3, a[1], e[2]);
    put4<0, 8, 2, 10>(m0v, a[0], a[1], r2, r3);
    hook(3);
    cm4(b[5], b[4], b[1], b[6], b[4], b[2], b[7], b[4], b[3], r0, a[0], e[3]);
    put4<44, 49, 46, 47>(m0v, b[3], b[8], b[5], b[6]);
    hook(4);
    cm4(a[8], a[0], F8, a[9], a[1], F8, a[10], a[2], F8, a[11], a[3], F8);
    put4<32, 33, 34, 35>(m0v, a[8], a[9], a[10], a[11]);
    hook(5);
    cm4(a[12], a[4], F8, a[13], a[5], F8, a[14], a[6], F8, a[15], a[7], F8);
    put4<36, 37, 38, 39>(m0v, a[12], a[13], a[14], a[15]);
    hook(6);
    cm4(a[16], a[8], F8, r1, a[1], e[3], r2, a[0], e[4], r3, a[1], e[4]);
    put4<3, 11, 4, 12>(m0v, r0, r1, r2, r3);
    hook(7);
    cm4(b[9], b[8], b[1], b[10], b[8], b[2], b[11], b[8], b[3], b[12], b[8], b[4]);
    put4<50, 51, 52, 53>(m0v, b[9], b[10], b[11], b[12]);
    hook(8);
    cm4(b[13], b[8], b[5], b[14], b[8], b[6], b[15], b[8], b[7], r0, a[0], e[5]);
    put4<54, 55, 56, 48>(m0v, b[13], b[14], b[15], b[7]);
    asm volatile("ds_write_addtid_b32 %0 offset:%1" :: "v"(pk2(a[16].x, a[16].y)), "n"(ka_off(40)) : "memory");
    hook(9);
    cm4(r1, a[1], e[5], r2, a[0], e[6], r3, a[1], e[6], F1, a[0], e[7]);
    put4<5, 13, 6, 14>(m0v, r0, r1, r2, r3);
    hook(10);
    cm4(r0, a[1], e[7], r1, a[2], e[2], r2, a[3], e[2], F2, a[2], e[4]);
    put4<7, 15, 17, 21>(m0v, F1, r0, r1, r2);
    hook(11);
    cm4(r3, a[3], e[4], F1, a[2], e[6], r0, a[3], e[6], r1, a[4], e[4]);
    put4<18, 22, 19, 23>(m0v, F2, r3, F1, r0);
    hook(12);
    cm4(r2, a[5], e[4], r3, a[6], e[4], F1, a[7], e[4], F2, a[7], e[4]);
    put4<25, 27, 29, 31>(m0v, r1, r2, r3, F1);
    asm volatile("s_waitcnt lgkmcnt(0)" ::: "memory");
}
__device__ __forceinline__ void unpack4(float (&o)[4], u32x2 v) {
    asm volatile("v_lshlrev_b32 %0, 16, %4\n\tv_and_b32 %1, 0xffff0000, %4\n\tv_lshlrev_b32 %2, 16, %5\n\tv_and_b32 %3, 0xffff0000, %5"
                 : "=&v"(o[0]), "=&v"(o[1]), "=&v"(o[2]), "=&v"(o[3]) : "v"(v.x), "v"(v.y));
}

__global__ __launch_bounds__(512, 2) void ka_conv(const u16* __restrict__ x, const u16* __restrict__ logA,
                                                  const u16* __restrict__ Aim, const u16* __restrict__ Cre,
                                                  const u16* __restrict__ Cim, const u16* __restrict__ logdt,
                                                  const u16* __restrict__ dskip, char* ws,
                                                  const u16* __restrict__ w1, const u16* __restrict__ b1) {
    __shared__ __attribute__((aligned(16))) char smem[KA_U + 4 * CV_UB];
    const int tid = threadIdx.x, lane = tid & 63;
    const int w = __builtin_amdgcn_readfirstlane(tid >> 6);
    STAMP(0, 0);
    asm volatile("" :: "s"(x), "s"(logA), "s"(Aim), "s"(Cre), "s"(Cim), "s"(logdt), "s"(dskip), "s"(ws));
    const int xcd = blockIdx.x & 7, idx = blockIdx.x >> 3;
    const bool conv = idx < 20;
    const int grp = xcd * 20 + (conv ? idx : 0);
    const int d0 = grp * 4;
    const int row = (xcd * 32 + idx) * 8 + w;
    unsigned sv[5], epoch, pp[5];
    float pf[5];
    asm volatile("global_load_dword %0, %11, %12\n\tglobal_load_dword %1, %11, %12 offset:256\n\t"
                 "global_load_dword %2, %11, %12 offset:512\n\tglobal_load_dword %3, %11, %12 offset:768\n\t"
                 "global_load_dword %4, %11, %12 offset:1024\n\tglobal_load_dword %5, %13, %14\n\t"
                 "global_load_ushort %6, %15, %16\n\tglobal_load_ushort %7, %15, %17\n\t"
                 "global_load_ushort %8, %15, %18\n\tglobal_load_ushort %9, %15, %19\n\t"
                 "global_load_ushort %10, %13, %20"
                 : "=&v"(sv[0]), "=&v"(sv[1]), "=&v"(sv[2]), "=&v"(sv[3]), "=&v"(sv[4]), "=&v"(epoch),
                   "=&v"(pp[0]), "=&v"(pp[1]), "=&v"(pp[2]), "=&v"(pp[3]), "=&v"(pp[4])
                 : "v"(lane * 4), "s"(x + (size_t)row * D_), "v"(0), "s"(ws + WS_EP),
                   "v"((d0 * N_ + tid) * 2), "s"(logA), "s"(Aim), "s"(Cre), "s"(Cim), "s"(logdt + d0 + (w >> 1))
                 : "memory");
    const int t = tid & 255;
    const __amdgpu_buffer_rsrc_t srdG = __builtin_amdgcn_make_buffer_rsrc((void*)(ws + WS_GR), (short)0, L_ * 8, 0x00020000);
    const __amdgpu_buffer_rsrc_t srdX = __builtin_amdgcn_make_buffer_rsrc((void*)x, (short)0, SZ_, 0x00020000);
    const int owner = conv ? w >> 2 : 0;
    u32x2 xc[8], xd;
    asm volatile("s_barrier\n\ts_cmp_eq_u32 %5, 0\n\ts_cbranch_scc1 .Lka_a%=\n\t"
                 "global_load_dwordx2 %4, %6, %7\n\t"
                 "buffer_load_dwordx2 %0, %8, %9, %10 offen\n\tbuffer_load_dwordx2 %1, %8, %9, %10 offen offset:1280\n\t"
                 "buffer_load_dwordx2 %2, %8, %9, %10 offen offset:2560\n\tbuffer_load_dwordx2 %3, %8, %9, %10 offen offset:3840\n\t"
                 "s_waitcnt vmcnt(5)\n\ts_branch .Lka_b%=\n"
                 ".Lka_a%=:\n\ts_waitcnt vmcnt(0)\n"
                 ".Lka_b%=:"
                 : "=&v"(xc[0]), "=&v"(xc[1]), "=&v"(xc[2]), "=&v"(xc[3]), "=&v"(xd)
                 : "s"(owner), "v"(0), "s"(dskip + d0), "v"(t * (8 * D_ * 2)), "s"(srdX), "s"(d0 * 2) : "scc", "memory");
    unsigned etag;
    {
        float v[12];
        unpack4(*(float(*)[4])v, (u32x2){sv[0], sv[1]});
        unpack4(*(float(*)[4])(v + 4), (u32x2){sv[2], sv[3]});
        asm volatile("v_lshlrev_b32 %0, 16, %3\n\tv_and_b32 %1, 0xffff0000, %3\n\tv_xor_b32 %2, 0x9e3779b9, %4"
                     : "=&v"(v[8]), "=&v"(v[9]), "=&v"(etag) : "v"(sv[4]), "v"(epoch));
        const float s = ((v[0] + v[1]) + (v[2] + v[3])) + ((v[4] + v[5]) + (v[6] + v[7])) + (v[8] + v[9]);
        const float mean = wave_sum(s) * (1.0f / D_);
        float q0 = 0.f, q1 = 0.f;
#pragma unroll
        for (int j = 0; j < 5; ++j) {
            const float a = v[2 * j] - mean, b = v[2 * j + 1] - mean;
            q0 = __builtin_fmaf(a, a, q0);
            q1 = __builtin_fmaf(b, b, q1);
        }
        const float q = wave_sum(q0 + q1);
        const float den = __builtin_amdgcn_sqrtf(rb(rb(q * (1.0f / D_)) + 1e-5f));
        asm volatile("buffer_store_dwordx2 %10, %11, %12, %13 offen sc1\n\t"
                     "v_lshlrev_b32 %0, 16, %5\n\tv_lshlrev_b32 %1, 16, %6\n\tv_lshlrev_b32 %2, 16, %7\n\t"
                     "v_lshlrev_b32 %3, 16, %8\n\tv_lshlrev_b32 %4, 16, %9"
                     : "=&v"(pf[0]), "=&v"(pf[1]), "=&v"(pf[2]), "=&v"(pf[3]), "=&v"(pf[4])
                     : "v"(pp[0]), "v"(pp[1]), "v"(pp[2]), "v"(pp[3]), "v"(pp[4]),
                       "v"((u32x2){pk2(mean, den), etag * 0x85ebca6bu}), "v"(lane * (L_ * 8)), "s"(srdG), "s"(row * 8) : "memory");
    }
    STAMP(0, 1);
    STAMPT(5, 4, 448);
    if (!conv) {
        const unsigned lds = (unsigned)(size_t)(LDS_AS char*)(smem + w * 1024);
        warm_w<0>(w1, b1, xcd, (idx - 20) * 8 + w, 96, lds, lane);
        if (idx == 31 && tid < 256) {
            const int r = xcd * 8 + (tid >> 5), c = tid & 31;
            *(unsigned*)(ws + WS_ID + r * (L_ * 2) + 4 * c) = (r >> 1) == c ? ((r & 1) ? 0x3f800000u : 0x3f80u) : 0u;
        }
        asm volatile("s_waitcnt vmcnt(0)" ::: "memory");
        STAMP(0, 7);
        return;
    }
    const unsigned lds0 = (unsigned)(size_t)(LDS_AS char*)smem;
    const int m = lane & 15, hg = lane >> 4;
    char* mine = smem + (w & 3) * KA_LO;
    char* minh = smem + KA_HI0 + (w & 3) * KA_HI;
    const unsigned m0v[2] = {lds0 + (w >> 1) * KA_LO + (w & 1) * 256, lds0 + KA_MB + (w >> 1) * KA_HI + (w & 1) * 256};
    ka_chain(m0v, pf[0], pf[1], pf[2], pf[3], pf[4], [&](int i) {
        if (i % XCS == 0 && i / XCS < 4)
            asm volatile("s_cmp_eq_u32 %1, 0\n\ts_cbranch_scc1 .Lka_c%=\n\tbuffer_load_dwordx2 %0, %2, %3, %4 offen offset:%5\n.Lka_c%=:"
                         : "=v"(xc[4 + i / XCS])
                         : "s"(owner), "v"(t * (8 * D_ * 2)), "s"(srdX), "s"(d0 * 2 + 4 * D_ * 2), "n"((i / XCS) * (D_ * 2)) : "scc", "memory");
    });
    STAMP(0, 2);
    STAMPT(1, 0, 448);
    __builtin_amdgcn_s_barrier();
    if (w < 4) {
        STAMPT(1, 1, 0);
        u16* kd = (u16*)(minh + 8192);
        {
            const char* fb = mine + m * KA_ROWB + hg * 16;
            const char* fh = minh + m * KA_ROWB + hg * 16;
            u32x4 fa[8][3], fl[8];
#pragma unroll
            for (int ks = 0; ks < 8; ++ks) {
                fl[ks] = *(const u32x4*)(fh + 9 * KA_ROWB + ks * 64);
                fa[ks][0] = *(const u32x4*)(fb + ks * 64);
                fa[ks][1] = *(const u32x4*)(fb + 16 * KA_ROWB + ks * 64);
                fa[ks][2] = *(const u32x4*)(fh + ks * 64);
            }
            __builtin_amdgcn_sched_barrier(0);
            f32x4 acc0 = {0.f, 0.f, 0.f, 0.f}, acc1 = acc0, acc2 = acc0;
#pragma unroll
            for (int ks = 0; ks < 8; ++ks) {
                acc0 = mfma16(fa[ks][0], fl[ks], acc0);
                acc1 = mfma16(fa[ks][1], fl[ks], acc1);
                acc2 = mfma16(fa[ks][2], fl[ks], acc2);
            }
            if (acc0[0] == 1234.5f) STAMPT(1, 7, 0);
            if (acc1[0] == 1234.5f) STAMPT(1, 7, 0);
            if (acc2[0] == 1234.5f) STAMPT(1, 7, 0);
            STAMPT(1, 2, 0);
            const int c = m;
            *(u32x2*)(kd + 128 * (hg >> 1) + 4 * (hg & 1) + 8 * c) = (u32x2){pk2(acc0[0], acc0[1]), pk2(acc0[2], acc0[3])};
            const unsigned p01 = pk2(acc1[0], acc1[1]), p23 = pk2(acc1[2], acc1[3]);
            const int i0 = hg < 2 ? 256 + 64 * hg + 4 * c : 256 + 64 * hg + 2 * c;
            *(unsigned*)(kd + i0) = p01;
            *(unsigned*)(kd + i0 + (hg < 2 ? 2 : 32)) = p23;
            const unsigned q01 = pk2(acc2[0], acc2[1]), q23 = pk2(acc2[2], acc2[3]);
            kd[512 + 64 * hg + c] = (u16)(q01 & 0xffff);
            kd[528 + 64 * hg + c] = (u16)(q01 >> 16);
            kd[544 + 64 * hg + c] = (u16)(q23 & 0xffff);
            kd[560 + 64 * hg + c] = (u16)(q23 >> 16);
            asm volatile("" ::: "memory");
        }
        const unsigned* kw = (const unsigned*)kd;
        const unsigned dw0 = kw[319 - lane], dw1 = kw[320 - lane];
        const unsigned cw0 = kw[255 - lane], cw1 = kw[256 - lane];
        const bool isA = lane >= 32;
        const char* bp = (const char*)kd + (isA ? 16 * (63 - lane) : 256 + 8 * (63 - lane));
        const u32x2 b01 = *(const u32x2*)bp, b23 = *(const u32x2*)(bp + 8);
        if (dw0 + dw1 + cw0 + cw1 + b01.x + b23.x == 1234) STAMPT(1, 7, 0);
        STAMPT(1, 3, 0);
        const unsigned P = __builtin_amdgcn_perm(0, dw1, 0x01000100), Q = __builtin_amdgcn_perm(0, dw0, 0x03020302),
                       M = __builtin_amdgcn_perm(0, dw0, 0x01000302), Z = __builtin_amdgcn_perm(0, dw0, 0x01000100);
        const unsigned c0 = __builtin_amdgcn_perm(0, cw1, 0x01000100), c1 = __builtin_amdgcn_perm(0, cw0, 0x03020302),
                       c2 = __builtin_amdgcn_perm(0, cw0, 0x01000302), c3 = __builtin_amdgcn_perm(0, cw0, 0x01000100);
        const unsigned o0 = __builtin_amdgcn_perm(b23.x, isA ? b23.y : b01.y, isA ? 0x01000302u : 0x03020504u),
                       o1 = __builtin_amdgcn_perm(b23.x, b01.y, isA ? 0x05040706u : 0x01000100u),
                       o2 = __builtin_amdgcn_perm(b01.y, b01.x, isA ? 0x05040706u : 0x03020504u),
                       o3 = __builtin_amdgcn_perm(0, b01.x, isA ? 0x01000302u : 0x01000100u);
        put_copies(mine + 3072 + lane * 16, o0, o1, o2, o3, next_lane(o0, 0), next_lane(o1, 0));
        put_copies(mine + 2048 + lane * 16, c0, c1, c2, c3, next_lane(c0, __builtin_amdgcn_readlane(o0, 0)),
                   next_lane(c1, __builtin_amdgcn_readlane(o1, 0)));
        put_copies(mine + lane * 32, P, P, Q, Q, Q, M);
        put_copies(mine + lane * 32 + 16, Q, M, Z, Z, next_lane(P, __builtin_amdgcn_readlane(c0, 0)),
                   next_lane(P, __builtin_amdgcn_readlane(c1, 0)));
        if (lane < 8) *(u32x4*)(mine + (lane >> 1) * CV_COPY + 4096 + (lane & 1) * 16) = (u32x4){0u, 0u, 0u, 0u};
        STAMP(0, 3);
        __syncthreads();
        STAMP(0, 4);
        const unsigned aa = (unsigned)(size_t)(LDS_AS char*)(mine + (3 - (m & 3)) * CV_COPY + 2 * (2028 - 32 * 63 - 4 * (m >> 2) + 8 * hg));
        const unsigned ba = (unsigned)(size_t)(LDS_AS char*)(smem + KA_U + w * CV_UB + (2 * m + hg) * 16);
        f32x4 acc[8];
#pragma unroll
        for (int q = 0; q < 8; ++q) acc[q] = (f32x4){0.f, 0.f, 0.f, 0.f};
        CvFrag f0, f1;
        cv_first<0>(f0, aa, ba);
        cv_round<0>(f0, f1, acc, aa, ba);
        STAMP(0, 5);
        const char* rp = minh + (16 * m + 4 * hg) * 4;
        char* hp = mine + (16 * m + 4 * hg) * 2;
#pragma unroll
        for (int q = 0; q < 8; ++q) {
            const f32x4 h = acc[q] + *(const f32x4*)(rp + q * 1024);
            *(u32x2*)(hp + q * 512) = (u32x2){pk2(h[0], h[1]), pk2(h[2], h[3])};
        }
    } else {
        const unsigned tag = etag * 0x85ebca6bu;
        u32x4 g[4];
        asm volatile("buffer_load_dwordx4 %0, %4, %5, 0 offen" GFIRST "\n\tbuffer_load_dwordx4 %1, %4, %5, 0 offen offset:16" GFIRST "\n\t"
                     "buffer_load_dwordx4 %2, %4, %5, 0 offen offset:32" GFIRST "\n\tbuffer_load_dwordx4 %3, %4, %5, 0 offen offset:48" GFIRST "\n\t"
                     "s_waitcnt vmcnt(4)"
                     : "=&v"(g[0]), "=&v"(g[1]), "=&v"(g[2]), "=&v"(g[3]) : "v"(t * 64), "s"(srdG) : "memory");
        float xf[8][4], dk[4];
#pragma unroll
        for (int k = 0; k < 8; ++k) unpack4(xf[k], xc[k]);
        unpack4(dk, xd);
        if (lane < 30) *(u32x4*)(smem + KA_U + (w - 4) * CV_UB + lane * 16) = (u32x4){0u, 0u, 0u, 0u};
        STAMPT(1, 4, 256);
        asm volatile("s_waitcnt vmcnt(0)" : "+v"(g[0]), "+v"(g[1]), "+v"(g[2]), "+v"(g[3]) :: "memory");
        unsigned spins = 0;
#pragma unroll 1
        for (; spins < (1u << 16); ++spins) {
            const bool bad = ((g[0].y ^ tag) | (g[0].w ^ tag) | (g[1].y ^ tag) | (g[1].w ^ tag) |
                              (g[2].y ^ tag) | (g[2].w ^ tag) | (g[3].y ^ tag) | (g[3].w ^ tag)) != 0;
            if (!__any(bad)) break;
            if (bad)
                asm volatile("buffer_load_dwordx4 %0, %4, %5, 0 offen sc1\n\tbuffer_load_dwordx4 %1, %4, %5, 0 offen offset:16 sc1\n\t"
                             "buffer_load_dwordx4 %2, %4, %5, 0 offen offset:32 sc1\n\tbuffer_load_dwordx4 %3, %4, %5, 0 offen offset:48 sc1\n\t"
                             "s_waitcnt vmcnt(0)"
                             : "+v"(g[0]), "+v"(g[1]), "+v"(g[2]), "+v"(g[3]) : "v"(t * 64), "s"(srdG) : "memory");
        }
        STAMPT(1, 5, 256);
        unsigned up[4][4];
#pragma unroll
        for (int k = 0; k < 4; ++k) {
            const float i0 = __builtin_amdgcn_rcpf(bfhi(g[k].x)), i1 = __builtin_amdgcn_rcpf(bfhi(g[k].z));
            const float m0 = bflo(g[k].x), m1 = bflo(g[k].z);
#pragma unroll
            for (int c = 0; c < 4; ++c) up[c][k] = pk2((xf[2 * k][c] - m0) * i0, (xf[2 * k + 1][c] - m1) * i1);
        }
#pragma unroll
        for (int c = 0; c < 4; ++c)
            *(u32x4*)(smem + KA_U + c * CV_UB + 480 + t * 16) = (u32x4){up[c][0], up[c][1], up[c][2], up[c][3]};
        STAMPT(1, 6, 256);
        __syncthreads();
        if (blockIdx.x == 0 && tid == 256)
            __hip_atomic_store((gu32*)(ws + WS_EP), (etag ^ 0x9e3779b9u) + 1u, __ATOMIC_RELAXED, __HIP_MEMORY_SCOPE_AGENT);
#pragma unroll
        for (int c = 0; c < 4; ++c) {
            f32x4 r0, r1;
#pragma unroll
            for (int k = 0; k < 2; ++k) {
                r0[2 * k] = __builtin_fmaf(bflo(up[c][k]), dk[c], xf[2 * k][c]);
                r0[2 * k + 1] = __builtin_fmaf(bfhi(up[c][k]), dk[c], xf[2 * k + 1][c]);
                r1[2 * k] = __builtin_fmaf(bflo(up[c][2 + k]), dk[c], xf[4 + 2 * k][c]);
                r1[2 * k + 1] = __builtin_fmaf(bfhi(up[c][2 + k]), dk[c], xf[5 + 2 * k][c]);
            }
            *(f32x4*)(smem + KA_HI0 + c * KA_HI + t * 32) = r0;
            *(f32x4*)(smem + KA_HI0 + c * KA_HI + t * 32 + 16) = r1;
        }
        asm volatile("s_waitcnt lgkmcnt(0)" ::: "memory");
        __builtin_amdgcn_s_barrier();
    }
    __syncthreads();
    {
        const int ch = tid & 3;
        const int dt = grp / 20, rem = grp % 20, mq = rem / 5, j = rem % 5;
#pragma unroll
        for (int i = 0; i < 2; ++i) {
            const int piece = (tid >> 2) + 128 * i;
            const int unit = ((piece >> 2) * 40 + dt * 5 + j) * 64 + (piece & 3) * 16 + 4 * mq + ch;
            *(u32x4*)(ws + WS_HB + (size_t)unit * 16) = *(const u32x4*)(smem + ch * KA_LO + piece * 16);
        }
    }
    STAMP(0, 6);
}

__device__ __forceinline__ float xor_lane(float v, int mask) { return __shfl_xor(v, mask, 64); }

__device__ __forceinline__ void block_stats(const float (&v)[8], float* red, int tid, float (&mb)[8], float (&inv)[8]) {
    const int lane = tid & 63, w = tid >> 6;
    float a[16];
#pragma unroll
    for (int e = 0; e < 8; ++e) { a[e] = v[e]; a[8 + e] = v[e] * v[e]; }
#pragma unroll
    for (int e = 0; e < 8; ++e) {
        const bool up = lane & 1;
        const float keep = up ? a[8 + e] : a[e], send = up ? a[e] : a[8 + e];
        a[e] = keep + xor_lane(send, 1);
    }
#pragma unroll
    for (int e = 0; e < 4; ++e) {
        const bool up = lane & 2;
        const float keep = up ? a[4 + e] : a[e], send = up ? a[e] : a[4 + e];
        a[e] = keep + xor_lane(send, 2);
    }
#pragma unroll
    for (int e = 0; e < 2; ++e) {
        const bool up = lane & 4;
        const float keep = up ? a[2 + e] : a[e], send = up ? a[e] : a[2 + e];
        a[e] = keep + xor_lane(send, 4);
    }
    {
        const bool up = lane & 8;
        const float keep = up ? a[1] : a[0], send = up ? a[0] : a[1];
        a[0] = keep + xor_lane(send, 8);
    }
    a[0] += xor_lane(a[0], 16);
    a[0] += xor_lane(a[0], 32);
    if (lane < 16) {
        const int row = ((lane >> 1) & 1) * 4 + ((lane >> 2) & 1) * 2 + ((lane >> 3) & 1);
        red[w * 16 + (lane & 1) * 8 + row] = a[0];
    }
    __syncthreads();
    if (tid < 8) {
        float s = 0.f, q = 0.f;
#pragma unroll
        for (int k = 0; k < 10; ++k) { s += red[k * 16 + tid]; q += red[k * 16 + 8 + tid]; }
        const float mean = s * (1.0f / D_);
        const float var = fmaxf(q * (1.0f / D_) - mean * mean, 0.f);
        const float den = rb(__builtin_sqrtf(rb(rb(var) + 1e-5f)));
        red[160 + tid] = rb(mean);
        red[168 + tid] = 1.0f / den;
    }
    __syncthreads();
#pragma unroll
    for (int e = 0; e < 8; ++e) { mb[e] = red[160 + e]; inv[e] = red[168 + e]; }
}

__global__ __launch_bounds__(640) void k3_ln2(const u16* __restrict__ hb, u16* __restrict__ hn) {
    __shared__ __attribute__((aligned(16))) float red[2][176];
    const int tid = threadIdx.x;
    STAMP(2, 0);
    const size_t g = ((size_t)((blockIdx.x >> 2) * 40 + (tid >> 4)) * 64 + (blockIdx.x & 3) * 16 + (tid & 15)) * 8;
    const u32x4 p = *(const u32x4*)(hb + g);
    float v[8], mb[8], inv[8];
#pragma unroll
    for (int e = 0; e < 4; ++e) { v[2 * e] = bflo(p[e]); v[2 * e + 1] = bfhi(p[e]); }
    if (v[0] == 12345.f) STAMP(2, 7);
    STAMP(2, 1);
    block_stats(v, red[0], tid, mb, inv);
    STAMP(2, 2);
#pragma unroll
    for (int e = 0; e < 4; ++e) {
        const unsigned t = pk2(v[2 * e] - mb[2 * e], v[2 * e + 1] - mb[2 * e + 1]);
        const unsigned o = pk2(bflo(t) * inv[2 * e], bfhi(t) * inv[2 * e + 1]);
        v[2 * e] = bflo(o);
        v[2 * e + 1] = bfhi(o);
    }
    block_stats(v, red[1], tid, mb, inv);
    u32x4 o;
#pragma unroll
    for (int e = 0; e < 4; ++e) {
        const unsigned t = pk2(v[2 * e] - mb[2 * e], v[2 * e + 1] - mb[2 * e + 1]);
        o[e] = pk2(bflo(t) * inv[2 * e], bfhi(t) * inv[2 * e + 1]);
    }
    *(u32x4*)(hn + g) = o;
    STAMP(2, 3);
}

#define GDEPTH 3
constexpr int G_D = GDEPTH;
constexpr int G_NS = G_D + 1;
constexpr int G_RING = G_NS * 4096;
constexpr int G_RED = 4 * G_RING;

__device__ __forceinline__ float gelu_tanh(float x) {
    const float p = __builtin_fmaf(x * x, -2.302208198f * 0.044715f, -2.302208198f);
    const float e = __builtin_amdgcn_exp2f(p * x);
    return x * __builtin_amdgcn_rcpf(1.0f + e);
}
__device__ __forceinline__ void g_issue(u32x4 (&a)[5], __amdgpu_buffer_rsrc_t sw, unsigned wo, unsigned m0v,
                                        unsigned wv0, unsigned wv1, unsigned wv2, unsigned wv3,
                                        __amdgpu_buffer_rsrc_t sa, unsigned ao, unsigned av0, unsigned av1) {
    asm volatile("s_mov_b32 m0, %5\n\ts_nop 0\n\t"
                 "buffer_load_dwordx4 %8, %6, %7 offen lds\n\t"
                 "buffer_load_dwordx4 %9, %6, %7 offen offset:1024 lds\n\t"
                 "buffer_load_dwordx4 %10, %6, %7 offen offset:2048 lds\n\t"
                 "buffer_load_dwordx4 %11, %6, %7 offen offset:3072 lds\n\t"
                 "buffer_load_dwordx4 %0, %14, %12, %13 offen\n\t"
                 "buffer_load_dwordx4 %1, %14, %12, %13 offen offset:1024\n\t"
                 "buffer_load_dwordx4 %2, %14, %12, %13 offen offset:2048\n\t"
                 "buffer_load_dwordx4 %3, %14, %12, %13 offen offset:3072\n\t"
                 "buffer_load_dwordx4 %4, %15, %12, %13 offen"
                 : "=&v"(a[0]), "=&v"(a[1]), "=&v"(a[2]), "=&v"(a[3]), "=&v"(a[4])
                 : "s"(m0v), "s"(sw), "s"(wo), "v"(wv0), "v"(wv1), "v"(wv2), "v"(wv3), "s"(sa), "s"(ao), "v"(av0), "v"(av1)
                 : "m0", "memory");
}
template <int N>
__device__ __forceinline__ void g_wait(u32x4 (&a)[5]) {
    asm volatile("s_waitcnt vmcnt(%5)" : "+v"(a[0]), "+v"(a[1]), "+v"(a[2]), "+v"(a[3]), "+v"(a[4]) : "n"(N) : "memory");
}

template <int MODE, int S, int NST>
struct GStep {
    template <class Issue>
    static __device__ __forceinline__ void run(u32x4 (&a)[G_NS][5], f32x4 (&acc)[4][5], const char* const (&tp)[4], Issue& issue) {
        constexpr int left = NST - 1 - S;
        g_wait<9 * (left < G_D - 1 ? left : G_D - 1)>(a[S % G_NS]);
        if (S == 0) STAMP(3 + MODE, 1);
        u32x4 wf[4];
#pragma unroll
        for (int i = 0; i < 4; ++i) {
            const int ii = (i + 1) & 3;
            const u32x2 lo = tr_read(tp[ii] + (S % G_NS) * 4096);
            const u32x2 hi = tr_read(tp[ii] + (S % G_NS) * 4096 + 512);
            wf[ii] = (u32x4){lo.x, lo.y, hi.x, hi.y};
        }
        __builtin_amdgcn_sched_barrier(0);
        if constexpr (S + G_D < NST) issue(a[(S + G_D) % G_NS], S + G_D);
        __builtin_amdgcn_sched_barrier(0);
#pragma unroll
        for (int i = 0; i < 4; ++i)
#pragma unroll
            for (int j = 0; j < 5; ++j) {
                const int ii = (i + 1) & 3;
                acc[ii][j] = MODE == 0 ? mfma16(wf[ii], a[S % G_NS][j], acc[ii][j]) : mfma16(a[S % G_NS][j], wf[ii], acc[ii][j]);
            }
        if constexpr (S + 1 < NST) {
            __builtin_amdgcn_sched_barrier(0);
            GStep<MODE, S + 1, NST>::run(a, acc, tp, issue);
        }
    }
};

template <int MODE>
__global__ __launch_bounds__(256) void k45_gemm(const u16* __restrict__ W, const char* __restrict__ ws,
                                                const u16* __restrict__ bias, u16* __restrict__ out,
                                                const u16* __restrict__ wn, const u16* __restrict__ bn) {
    constexpr int LDW = MODE == 0 ? F_ : L_;
    constexpr int NST = MODE == 0 ? 16 : 14;
    __shared__ __attribute__((aligned(16))) char smem[G_RED + 61440];
    const int tid = threadIdx.x, lane = tid & 63;
    const int w = __builtin_amdgcn_readfirstlane(tid >> 6);
    STAMP(3 + MODE, 0);
    int mt, dt;
    {
        const int xcd = blockIdx.x & 7, idx = blockIdx.x >> 3;
        constexpr int PER = MODE == 0 ? 27 : 32;
        if (MODE == 0 && idx >= 27) {
            warm_w<1>(wn, bn, xcd, (idx - 27) * 4 + w, 20, (unsigned)(size_t)(LDS_AS char*)(smem + w * 1024), lane);
            asm volatile("s_waitcnt vmcnt(0)" ::: "memory");
            STAMP(3, 5);
            return;
        }
        const int t = (xcd >> 1) * PER + idx;
        mt = t >> 2;
        dt = (xcd & 1) * 4 + (t & 3);
    }
    const int m0 = mt * 64;
    const int ks0 = w * NST;
    char* ring = smem + w * G_RING;
    const unsigned ring_a = (unsigned)(size_t)(LDS_AS char*)ring;
    const __amdgpu_buffer_rsrc_t srdW = __builtin_amdgcn_make_buffer_rsrc((void*)W, (short)0, L_ * F_ * 2, 0x00020000);
    const __amdgpu_buffer_rsrc_t srdA = __builtin_amdgcn_make_buffer_rsrc((void*)ws, (short)0, WS_END, 0x00020000);
    const bool idw = MODE == 1 && w == 3;
    const __amdgpu_buffer_rsrc_t srdL =
        __builtin_amdgcn_make_buffer_rsrc((void*)(idw ? ws + WS_ID : (const char*)W), (short)0, L_ * F_ * 2, 0x00020000);
    const int wr = lane >> 3, wslot = lane & 7;
    const unsigned wv0 = (wr * LDW + ((((wslot >> 1) ^ ((wr >> 1) & 1)) * 2 + (wslot & 1)) * 8)) * 2;
    const unsigned wv1 = ((wr + 8) * LDW + ((((wslot >> 1) ^ (((wr >> 1) & 1) | 2)) * 2 + (wslot & 1)) * 8)) * 2 - 1024;
    const unsigned wv2 = wv0 + 16 * LDW * 2 - 2048, wv3 = wv1 + 16 * LDW * 2 - 2048;
    const unsigned av0 = lane * 16, av1 = lane * 16 + 4096;
    const unsigned ws0 = (ks0 * 32 * LDW + m0) * 2;
    const unsigned as0 = (MODE == 0 ? WS_HN : WS_GB) + (ks0 * 40 + dt * 5) * 1024;
    const unsigned wsl = idw ? 0u : ws0 + (NST - 2) * (32 * LDW * 2);
    const unsigned asl = idw ? WS_HB + (mt * 2 * 40 + dt * 5) * 1024 : as0 + (NST - 2) * 40960;
    auto issue = [&](u32x4 (&a)[5], int s) {
        const unsigned dst = ring_a + (s % G_NS) * 4096;
        if (MODE == 1 && s >= NST - 2)
            g_issue(a, srdL, wsl + (s - (NST - 2)) * (32 * LDW * 2), dst, wv0, wv1, wv2, wv3, srdA, asl + (s - (NST - 2)) * 40960, av0, av1);
        else
            g_issue(a, srdW, ws0 + s * (32 * LDW * 2), dst, wv0, wv1, wv2, wv3, srdA, as0 + s * 40960, av0, av1);
    };
    const int n = lane & 15, hg = lane >> 4;
    u32x2 bb;
    {
        const u16* bp = bias + m0 + 16 * w + (MODE == 0 ? 4 * hg : (n & ~3));
        asm volatile("global_load_dwordx2 %0, %1, off" : "=v"(bb) : "v"(bp) : "memory");
    }
    u32x4 a[G_NS][5];
#pragma unroll
    for (int s = 0; s < G_D; ++s) issue(a[s], s);
    f32x4 acc[4][5];
#pragma unroll
    for (int c = 0; c < 4; ++c)
#pragma unroll
        for (int j = 0; j < 5; ++j) acc[c][j] = (f32x4){0.f, 0.f, 0.f, 0.f};
    const int q4 = n >> 2, p4 = n & 3;
    const int fsw = ((q4 >> 1) & 1) | ((hg & 1) << 1);
    const char* tb = ring + (8 * hg + q4) * 128 + p4 * 8;
    const char* const tp[4] = {tb + ((((0 + w) & 3) ^ fsw) * 32), tb + ((((1 + w) & 3) ^ fsw) * 32),
                               tb + ((((2 + w) & 3) ^ fsw) * 32), tb + ((((3 + w) & 3) ^ fsw) * 32)};
    asm volatile("s_waitcnt vmcnt(%1)" : "+v"(bb) : "n"(9 * G_D) : "memory");
    {
        f32x4 bs = {bflo(bb.x), bfhi(bb.x), bflo(bb.y), bfhi(bb.y)};
        if (MODE == 1) {
            const unsigned t = (n & 2) ? bb.y : bb.x;
            const float b = (n & 1) ? bfhi(t) : bflo(t);
            bs = (f32x4){b, b, b, b};
        }
#pragma unroll
        for (int j = 0; j < 5; ++j) acc[0][j] = bs;
    }
    GStep<MODE, 0, NST>::run(a, acc, tp, issue);
    STAMP(3 + MODE, 2);
    char* red = smem + G_RED + lane * 16;
#pragma unroll
    for (int i = 1; i < 4; ++i) {
        char* dst = red + (((i + w) & 3) * 3 + i - 1) * 5120;
#pragma unroll
        for (int j = 0; j < 5; ++j) *(f32x4*)(dst + j * 1024) = acc[i][j];
    }
    __syncthreads();
    f32x4 r[5];
    {
        const char* src = red + w * 15360;
        f32x4 p[5][3];
#pragma unroll
        for (int j = 0; j < 5; ++j)
#pragma unroll
            for (int o = 0; o < 3; ++o) p[j][o] = *(const f32x4*)(src + o * 5120 + j * 1024);
        __builtin_amdgcn_sched_barrier(0);
#pragma unroll
        for (int j = 0; j < 5; ++j) r[j] = (acc[0][j] + p[j][0]) + (p[j][1] + p[j][2]);
    }
    STAMP(3 + MODE, 3);
    if (MODE == 0) {
        char* gp = (char*)out + (((m0 >> 5) + (w >> 1)) * 40 + dt * 5) * 1024 + (((w & 1) * 2 + (hg >> 1)) * 16 + n) * 16 + (hg & 1) * 8;
#pragma unroll
        for (int j = 0; j < 5; ++j) {
            u32x2 o;
            o.x = pk2(gelu_tanh(r[j][0]), gelu_tanh(r[j][1]));
            o.y = pk2(gelu_tanh(r[j][2]), gelu_tanh(r[j][3]));
            *(u32x2*)(gp + j * 1024) = o;
        }
    } else {
        u16* op = out + (size_t)(m0 + 16 * w + n) * D_ + dt * 80 + 20 * hg;
        typedef unsigned u32x4u __attribute__((ext_vector_type(4), aligned(8)));
        u32x4u o0 = {pk2(r[0][0], r[0][1]), pk2(r[0][2], r[0][3]), pk2(r[1][0], r[1][1]), pk2(r[1][2], r[1][3])};
        u32x4u o1 = {pk2(r[2][0], r[2][1]), pk2(r[2][2], r[2][3]), pk2(r[3][0], r[3][1]), pk2(r[3][2], r[3][3])};
        u32x2 o2 = {pk2(r[4][0], r[4][1]), pk2(r[4][2], r[4][3])};
        *(u32x4u*)op = o0;
        *(u32x4u*)(op + 8) = o1;
        *(u32x2*)(op + 16) = o2;
    }
    STAMP(3 + MODE, 4);
}

extern "C" void kernel_launch(void* const* d_in, const int* in_sizes, int n_in, void* d_out, int out_size,
                              void* d_ws, size_t ws_size, hipStream_t stream) {
    const u16* x = (const u16*)d_in[0];
    const u16* logA = (const u16*)d_in[1];
    const u16* Aim = (const u16*)d_in[2];
    const u16* Cre = (const u16*)d_in[3];
    const u16* Cim = (const u16*)d_in[4];
    const u16* logdt = (const u16*)d_in[5];
    const u16* dskip = (const u16*)d_in[6];
    const u16* w1 = (const u16*)d_in[8];
    const u16* b1 = (const u16*)d_in[9];
    const u16* w2 = (const u16*)d_in[10];
    const u16* b2 = (const u16*)d_in[11];
    char* ws = (char*)d_ws;
    u16* hb = (u16*)(ws + WS_HB);
    u16* hn = (u16*)(ws + WS_HN);
    u16* gb = (u16*)(ws + WS_GB);
    ka_conv<<<256, 512, 0, stream>>>(x, logA, Aim, Cre, Cim, logdt, dskip, ws, w1, b1);
    k3_ln2<<<256, 640, 0, stream>>>(hb, hn);
    k45_gemm<0><<<256, 256, 0, stream>>>(w1, ws, b1, gb, w2, b2);
    k45_gemm<1><<<256, 256, 0, stream>>>(w2, ws, b2, (u16*)d_out, nullptr, nullptr);
}
```

```cpp
#include <hip/hip_runtime.h>
#include <hip/hip_bf16.h>
#include <stdint.h>
#pragma clang diagnostic ignored "-Winline-asm"

typedef unsigned short u16;
typedef __bf16 bf16x8 __attribute__((ext_vector_type(8)));
typedef __bf16 bf16x2 __attribute__((ext_vector_type(2)));
typedef short s16x4 __attribute__((ext_vector_type(4)));
typedef float f32x4 __attribute__((ext_vector_type(4)));
typedef float f32x2 __attribute__((ext_vector_type(2)));
typedef unsigned u32x4 __attribute__((ext_vector_type(4)));
typedef unsigned u32x2 __attribute__((ext_vector_type(2)));
#define LDS_AS __attribute__((address_space(3)))

#define STAMP(k, i) do { } while (0)
#define STAMPT(k, i, T) do { } while (0)

constexpr int L_ = 2048;
constexpr int D_ = 640;
constexpr int N_ = 128;
constexpr int F_ = 1728;
constexpr unsigned SZ_ = L_ * D_ * 2;
constexpr unsigned WS_HB = 0, WS_GB = SZ_, WS_END = 2 * SZ_;
constexpr unsigned WS_ID = 2 * SZ_, WS_GR = WS_ID + 64 * L_ * 2, WS_EP = WS_GR + 32768;
constexpr unsigned WS_G2 = WS_EP + 4096, WS_FL = WS_G2 + L_ * 8;

__device__ __forceinline__ float bf2f(unsigned v) { return __uint_as_float(v << 16); }
__device__ __forceinline__ float bflo(unsigned v) { return __uint_as_float(v << 16); }
__device__ __forceinline__ float bfhi(unsigned v) { return __uint_as_float(v & 0xffff0000u); }
__device__ __forceinline__ unsigned pk2(float lo, float hi) {
    f32x2 f = {lo, hi};
    bf16x2 b = __builtin_convertvector(f, bf16x2);
    return __builtin_bit_cast(unsigned, b);
}
__device__ __forceinline__ float rb(float x) { return bflo(pk2(x, x)); }

__device__ __forceinline__ f32x4 mfma16(u32x4 a, u32x4 b, f32x4 c) {
    return __builtin_amdgcn_mfma_f32_16x16x32_bf16(__builtin_bit_cast(bf16x8, a), __builtin_bit_cast(bf16x8, b), c, 0, 0, 0);
}
__device__ __forceinline__ u32x2 tr_read(const char* p) {
    s16x4 v = __builtin_amdgcn_ds_read_tr16_b64_v4i16((LDS_AS s16x4*)p);
    return __builtin_bit_cast(u32x2, v);
}
template <int CTRL>
__device__ __forceinline__ float dpp_add(float v) {
    int t = __builtin_amdgcn_update_dpp(0, __float_as_int(v), CTRL, 0xf, 0xf, true);
    return v + __int_as_float(t);
}
__device__ __forceinline__ float row16_sum(float v) {
    v = dpp_add<0x128>(v);
    v = dpp_add<0x124>(v);
    v = dpp_add<0x122>(v);
    v = dpp_add<0x121>(v);
    return v;
}

template <int NT>
__device__ __forceinline__ void touch4(__amdgpu_buffer_rsrc_t srd, unsigned voff, unsigned soff, unsigned m0v) {
    if (NT) asm volatile("s_mov_b32 m0, %0\n\ts_nop 0\n\tbuffer_load_dword %1, %2, %3 offen nt lds"
                 :: "s"(m0v), "v"(voff), "s"(srd), "s"(soff) : "m0", "memory");
    else asm volatile("s_mov_b32 m0, %0\n\ts_nop 0\n\tbuffer_load_dword %1, %2, %3 offen lds"
                 :: "s"(m0v), "v"(voff), "s"(srd), "s"(soff) : "m0", "memory");
}
template <int MODE>
__device__ __forceinline__ void warm_w(const u16* W, const u16* bias, int xcd, int gw, int nw, unsigned lds, int lane) {
    constexpr int LDW = MODE == 0 ? F_ : L_;
    const __amdgpu_buffer_rsrc_t srdW = __builtin_amdgcn_make_buffer_rsrc((void*)W, (short)0, L_ * F_ * 2, 0x00020000);
    if (gw == 0) touch4<0>(__builtin_amdgcn_make_buffer_rsrc((void*)bias, (short)0, LDW * 2, 0x00020000), lane * 128, 0, lds);
    const int mt0 = MODE == 0 ? ((xcd >> 1) * 27) >> 2 : (xcd >> 1) * 8;
    const unsigned voff = ((lane >> 3) * LDW + (lane & 7) * 64) * 2;
    for (int j = gw; j < (MODE == 0 ? 256 : 224); j += nw) {
        const int s = j >> 4, w = (j >> 2) & 3;
        const int ks = MODE == 0 ? w * 16 + s : w * 14 + s;
        if (MODE == 1 && ks >= 54) continue;
        touch4<0>(srdW, voff, ((ks * 32 + (j & 3) * 8) * LDW + mt0 * 64) * 2, lds);
    }
}

constexpr int KA_ROWB = 528;
constexpr int KA_LO = 32 * KA_ROWB;
constexpr int KA_HI0 = 4 * KA_LO;
constexpr int KA_HI = 13312;
constexpr int KA_MB = 20000;
constexpr int CV_COPY = 4160;
constexpr int CV_UB = 4608;
constexpr int KA_U = KA_HI0 + 4 * KA_HI;
#define XC1 4
#define XCS 3
#define GFIRST " sc1"
typedef __attribute__((address_space(1))) unsigned gu32;

__device__ __forceinline__ void cm4(f32x2& r0, f32x2 a0, f32x2 b0, f32x2& r1, f32x2 a1, f32x2 b1,
                                    f32x2& r2, f32x2 a2, f32x2 b2, f32x2& r3, f32x2 a3, f32x2 b3) {
    asm("v_pk_mul_f32 %0, %4, %5 op_sel:[0,0] op_sel_hi:[1,0]\n\t"
        "v_pk_mul_f32 %1, %6, %7 op_sel:[0,0] op_sel_hi:[1,0]\n\t"
        "v_pk_mul_f32 %2, %8, %9 op_sel:[0,0] op_sel_hi:[1,0]\n\t"
        "v_pk_mul_f32 %3, %10, %11 op_sel:[0,0] op_sel_hi:[1,0]\n\t"
        "v_pk_fma_f32 %0, %4, %5, %0 op_sel:[1,1,0] op_sel_hi:[0,1,1] neg_lo:[1,0,0]\n\t"
        "v_pk_fma_f32 %1, %6, %7, %1 op_sel:[1,1,0] op_sel_hi:[0,1,1] neg_lo:[1,0,0]\n\t"
        "v_pk_fma_f32 %2, %8, %9, %2 op_sel:[1,1,0] op_sel_hi:[0,1,1] neg_lo:[1,0,0]\n\t"
        "v_pk_fma_f32 %3, %10, %11, %3 op_sel:[1,1,0] op_sel_hi:[0,1,1] neg_lo:[1,0,0]"
        : "=&v"(r0), "=&v"(r1), "=&v"(r2), "=&v"(r3)
        : "v"(a0), "v"(b0), "v"(a1), "v"(b1), "v"(a2), "v"(b2), "v"(a3), "v"(b3));
}
constexpr int ka_off(int r) { return r < 32 ? r * KA_ROWB : KA_HI0 - KA_MB + (r - 32) * KA_ROWB; }
template <int R0, int R1, int R2, int R3>
__device__ __forceinline__ void put4(const unsigned (&m0v)[2], f32x2 v0, f32x2 v1, f32x2 v2, f32x2 v3) {
    static_assert((R0 < 32) == (R1 < 32) && (R0 < 32) == (R2 < 32) && (R0 < 32) == (R3 < 32), "");
    unsigned t0, t1, t2, t3;
    asm volatile("s_mov_b32 m0, %4\n\t"
                 "v_cvt_pk_bf16_f32 %0, %5, %6\n\tv_cvt_pk_bf16_f32 %1, %7, %8\n\t"
                 "v_cvt_pk_bf16_f32 %2, %9, %10\n\tv_cvt_pk_bf16_f32 %3, %11, %12\n\t"
                 "ds_write_addtid_b32 %0 offset:%13\n\tds_write_addtid_b32 %1 offset:%14\n\t"
                 "ds_write_addtid_b32 %2 offset:%15\n\tds_write_addtid_b32 %3 offset:%16"
                 : "=&v"(t0), "=&v"(t1), "=&v"(t2), "=&v"(t3)
                 : "s"(m0v[R0 >= 32]), "v"(v0.x), "v"(v0.y), "v"(v1.x), "v"(v1.y), "v"(v2.x), "v"(v2.y), "v"(v3.x), "v"(v3.y),
                   "n"(ka_off(R0)), "n"(ka_off(R1)), "n"(ka_off(R2)), "n"(ka_off(R3))
                 : "m0", "memory");
}
__device__ __forceinline__ f32x2 cexp_p(float zr, float zi, float p) {
    const float mag = __builtin_amdgcn_exp2f(zr * p);
    const float rev = __builtin_amdgcn_fractf(zi * p);
    f32x2 r;
    r.x = mag * __builtin_amdgcn_cosf(rev);
    r.y = mag * __builtin_amdgcn_sinf(rev);
    return r;
}
__device__ __forceinline__ float wave_sum(float v) {
    v = row16_sum(v);
    const int i = __float_as_int(v);
    return (__int_as_float(__builtin_amdgcn_readlane(i, 0)) + __int_as_float(__builtin_amdgcn_readlane(i, 16))) +
           (__int_as_float(__builtin_amdgcn_readlane(i, 32)) + __int_as_float(__builtin_amdgcn_readlane(i, 48)));
}
__device__ __forceinline__ unsigned next_lane(unsigned v, unsigned last) {
    return (unsigned)__builtin_amdgcn_update_dpp((int)last, (int)v, 0x130, 0xf, 0xf, false);
}
__device__ __forceinline__ void put_copies(char* p, unsigned r0, unsigned r1, unsigned r2, unsigned r3, unsigned r4, unsigned r5) {
    const unsigned s0 = __builtin_amdgcn_alignbit(r1, r0, 16), s1 = __builtin_amdgcn_alignbit(r2, r1, 16),
                   s2 = __builtin_amdgcn_alignbit(r3, r2, 16), s3 = __builtin_amdgcn_alignbit(r4, r3, 16),
                   s4 = __builtin_amdgcn_alignbit(r5, r4, 16);
    *(u32x4*)(p + 0 * CV_COPY) = (u32x4){r0, r1, r2, r3};
    *(u32x4*)(p + 1 * CV_COPY) = (u32x4){s0, s1, s2, s3};
    *(u32x4*)(p + 2 * CV_COPY) = (u32x4){r1, r2, r3, r4};
    *(u32x4*)(p + 3 * CV_COPY) = (u32x4){s1, s2, s3, s4};
}
template <int O> __device__ __forceinline__ void rd64(u32x2& v, unsigned a) {
    asm volatile("ds_read_b64 %0, %1 offset:%2" : "=v"(v) : "v"(a), "n"(O) : "memory");
}
template <int O> __device__ __forceinline__ void rd128(u32x4& v, unsigned a) {
    asm volatile("ds_read_b128 %0, %1 offset:%2" : "=v"(v) : "v"(a), "n"(O) : "memory");
}
struct CvFrag { u32x2 lo[8], hi[8]; u32x4 b[8]; };
__device__ __forceinline__ void cv_wait(CvFrag& f) {
    asm volatile("s_waitcnt lgkmcnt(0)"
                 : "+v"(f.lo[0]), "+v"(f.lo[1]), "+v"(f.lo[2]), "+v"(f.lo[3]), "+v"(f.lo[4]), "+v"(f.lo[5]), "+v"(f.lo[6]), "+v"(f.lo[7]),
                   "+v"(f.hi[0]), "+v"(f.hi[1]), "+v"(f.hi[2]), "+v"(f.hi[3]), "+v"(f.hi[4]), "+v"(f.hi[5]), "+v"(f.hi[6]), "+v"(f.hi[7]),
                   "+v"(f.b[0]), "+v"(f.b[1]), "+v"(f.b[2]), "+v"(f.b[3]), "+v"(f.b[4]), "+v"(f.b[5]), "+v"(f.b[6]), "+v"(f.b[7])
                 :: "memory");
}
template <int T>
__device__ __forceinline__ void cv_mma(const CvFrag& f, f32x4 (&acc)[8]) {
    constexpr int q = T < 1 ? 0 : T < 3 ? 1 : T < 6 ? 2 : T < 10 ? 3 : T < 15 ? 4 : T < 21 ? 5 : T < 28 ? 6 : 7;
    constexpr int g1 = T - q * (q + 1) / 2;
    const u32x4 a = {f.lo[g1].x, f.lo[g1].y, f.hi[g1].x, f.hi[g1].y};
    acc[q] = mfma16(a, f.b[q - g1], acc[q]);
}
template <int G0, int R>
__device__ __forceinline__ void cv_read(CvFrag& f, unsigned aa, unsigned ba) {
    constexpr int G1 = R / 3;
    if constexpr (R % 3 == 0) rd64<(63 - 8 * G1 - G0) * 64>(f.lo[G1], aa);
    else if constexpr (R % 3 == 1) rd64<(63 - 8 * G1 - G0) * 64 + 8>(f.hi[G1], aa);
    else rd128<(8 * G1 - G0 + 7) * 64>(f.b[G1], ba);
}
template <int G0, int T>
__device__ __forceinline__ void cv_group(const CvFrag& cur, CvFrag& nxt, f32x4 (&acc)[8], unsigned aa, unsigned ba) {
    cv_mma<T>(cur, acc);
    __builtin_amdgcn_sched_barrier(0);
    if constexpr (G0 < 7 && T < 30 && T % 5 != 4) {
        cv_read<G0 + 1, T - T / 5>(nxt, aa, ba);
        __builtin_amdgcn_sched_barrier(0);
    }
    if constexpr (T < 35) cv_group<G0, T + 1>(cur, nxt, acc, aa, ba);
}
template <int G0>
__device__ __forceinline__ void cv_round(CvFrag& cur, CvFrag& nxt, f32x4 (&acc)[8], unsigned aa, unsigned ba) {
    cv_wait(cur);
    if constexpr (G0 == 5) __builtin_amdgcn_s_barrier();
    __builtin_amdgcn_sched_barrier(0);
    cv_group<G0, 0>(cur, nxt, acc, aa, ba);
    if constexpr (G0 < 7) cv_round<G0 + 1>(nxt, cur, acc, aa, ba);
}
template <int I>
__device__ __forceinline__ void cv_first(CvFrag& f, unsigned aa, unsigned ba) {
    cv_read<0, I>(f, aa, ba);
    if constexpr (I < 23) cv_first<I + 1>(f, aa, ba);
}

template <class Hook>
__device__ __forceinline__ void ka_chain(const unsigned (&m0v)[2], float p_la, float p_ai, float p_cr, float p_ci, float p_dt, Hook hook) {
    const float dt = rb(__builtin_amdgcn_exp2f(p_dt * 1.44269504088896341f));
    const float ar = -rb(__builtin_amdgcn_exp2f(p_la * 1.44269504088896341f));
    const float zr = ar * dt * 1.44269504088896341f, zi = p_ai * dt * 0.15915494309189535f;
    f32x2 a[17], b[16], e[8], F1, F2, F4, F8, r0, r1, r2, r3;
    a[0] = (f32x2){2.0f * (p_cr * dt), -2.0f * (p_ci * dt)};
    e[1] = cexp_p(zr, -zi, 1.f);
    b[1] = cexp_p(zr, zi, 8.f);
    F1 = cexp_p(zr, -zi, 128.f);
    cm4(e[2], e[1], e[1], b[2], b[1], b[1], F2, F1, F1, a[1], a[0], F1);
    cm4(e[4], e[2], e[2], b[4], b[2], b[2], F4, F2, F2, e[3], e[2], e[1]);
    put4<41, 42, 43, 45>(m0v, (f32x2){1.f, 0.f}, b[1], b[2], b[4]);
    hook(0);
    cm4(a[2], a[0], F2, a[3], a[1], F2, b[3], b[2], b[1], r0, a[0], e[1]);
    cm4(F8, F4, F4, b[8], b[4], b[4], r1, a[1], e[1], r2, a[0], e[2]);
    put4<16, 20, 1, 9>(m0v, a[2], a[3], r0, r1);
    hook(1);
    cm4(a[4], a[0], F4, a[5], a[1], F4, a[6], a[2], F4, a[7], a[3], F4);
    put4<24, 26, 28, 30>(m0v, a[4], a[5], a[6], a[7]);
    hook(2);
    cm4(e[5], e[4], e[1], e[6], e[4], e[2], e[7], e[4], e[3], r3, a[1], e[2]);
    put4<0, 8, 2, 10>(m0v, a[0], a[1], r2, r3);
    hook(3);
    cm4(b[5], b[4], b[1], b[6], b[4], b[2], b[7], b[4], b[3], r0, a[0], e[3]);
    put4<44, 49, 46, 47>(m0v, b[3], b[8], b[5], b[6]);
    hook(4);
    cm4(a[8], a[0], F8, a[9], a[1], F8, a[10], a[2], F8, a[11], a[3], F8);
    put4<32, 33, 34, 35>(m0v, a[8], a[9], a[10], a[11]);
    hook(5);
    cm4(a[12], a[4], F8, a[13], a[5], F8, a[14], a[6], F8, a[15], a[7], F8);
    put4<36, 37, 38, 39>(m0v, a[12], a[13], a[14], a[15]);
    hook(6);
    cm4(a[16], a[8], F8, r1, a[1], e[3], r2, a[0], e[4], r3, a[1], e[4]);
    put4<3, 11, 4, 12>(m0v, r0, r1, r2, r3);
    hook(7);
    cm4(b[9], b[8], b[1], b[10], b[8], b[2], b[11], b[8], b[3], b[12], b[8], b[4]);
    put4<50, 51, 52, 53>(m0v, b[9], b[10], b[11], b[12]);
    hook(8);
    cm4(b[13], b[8], b[5], b[14], b[8], b[6], b[15], b[8], b[7], r0, a[0], e[5]);
    put4<54, 55, 56, 48>(m0v, b[13], b[14], b[15], b[7]);
    asm volatile("ds_write_addtid_b32 %0 offset:%1" :: "v"(pk2(a[16].x, a[16].y)), "n"(ka_off(40)) : "memory");
    hook(9);
    cm4(r1, a[1], e[5], r2, a[0], e[6], r3, a[1], e[6], F1, a[0], e[7]);
    put4<5, 13, 6, 14>(m0v, r0, r1, r2, r3);
    hook(10);
    cm4(r0, a[1], e[7], r1, a[2], e[2], r2, a[3], e[2], F2, a[2], e[4]);
    put4<7, 15, 17, 21>(m0v, F1, r0, r1, r2);
    hook(11);
    cm4(r3, a[3], e[4], F1, a[2], e[6], r0, a[3], e[6], r1, a[4], e[4]);
    put4<18, 22, 19, 23>(m0v, F2, r3, F1, r0);
    hook(12);
    cm4(r2, a[5], e[4], r3, a[6], e[4], F1, a[7], e[4], F2, a[7], e[4]);
    put4<25, 27, 29, 31>(m0v, r1, r2, r3, F1);
    asm volatile("s_waitcnt lgkmcnt(0)" ::: "memory");
}
__device__ __forceinline__ void unpack4(float (&o)[4], u32x2 v) {
    asm volatile("v_lshlrev_b32 %0, 16, %4\n\tv_and_b32 %1, 0xffff0000, %4\n\tv_lshlrev_b32 %2, 16, %5\n\tv_and_b32 %3, 0xffff0000, %5"
                 : "=&v"(o[0]), "=&v"(o[1]), "=&v"(o[2]), "=&v"(o[3]) : "v"(v.x), "v"(v.y));
}

__global__ __launch_bounds__(512, 2) void ka_conv(const u16* __restrict__ x, const u16* __restrict__ logA,
                                                  const u16* __restrict__ Aim, const u16* __restrict__ Cre,
                                                  const u16* __restrict__ Cim, const u16* __restrict__ logdt,
                                                  const u16* __restrict__ dskip, char* ws,
                                                  const u16* __restrict__ w1, const u16* __restrict__ b1) {
    __shared__ __attribute__((aligned(16))) char smem[KA_U + 4 * CV_UB];
    const int tid = threadIdx.x, lane = tid & 63;
    const int w = __builtin_amdgcn_readfirstlane(tid >> 6);
    STAMP(0, 0);
    asm volatile("" :: "s"(x), "s"(logA), "s"(Aim), "s"(Cre), "s"(Cim), "s"(logdt), "s"(dskip), "s"(ws));
    const int xcd = blockIdx.x & 7, idx = blockIdx.x >> 3;
    const bool conv = idx < 20;
    const int grp = xcd * 20 + (conv ? idx : 0);
    const int d0 = grp * 4;
    const int row = (xcd * 32 + idx) * 8 + w;
    unsigned sv[5], epoch, pp[5];
    float pf[5];
    asm volatile("global_load_dword %0, %11, %12\n\tglobal_load_dword %1, %11, %12 offset:256\n\t"
                 "global_load_dword %2, %11, %12 offset:512\n\tglobal_load_dword %3, %11, %12 offset:768\n\t"
                 "global_load_dword %4, %11, %12 offset:1024\n\tglobal_load_dword %5, %13, %14\n\t"
                 "global_load_ushort %6, %15, %16\n\tglobal_load_ushort %7, %15, %17\n\t"
                 "global_load_ushort %8, %15, %18\n\tglobal_load_ushort %9, %15, %19\n\t"
                 "global_load_ushort %10, %13, %20"
                 : "=&v"(sv[0]), "=&v"(sv[1]), "=&v"(sv[2]), "=&v"(sv[3]), "=&v"(sv[4]), "=&v"(epoch),
                   "=&v"(pp[0]), "=&v"(pp[1]), "=&v"(pp[2]), "=&v"(pp[3]), "=&v"(pp[4])
                 : "v"(lane * 4), "s"(x + (size_t)row * D_), "v"(0), "s"(ws + WS_EP),
                   "v"((d0 * N_ + tid) * 2), "s"(logA), "s"(Aim), "s"(Cre), "s"(Cim), "s"(logdt + d0 + (w >> 1))
                 : "memory");
    const int t = tid & 255;
    const __amdgpu_buffer_rsrc_t srdG = __builtin_amdgcn_make_buffer_rsrc((void*)(ws + WS_GR), (short)0, L_ * 8, 0x00020000);
    const __amdgpu_buffer_rsrc_t srdX = __builtin_amdgcn_make_buffer_rsrc((void*)x, (short)0, SZ_, 0x00020000);
    const int owner = conv ? w >> 2 : 0;
    u32x2 xc[8], xd;
    asm volatile("s_barrier\n\ts_cmp_eq_u32 %5, 0\n\ts_cbranch_scc1 .Lka_a%=\n\t"
                 "global_load_dwordx2 %4, %6, %7\n\t"
                 "buffer_load_dwordx2 %0, %8, %9, %10 offen\n\tbuffer_load_dwordx2 %1, %8, %9, %10 offen offset:1280\n\t"
                 "buffer_load_dwordx2 %2, %8, %9, %10 offen offset:2560\n\tbuffer_load_dwordx2 %3, %8, %9, %10 offen offset:3840\n\t"
                 "s_waitcnt vmcnt(5)\n\ts_branch .Lka_b%=\n"
                 ".Lka_a%=:\n\ts_waitcnt vmcnt(0)\n"
                 ".Lka_b%=:"
                 : "=&v"(xc[0]), "=&v"(xc[1]), "=&v"(xc[2]), "=&v"(xc[3]), "=&v"(xd)
                 : "s"(owner), "v"(0), "s"(dskip + d0), "v"(t * (8 * D_ * 2)), "s"(srdX), "s"(d0 * 2) : "scc", "memory");
    unsigned etag;
    {
        float v[12];
        unpack4(*(float(*)[4])v, (u32x2){sv[0], sv[1]});
        unpack4(*(float(*)[4])(v + 4), (u32x2){sv[2], sv[3]});
        asm volatile("v_lshlrev_b32 %0, 16, %3\n\tv_and_b32 %1, 0xffff0000, %3\n\tv_xor_b32 %2, 0x9e3779b9, %4"
                     : "=&v"(v[8]), "=&v"(v[9]), "=&v"(etag) : "v"(sv[4]), "v"(epoch));
        const float s = ((v[0] + v[1]) + (v[2] + v[3])) + ((v[4] + v[5]) + (v[6] + v[7])) + (v[8] + v[9]);
        const float mean = wave_sum(s) * (1.0f / D_);
        float q0 = 0.f, q1 = 0.f;
#pragma unroll
        for (int j = 0; j < 5; ++j) {
            const float a = v[2 * j] - mean, b = v[2 * j + 1] - mean;
            q0 = __builtin_fmaf(a, a, q0);
            q1 = __builtin_fmaf(b, b, q1);
        }
        const float q = wave_sum(q0 + q1);
        const float den = __builtin_amdgcn_sqrtf(rb(rb(q * (1.0f / D_)) + 1e-5f));
        asm volatile("buffer_store_dwordx2 %10, %11, %12, %13 offen sc1\n\t"
                     "v_lshlrev_b32 %0, 16, %5\n\tv_lshlrev_b32 %1, 16, %6\n\tv_lshlrev_b32 %2, 16, %7\n\t"
                     "v_lshlrev_b32 %3, 16, %8\n\tv_lshlrev_b32 %4, 16, %9"
                     : "=&v"(pf[0]), "=&v"(pf[1]), "=&v"(pf[2]), "=&v"(pf[3]), "=&v"(pf[4])
                     : "v"(pp[0]), "v"(pp[1]), "v"(pp[2]), "v"(pp[3]), "v"(pp[4]),
                       "v"((u32x2){pk2(mean, den), etag * 0x85ebca6bu}), "v"(lane * (L_ * 8)), "s"(srdG), "s"(row * 8) : "memory");
    }
    STAMP(0, 1);
    STAMPT(5, 4, 448);
    if (!conv) {
        const unsigned lds = (unsigned)(size_t)(LDS_AS char*)(smem + w * 1024);
        warm_w<0>(w1, b1, xcd, (idx - 20) * 8 + w, 96, lds, lane);
        if (idx == 31 && tid < 256) {
            const int r = xcd * 8 + (tid >> 5), c = tid & 31;
            *(unsigned*)(ws + WS_ID + r * (L_ * 2) + 4 * c) = (r >> 1) == c ? ((r & 1) ? 0x3f800000u : 0x3f80u) : 0u;
        }
        if (idx == 30 && tid < 128) *(u32x4*)(ws + WS_G2 + (xcd * 128 + tid) * 16) = (u32x4){0u, 0u, 0u, 0u};
        if (idx == 29 && tid < 32) *(unsigned*)(ws + WS_FL + (xcd * 32 + tid) * 128) = 0u;
        asm volatile("s_waitcnt vmcnt(0)" ::: "memory");
        STAMP(0, 7);
        return;
    }
    const unsigned lds0 = (unsigned)(size_t)(LDS_AS char*)smem;
    const int m = lane & 15, hg = lane >> 4;
    char* mine = smem + (w & 3) * KA_LO;
    char* minh = smem + KA_HI0 + (w & 3) * KA_HI;
    const unsigned m0v[2] = {lds0 + (w >> 1) * KA_LO + (w & 1) * 256, lds0 + KA_MB + (w >> 1) * KA_HI + (w & 1) * 256};
    ka_chain(m0v, pf[0], pf[1], pf[2], pf[3], pf[4], [&](int i) {
        if (i % XCS == 0 && i / XCS < 4)
            asm volatile("s_cmp_eq_u32 %1, 0\n\ts_cbranch_scc1 .Lka_c%=\n\tbuffer_load_dwordx2 %0, %2, %3, %4 offen offset:%5\n.Lka_c%=:"
                         : "=v"(xc[4 + i / XCS])
                         : "s"(owner), "v"(t * (8 * D_ * 2)), "s"(srdX), "s"(d0 * 2 + 4 * D_ * 2), "n"((i / XCS) * (D_ * 2)) : "scc", "memory");
    });
    STAMP(0, 2);
    STAMPT(1, 0, 448);
    __builtin_amdgcn_s_barrier();
    if (w < 4) {
        STAMPT(1, 1, 0);
        u16* kd = (u16*)(minh + 8192);
        {
            const char* fb = mine + m * KA_ROWB + hg * 16;
            const char* fh = minh + m * KA_ROWB + hg * 16;
            u32x4 fa[8][3], fl[8];
#pragma unroll
            for (int ks = 0; ks < 8; ++ks) {
                fl[ks] = *(const u32x4*)(fh + 9 * KA_ROWB + ks * 64);
                fa[ks][0] = *(const u32x4*)(fb + ks * 64);
                fa[ks][1] = *(const u32x4*)(fb + 16 * KA_ROWB + ks * 64);
                fa[ks][2] = *(const u32x4*)(fh + ks * 64);
            }
            __builtin_amdgcn_sched_barrier(0);
            f32x4 acc0 = {0.f, 0.f, 0.f, 0.f}, acc1 = acc0, acc2 = acc0;
#pragma unroll
            for (int ks = 0; ks < 8; ++ks) {
                acc0 = mfma16(fa[ks][0], fl[ks], acc0);
                acc1 = mfma16(fa[ks][1], fl[ks], acc1);
                acc2 = mfma16(fa[ks][2], fl[ks], acc2);
            }
            if (acc0[0] == 1234.5f) STAMPT(1, 7, 0);
            if (acc1[0] == 1234.5f) STAMPT(1, 7, 0);
            if (acc2[0] == 1234.5f) STAMPT(1, 7, 0);
            STAMPT(1, 2, 0);
            const int c = m;
            *(u32x2*)(kd + 128 * (hg >> 1) + 4 * (hg & 1) + 8 * c) = (u32x2){pk2(acc0[0], acc0[1]), pk2(acc0[2], acc0[3])};
            const unsigned p01 = pk2(acc1[0], acc1[1]), p23 = pk2(acc1[2], acc1[3]);
            const int i0 = hg < 2 ? 256 + 64 * hg + 4 * c : 256 + 64 * hg + 2 * c;
            *(unsigned*)(kd + i0) = p01;
            *(unsigned*)(kd + i0 + (hg < 2 ? 2 : 32)) = p23;
            const unsigned q01 = pk2(acc2[0], acc2[1]), q23 = pk2(acc2[2], acc2[3]);
            kd[512 + 64 * hg + c] = (u16)(q01 & 0xffff);
            kd[528 + 64 * hg + c] = (u16)(q01 >> 16);
            kd[544 + 64 * hg + c] = (u16)(q23 & 0xffff);
            kd[560 + 64 * hg + c] = (u16)(q23 >> 16);
            asm volatile("" ::: "memory");
        }
        const unsigned* kw = (const unsigned*)kd;
        const unsigned dw0 = kw[319 - lane], dw1 = kw[320 - lane];
        const unsigned cw0 = kw[255 - lane], cw1 = kw[256 - lane];
        const bool isA = lane >= 32;
        const char* bp = (const char*)kd + (isA ? 16 * (63 - lane) : 256 + 8 * (63 - lane));
        const u32x2 b01 = *(const u32x2*)bp, b23 = *(const u32x2*)(bp + 8);
        if (dw0 + dw1 + cw0 + cw1 + b01.x + b23.x == 1234) STAMPT(1, 7, 0);
        STAMPT(1, 3, 0);
        const unsigned P = __builtin_amdgcn_perm(0, dw1, 0x01000100), Q = __builtin_amdgcn_perm(0, dw0, 0x03020302),
                       M = __builtin_amdgcn_perm(0, dw0, 0x01000302), Z = __builtin_amdgcn_perm(0, dw0, 0x01000100);
        const unsigned c0 = __builtin_amdgcn_perm(0, cw1, 0x01000100), c1 = __builtin_amdgcn_perm(0, cw0, 0x03020302),
                       c2 = __builtin_amdgcn_perm(0, cw0, 0x01000302), c3 = __builtin_amdgcn_perm(0, cw0, 0x01000100);
        const unsigned o0 = __builtin_amdgcn_perm(b23.x, isA ? b23.y : b01.y, isA ? 0x01000302u : 0x03020504u),
                       o1 = __builtin_amdgcn_perm(b23.x, b01.y, isA ? 0x05040706u : 0x01000100u),
                       o2 = __builtin_amdgcn_perm(b01.y, b01.x, isA ? 0x05040706u : 0x03020504u),
                       o3 = __builtin_amdgcn_perm(0, b01.x, isA ? 0x01000302u : 0x01000100u);
        put_copies(mine + 3072 + lane * 16, o0, o1, o2, o3, next_lane(o0, 0), next_lane(o1, 0));
        put_copies(mine + 2048 + lane * 16, c0, c1, c2, c3, next_lane(c0, __builtin_amdgcn_readlane(o0, 0)),
                   next_lane(c1, __builtin_amdgcn_readlane(o1, 0)));
        put_copies(mine + lane * 32, P, P, Q, Q, Q, M);
        put_copies(mine + lane * 32 + 16, Q, M, Z, Z, next_lane(P, __builtin_amdgcn_readlane(c0, 0)),
                   next_lane(P, __builtin_amdgcn_readlane(c1, 0)));
        if (lane < 8) *(u32x4*)(mine + (lane >> 1) * CV_COPY + 4096 + (lane & 1) * 16) = (u32x4){0u, 0u, 0u, 0u};
        STAMP(0, 3);
        __syncthreads();
        STAMP(0, 4);
        const unsigned aa = (unsigned)(size_t)(LDS_AS char*)(mine + (3 - (m & 3)) * CV_COPY + 2 * (2028 - 32 * 63 - 4 * (m >> 2) + 8 * hg));
        const unsigned ba = (unsigned)(size_t)(LDS_AS char*)(smem + KA_U + w * CV_UB + (2 * m + hg) * 16);
        f32x4 acc[8];
#pragma unroll
        for (int q = 0; q < 8; ++q) acc[q] = (f32x4){0.f, 0.f, 0.f, 0.f};
        CvFrag f0, f1;
        cv_first<0>(f0, aa, ba);
        cv_round<0>(f0, f1, acc, aa, ba);
        STAMP(0, 5);
        const char* rp = minh + (16 * m + 4 * hg) * 4;
        char* hp = mine + (16 * m + 4 * hg) * 2;
#pragma unroll
        for (int q = 0; q < 8; ++q) {
            const f32x4 h = acc[q] + *(const f32x4*)(rp + q * 1024);
            *(u32x2*)(hp + q * 512) = (u32x2){pk2(h[0], h[1]), pk2(h[2], h[3])};
        }
    } else {
        const unsigned tag = etag * 0x85ebca6bu;
        u32x4 g[4];
        asm volatile("buffer_load_dwordx4 %0, %4, %5, 0 offen" GFIRST "\n\tbuffer_load_dwordx4 %1, %4, %5, 0 offen offset:16" GFIRST "\n\t"
                     "buffer_load_dwordx4 %2, %4, %5, 0 offen offset:32" GFIRST "\n\tbuffer_load_dwordx4 %3, %4, %5, 0 offen offset:48" GFIRST "\n\t"
                     "s_waitcnt vmcnt(4)"
                     : "=&v"(g[0]), "=&v"(g[1]), "=&v"(g[2]), "=&v"(g[3]) : "v"(t * 64), "s"(srdG) : "memory");
        float xf[8][4], dk[4];
#pragma unroll
        for (int k = 0; k < 8; ++k) unpack4(xf[k], xc[k]);
        unpack4(dk, xd);
        if (lane < 30) *(u32x4*)(smem + KA_U + (w - 4) * CV_UB + lane * 16) = (u32x4){0u, 0u, 0u, 0u};
        STAMPT(1, 4, 256);
        asm volatile("s_waitcnt vmcnt(0)" : "+v"(g[0]), "+v"(g[1]), "+v"(g[2]), "+v"(g[3]) :: "memory");
        unsigned spins = 0;
#pragma unroll 1
        for (; spins < (1u << 16); ++spins) {
            const bool bad = ((g[0].y ^ tag) | (g[0].w ^ tag) | (g[1].y ^ tag) | (g[1].w ^ tag) |
                              (g[2].y ^ tag) | (g[2].w ^ tag) | (g[3].y ^ tag) | (g[3].w ^ tag)) != 0;
            if (!__any(bad)) break;
            if (bad)
                asm volatile("buffer_load_dwordx4 %0, %4, %5, 0 offen sc1\n\tbuffer_load_dwordx4 %1, %4, %5, 0 offen offset:16 sc1\n\t"
                             "buffer_load_dwordx4 %2, %4, %5, 0 offen offset:32 sc1\n\tbuffer_load_dwordx4 %3, %4, %5, 0 offen offset:48 sc1\n\t"
                             "s_waitcnt vmcnt(0)"
                             : "+v"(g[0]), "+v"(g[1]), "+v"(g[2]), "+v"(g[3]) : "v"(t * 64), "s"(srdG) : "memory");
        }
        STAMPT(1, 5, 256);
        unsigned up[4][4];
#pragma unroll
        for (int k = 0; k < 4; ++k) {
            const float i0 = __builtin_amdgcn_rcpf(bfhi(g[k].x)), i1 = __builtin_amdgcn_rcpf(bfhi(g[k].z));
            const float m0 = bflo(g[k].x), m1 = bflo(g[k].z);
#pragma unroll
            for (int c = 0; c < 4; ++c) up[c][k] = pk2((xf[2 * k][c] - m0) * i0, (xf[2 * k + 1][c] - m1) * i1);
        }
#pragma unroll
        for (int c = 0; c < 4; ++c)
            *(u32x4*)(smem + KA_U + c * CV_UB + 480 + t * 16) = (u32x4){up[c][0], up[c][1], up[c][2], up[c][3]};
        STAMPT(1, 6, 256);
        __syncthreads();
        if (blockIdx.x == 0 && tid == 256)
            __hip_atomic_store((gu32*)(ws + WS_EP), (etag ^ 0x9e3779b9u) + 1u, __ATOMIC_RELAXED, __HIP_MEMORY_SCOPE_AGENT);
#pragma unroll
        for (int c = 0; c < 4; ++c) {
            f32x4 r0, r1;
#pragma unroll
            for (int k = 0; k < 2; ++k) {
                r0[2 * k] = __builtin_fmaf(bflo(up[c][k]), dk[c], xf[2 * k][c]);
                r0[2 * k + 1] = __builtin_fmaf(bfhi(up[c][k]), dk[c], xf[2 * k + 1][c]);
                r1[2 * k] = __builtin_fmaf(bflo(up[c][2 + k]), dk[c], xf[4 + 2 * k][c]);
                r1[2 * k + 1] = __builtin_fmaf(bfhi(up[c][2 + k]), dk[c], xf[5 + 2 * k][c]);
            }
            *(f32x4*)(smem + KA_HI0 + c * KA_HI + t * 32) = r0;
            *(f32x4*)(smem + KA_HI0 + c * KA_HI + t * 32 + 16) = r1;
        }
        asm volatile("s_waitcnt lgkmcnt(0)" ::: "memory");
        __builtin_amdgcn_s_barrier();
    }
    __syncthreads();
    {
        const int ch = tid & 3;
        const int dt = grp / 20, rem = grp % 20, mq = rem / 5, j = rem % 5;
#pragma unroll
        for (int i = 0; i < 2; ++i) {
            const int piece = (tid >> 2) + 128 * i;
            const int unit = ((piece >> 2) * 40 + dt * 5 + j) * 64 + (piece & 3) * 16 + 4 * mq + ch;
            *(u32x4*)(ws + WS_HB + (size_t)unit * 16) = *(const u32x4*)(smem + ch * KA_LO + piece * 16);
        }
    }
    STAMP(0, 6);
}

constexpr int B_REG = 40960;
constexpr int B_RED = 24576;
constexpr int B_TAB = 32768;
#define PPOS 6
#define PRE 3

__device__ __forceinline__ float gelu_tanh(float x) {
    const float p = __builtin_fmaf(x * x, -2.302208198f * 0.044715f, -2.302208198f);
    const float e = __builtin_amdgcn_exp2f(p * x);
    return x * __builtin_amdgcn_rcpf(1.0f + e);
}
template <int I>
__device__ __forceinline__ void w_piece(__amdgpu_buffer_rsrc_t srd, unsigned v, unsigned so, unsigned m0v) {
    if (I == 0) asm volatile("s_mov_b32 m0, %0\n\ts_nop 0\n\tbuffer_load_dwordx4 %1, %2, %3 offen lds"
                             :: "s"(m0v), "v"(v), "s"(srd), "s"(so) : "m0", "memory");
    else asm volatile("buffer_load_dwordx4 %0, %1, %2 offen offset:%3 lds"
                      :: "v"(v), "s"(srd), "s"(so), "n"(I * 1024) : "memory");
}
__device__ __forceinline__ void w_issue(__amdgpu_buffer_rsrc_t srd, const unsigned (&v)[4], unsigned so, unsigned m0v) {
    asm volatile("s_mov_b32 m0, %0\n\ts_nop 0\n\t"
                 "buffer_load_dwordx4 %1, %5, %6 offen lds\n\tbuffer_load_dwordx4 %2, %5, %6 offen offset:1024 lds\n\t"
                 "buffer_load_dwordx4 %3, %5, %6 offen offset:2048 lds\n\tbuffer_load_dwordx4 %4, %5, %6 offen offset:3072 lds"
                 :: "s"(m0v), "v"(v[0]), "v"(v[1]), "v"(v[2]), "v"(v[3]), "s"(srd), "s"(so) : "m0", "memory");
}
template <int SC, int SL, int J>
__device__ __forceinline__ void a_piece(__amdgpu_buffer_rsrc_t srd, unsigned av0, unsigned av1, unsigned so) {
    if (SC) asm volatile("buffer_load_dwordx4 a[%c0:%c1], %2, %3, %4 offen offset:%5 sc1"
                         :: "i"(SL * 20 + J * 4), "i"(SL * 20 + J * 4 + 3), "v"(J < 4 ? av0 : av1), "s"(srd), "s"(so), "n"((J & 3) * 1024) : "memory");
    else asm volatile("buffer_load_dwordx4 a[%c0:%c1], %2, %3, %4 offen offset:%5"
                      :: "i"(SL * 20 + J * 4), "i"(SL * 20 + J * 4 + 3), "v"(J < 4 ? av0 : av1), "s"(srd), "s"(so), "n"((J & 3) * 1024) : "memory");
}
template <int SC, int SL>
__device__ __forceinline__ void a_issue(__amdgpu_buffer_rsrc_t srd, unsigned av0, unsigned av1, unsigned so) {
    a_piece<SC, SL, 0>(srd, av0, av1, so); a_piece<SC, SL, 1>(srd, av0, av1, so); a_piece<SC, SL, 2>(srd, av0, av1, so);
    a_piece<SC, SL, 3>(srd, av0, av1, so); a_piece<SC, SL, 4>(srd, av0, av1, so);
}
template <int N> __device__ __forceinline__ void vm_wait() { asm volatile("s_waitcnt vmcnt(%0)" :: "n"(N) : "memory"); }
template <int R>
__device__ __forceinline__ void affine4(f32x4& d, u32x2 al, f32x4 be) {
    asm volatile("v_mfma_f32_4x4x4_16b_bf16 %0, %1, a[%c2:%c3], %4" : "=&v"(d) : "v"(al), "i"(R), "i"(R + 1), "v"(be));
}
template <int INIT>
__device__ __forceinline__ void mma_vv(f32x4& c, u32x4 a, u32x4 b, f32x4 c0) {
    if (INIT == 0) asm volatile("v_mfma_f32_16x16x32_bf16 %0, %1, %2, %0" : "+v"(c) : "v"(a), "v"(b));
    if (INIT == 1) asm volatile("v_mfma_f32_16x16x32_bf16 %0, %1, %2, 0" : "=&v"(c) : "v"(a), "v"(b));
    if (INIT == 2) asm volatile("v_mfma_f32_16x16x32_bf16 %0, %1, %2, %3" : "=&v"(c) : "v"(a), "v"(b), "v"(c0));
}
template <int INIT, int R>
__device__ __forceinline__ void mma_av(f32x4& c, u32x4 b, f32x4 c0) {
    if (INIT == 0) asm volatile("v_mfma_f32_16x16x32_bf16 %0, a[%c1:%c2], %3, %0" : "+v"(c) : "i"(R), "i"(R + 3), "v"(b));
    if (INIT == 1) asm volatile("v_mfma_f32_16x16x32_bf16 %0, a[%c1:%c2], %3, 0" : "=&v"(c) : "i"(R), "i"(R + 3), "v"(b));
    if (INIT == 2) asm volatile("v_mfma_f32_16x16x32_bf16 %0, a[%c1:%c2], %3, %4" : "=&v"(c) : "i"(R), "i"(R + 3), "v"(b), "v"(c0));
}
#define SB() __builtin_amdgcn_sched_barrier(0)
struct Nrm { u32x2 a0, a1; f32x4 b0, b1; };
struct KbCtx {
    __amdgpu_buffer_rsrc_t srdW1, srdW2, srdL, srdA, srdH;
    unsigned wv1[4], wv2[4], av0, av1, ring, w1o, w2o, wlo, a1o, a2o, alo;
    const char* tp[4];
    const char* ta;
    const char* tb;
    char* red;
    f32x4 bias;
};
template <int R>
__device__ __forceinline__ void w_read(u32x4 (&wf)[4], const KbCtx& c, int SL) {
    const u32x2 v = tr_read(c.tp[R >> 1] + SL * 4096 + (R & 1) * 512);
    if (R & 1) { wf[R >> 1].z = v.x; wf[R >> 1].w = v.y; } else { wf[R >> 1].x = v.x; wf[R >> 1].y = v.y; }
}
__device__ __forceinline__ void nrm_read(Nrm& t, const KbCtx& c, int S) {
    t.a0 = *(const u32x2*)(c.ta + S * 256); t.a1 = *(const u32x2*)(c.ta + S * 256 + 32);
    t.b0 = *(const f32x4*)(c.tb + S * 128); t.b1 = *(const f32x4*)(c.tb + S * 128 + 16);
}
template <int T>
__device__ __forceinline__ void red_put(const f32x4 (&acc)[4][5], const KbCtx& c) {
    if constexpr (T >= 8) *(f32x4*)(c.red + (T - 8) * 1024) = acc[(T - 8) / 5 + 1][(T - 8) % 5];
    if constexpr (T == 19) {
        SB();
#pragma unroll
        for (int j = 2; j < 5; ++j) *(f32x4*)(c.red + (10 + j) * 1024) = acc[3][j];
    }
}
constexpr int g1_cnt(int t) { return t <= 15 ? 9 : t <= 21 ? 4 : 0; }
constexpr int g1_young(int s) { int r = 0; for (int t = s + 2; t <= s + 7; ++t) r += g1_cnt(t); return r; }
template <int S, int P>
__device__ __forceinline__ void g1_req(const KbCtx& c) {
    if constexpr (S < 8) {
        if constexpr (P < 4) w_piece<P>(c.srdW1, c.wv1[P], c.w1o + (S + 8) * (32 * F_ * 2), c.ring + S * 4096);
        else if constexpr (P < 9) a_piece<0, S, P - 4>(c.srdH, c.av0, c.av1, c.a1o + (S + 8) * 40960);
    } else if constexpr (S < 14 && P < 4) {
        w_piece<P>(c.srdW2, c.wv2[P], c.w2o + (S - 8) * (32 * L_ * 2), c.ring + (S - 8) * 4096);
    }
}
template <int S, int T>
__device__ __forceinline__ void g1_mma(f32x4 (&acc)[4][5], u32x4 (&n)[2][5], u32x4 (&wf)[2][4], const KbCtx& c) {
    mma_vv<S ? 0 : (T & 3) ? 1 : 2>(acc[T & 3][T >> 2], wf[S & 1][T & 3], n[S & 1][T >> 2], c.bias);
    SB();
}
template <int S, int T>
__device__ __forceinline__ void g1_tail(f32x4 (&acc)[4][5], u32x4 (&n)[2][5], u32x4 (&wf)[2][4], f32x4 (&d)[10], const KbCtx& c) {
    g1_mma<S, T>(acc, n, wf, c);
    constexpr int u = T - 4, c0 = u + (u + 3) / 4, c1 = u + 1 + (u + 4) / 4;
#pragma unroll
    for (int k = c0; k < c1; ++k) n[(S + 1) & 1][k >> 2][k & 3] = pk2(d[k >> 1][(k & 1) * 2], d[k >> 1][(k & 1) * 2 + 1]);
    if constexpr ((u & 3) != 0) g1_req<S, u - 1 - u / 4>(c);
    SB();
    if constexpr (T < 19) g1_tail<S, T + 1>(acc, n, wf, d, c);
}
template <int S, int T>
__device__ __forceinline__ void g1_mid(u32x4 (&wf)[4], f32x4 (&d)[10], const Nrm& t, const KbCtx& c) {
    affine4<(S & 7) * 20 + T * 2>(d[T], (T & 1) ? t.a1 : t.a0, (T & 1) ? t.b1 : t.b0);
    if constexpr (T < 8) w_read<S == 15 ? (T + 2) & 7 : T>(wf, c, S & 7);
    SB();
    if constexpr (T < 9) g1_mid<S, T + 1>(wf, d, t, c);
}
template <int T>
__device__ __forceinline__ void g1_last(f32x4 (&acc)[4][5], u32x4 (&n)[5], u32x4 (&wf)[4], const KbCtx& c) {
    constexpr int i = (T / 5 + 1) & 3, j = T % 5;
    mma_vv<0>(acc[i][j], wf[i], n[j], c.bias);
    SB();
    red_put<T>(acc, c);
    SB();
    if constexpr (T < 19) g1_last<T + 1>(acc, n, wf, c);
}
template <int S>
__device__ __forceinline__ void g1_step(f32x4 (&acc)[4][5], u32x4 (&n)[2][5], u32x4 (&wf)[2][4], Nrm (&t)[2], const KbCtx& c) {
    if constexpr (S == 15) {
        asm volatile("s_nop 1");
        g1_last<0>(acc, n[1], wf[1], c);
    } else {
        g1_mma<S, 0>(acc, n, wf, c);
        if constexpr (S < 14) nrm_read(t[S & 1], c, S + 2);
        SB();
        g1_mma<S, 1>(acc, n, wf, c); g1_mma<S, 2>(acc, n, wf, c); g1_mma<S, 3>(acc, n, wf, c);
        vm_wait<g1_young(S)>();
        f32x4 d[10];
        g1_mid<S + 1, 0>(wf[(S + 1) & 1], d, t[(S + 1) & 1], c);
        g1_tail<S, 4>(acc, n, wf, d, c);
        g1_step<S + 1>(acc, n, wf, t, c);
    }
}
constexpr int g2_cnt(int t) { return t <= 7 ? 5 : t <= 11 ? 9 : t <= 13 ? 5 : 0; }
constexpr int g2_young(int s) { int r = 0; for (int t = s + 1; t <= (s < 1 ? 7 : s + 6); ++t) r += g2_cnt(t); return r; }
template <int S, int P>
__device__ __forceinline__ void g2_req(const KbCtx& c) {
    if constexpr (S >= 1 && S <= 4 && P < 4) {
        if constexpr (S <= 2) w_piece<P>(c.srdW2, c.wv2[P], c.w2o + (S + 9) * (32 * L_ * 2), c.ring + (S - 1) * 4096);
        else w_piece<P>(c.srdL, c.wv2[P], c.wlo + (S - 3) * (32 * L_ * 2), c.ring + (S - 1) * 4096);
    } else if constexpr (S >= 1 && S <= 6 && P >= 4 && P < 9) {
        a_piece<1, S - 1, P - 4>(c.srdA, c.av0, c.av1, S <= 4 ? c.a2o + (S + 7) * 40960 : c.alo + (S - 5) * 40960);
    }
}
template <int S, int T>
__device__ __forceinline__ void g2_body(f32x4 (&acc)[4][5], u32x4 (&wf)[2][4], const KbCtx& c) {
    constexpr int i = (T / 5 + 1) & 3, j = T % 5;
    mma_av<S ? 0 : i ? 1 : 2, (S & 7) * 20 + j * 4>(acc[i][j], wf[S & 1][i], c.bias);
    SB();
    if constexpr (S == 13) red_put<T>(acc, c);
    else if constexpr (T < 9) g2_req<S, T>(c);
    else if constexpr (T < 17) w_read<(T - 9 + 2) & 7>(wf[(S + 1) & 1], c, (S + 1) % 10);
    SB();
    if constexpr (T < 19) g2_body<S, T + 1>(acc, wf, c);
}
template <int S>
__device__ __forceinline__ void g2_step(f32x4 (&acc)[4][5], u32x4 (&wf)[2][4], const KbCtx& c) {
    vm_wait<g2_young(S)>();
    if (S == 0) STAMP(4, 1);
    SB();
    g2_body<S, 0>(acc, wf, c);
    if constexpr (S < 13) g2_step<S + 1>(acc, wf, c);
}
__device__ __forceinline__ void kb_reduce(f32x4 (&r)[5], const f32x4 (&acc)[4][5], char* smem, int w, int lane) {
    asm volatile("s_waitcnt lgkmcnt(0)\n\ts_barrier" ::: "memory");
    f32x4 p[5][3];
#pragma unroll
    for (int j = 0; j < 5; ++j)
#pragma unroll
        for (int i = 1; i < 4; ++i)
            p[j][i - 1] = *(const f32x4*)(smem + ((w - i) & 3) * B_REG + B_RED + lane * 16 + (i - 1) * 5120 + j * 1024);
    SB();
#pragma unroll
    for (int j = 0; j < 5; ++j) r[j] = (acc[0][j] + p[j][0]) + (p[j][1] + p[j][2]);
}
template <int S0, int S1>
__device__ __forceinline__ void g1_groups(const KbCtx& c) {
    if constexpr (S0 < S1) {
        if (S0 >= 3) vm_wait<54>();
        w_issue(c.srdW1, c.wv1, c.w1o + S0 * (32 * F_ * 2), c.ring + S0 * 4096);
        a_issue<0, S0>(c.srdH, c.av0, c.av1, c.a1o + S0 * 40960);
        g1_groups<S0 + 1, S1>(c);
    }
}
template <int S>
__device__ __forceinline__ void g2_first(const KbCtx& c) {
    a_issue<1, S>(c.srdA, c.av0, c.av1, c.a2o + S * 40960);
    if constexpr (S < 7) g2_first<S + 1>(c);
}
template <int CTRL> __device__ __forceinline__ f32x2 dpp_add2(f32x2 v) { return (f32x2){dpp_add<CTRL>(v.x), dpp_add<CTRL>(v.y)}; }

__global__ __launch_bounds__(256, 1) void kb_mlp(const u16* __restrict__ w1, const u16* __restrict__ b1, const u16* __restrict__ w2,
                                                 const u16* __restrict__ b2, char* ws, u16* __restrict__ out) {
    __shared__ __attribute__((aligned(16))) char smem[4 * B_REG];
    const int tid = threadIdx.x, lane = tid & 63;
    const int w = __builtin_amdgcn_readfirstlane(tid >> 6);
    STAMP(2, 0);
    const int n = lane & 15, hg = lane >> 4;
    KbCtx c;
    c.srdA = __builtin_amdgcn_make_buffer_rsrc((void*)ws, (short)0, WS_END, 0x00020000);
    u32x4 hs[3];
    {
        const unsigned v = hg * 1024 + n * 16;
        asm volatile("buffer_load_dwordx4 %0, %3, %5, %6 offen\n\tbuffer_load_dwordx4 %1, %3, %5, %7 offen\n\tbuffer_load_dwordx4 %2, %4, %5, %7 offen"
                     : "=&v"(hs[0]), "=&v"(hs[1]), "=&v"(hs[2])
                     : "v"(v), "v"(v + 4096 + (lane >> 5) * WS_END), "s"(c.srdA),
                       "s"(WS_HB + ((blockIdx.x >> 2) * 40 + 10 * w) * 1024 + (blockIdx.x & 3) * 256),
                       "s"(WS_HB + ((blockIdx.x >> 2) * 40 + 10 * w + 4) * 1024 + (blockIdx.x & 3) * 256) : "memory");
    }
    const int xcd = blockIdx.x & 7, idx = blockIdx.x >> 3;
    const bool g1 = idx < 27;
    const int t1 = (xcd >> 1) * 27 + (g1 ? idx : 0), t2 = (xcd >> 1) * 32 + idx;
    const int mt1 = t1 >> 2, dt1 = (xcd & 1) * 4 + (t1 & 3), mt2 = t2 >> 2, dt2 = (xcd & 1) * 4 + (t2 & 3);
    char* ring = smem + w * B_REG;
    c.ring = (unsigned)(size_t)(LDS_AS char*)ring;
    c.srdW1 = __builtin_amdgcn_make_buffer_rsrc((void*)w1, (short)0, g1 ? L_ * F_ * 2 : 0, 0x00020000);
    c.srdH = __builtin_amdgcn_make_buffer_rsrc((void*)ws, (short)0, g1 ? WS_END : 0, 0x00020000);
    c.srdW2 = __builtin_amdgcn_make_buffer_rsrc((void*)w2, (short)0, L_ * F_ * 2, 0x00020000);
    const bool idw = w == 3;
    c.srdL = __builtin_amdgcn_make_buffer_rsrc((void*)(idw ? ws + WS_ID : (const char*)w2), (short)0, L_ * F_ * 2, 0x00020000);
    {
        const int wr = lane >> 3, wslot = lane & 7;
        const unsigned s0 = ((((wslot >> 1) ^ ((wr >> 1) & 1)) * 2 + (wslot & 1)) * 8) * 2;
        const unsigned s1 = ((((wslot >> 1) ^ (((wr >> 1) & 1) | 2)) * 2 + (wslot & 1)) * 8) * 2;
        c.wv1[0] = wr * F_ * 2 + s0; c.wv1[1] = (wr + 8) * F_ * 2 + s1 - 1024;
        c.wv1[2] = c.wv1[0] + 16 * F_ * 2 - 2048; c.wv1[3] = c.wv1[1] + 16 * F_ * 2 - 2048;
        c.wv2[0] = wr * L_ * 2 + s0; c.wv2[1] = (wr + 8) * L_ * 2 + s1 - 1024;
        c.wv2[2] = c.wv2[0] + 16 * L_ * 2 - 2048; c.wv2[3] = c.wv2[1] + 16 * L_ * 2 - 2048;
    }
    c.av0 = lane * 16; c.av1 = lane * 16 + 4096;
    c.w1o = (w * 16 * 32 * F_ + mt1 * 64) * 2;
    c.a1o = WS_HB + (w * 16 * 40 + dt1 * 5) * 1024;
    c.w2o = (w * 14 * 32 * L_ + mt2 * 64) * 2;
    c.a2o = WS_GB + (w * 14 * 40 + dt2 * 5) * 1024;
    c.wlo = idw ? 0u : c.w2o + 12 * (32 * L_ * 2);
    c.alo = idw ? WS_HB + (mt2 * 2 * 40 + dt2 * 5) * 1024 : c.a2o + 12 * 40960;
    u32x2 bb;
    asm volatile("global_load_dwordx2 %0, %1, %2" : "=v"(bb) : "v"(hg * 8), "s"(b1 + mt1 * 64 + 16 * w) : "memory");
    asm volatile("" ::: "a0", "a159");
    g1_groups<0, PRE>(c);
    {
        const int q4 = n >> 2, p4 = n & 3;
        const int fsw = ((q4 >> 1) & 1) | ((hg & 1) << 1);
        const char* tb = ring + (8 * hg + q4) * 128 + p4 * 8;
#pragma unroll
        for (int i = 0; i < 4; ++i) c.tp[i] = tb + ((((i + w) & 3) ^ fsw) * 32);
        c.ta = ring + B_TAB + (8 * hg + (lane & 3)) * 8;
        c.tb = ring + B_TAB + 4096 + hg * 32;
        c.red = ring + B_RED + lane * 16;
    }
    const __amdgpu_buffer_rsrc_t srdG = __builtin_amdgcn_make_buffer_rsrc((void*)(ws + WS_G2), (short)0, L_ * 8, 0x00020000);
    {
        const unsigned one = (n & 1) ? 0x3f800000u : 0x3f80u;
        const int dw = (hg & 1) == (n >> 3) ? (n >> 1) & 3 : 4;
        const u32x4 sel = {dw == 0 ? one : 0u, dw == 1 ? one : 0u, dw == 2 ? one : 0u, dw == 3 ? one : 0u};
        const u32x4 z4 = {0u, 0u, 0u, 0u};
        const u32x4 selA = hg < 2 ? sel : z4, selB = hg < 2 ? z4 : sel;
        f32x4 sum, x0, x1, x2, x3, x4;
        asm volatile("s_waitcnt vmcnt(%12)\n\t"
                     "v_mfma_f32_16x16x32_bf16 %1, %6, %9, 0\n\tv_mfma_f32_16x16x32_bf16 %2, %6, %10, 0\n\t"
                     "v_mfma_f32_16x16x32_bf16 %3, %7, %9, 0\n\tv_mfma_f32_16x16x32_bf16 %4, %7, %10, 0\n\t"
                     "v_mfma_f32_16x16x32_bf16 %5, %8, %9, 0\n\tv_mfma_f32_16x16x32_bf16 %0, %6, %11, 0\n\t"
                     "v_mfma_f32_16x16x32_bf16 %0, %7, %11, %0\n\tv_mfma_f32_16x16x32_bf16 %0, %8, %11, %0\n\ts_nop 4"
                     : "=&v"(sum), "=&v"(x0), "=&v"(x1), "=&v"(x2), "=&v"(x3), "=&v"(x4)
                     : "v"(hs[0]), "v"(hs[1]), "v"(hs[2]), "v"(selA), "v"(selB), "v"(sel), "n"(9 * PRE + 1) : "memory");
        STAMP(2, 1);
        f32x4 q = (x0 * x0 + x1 * x1) + (x2 * x2 + x3 * x3) + x4 * x4;
        asm volatile("s_nop 2" : "+v"(sum), "+v"(q));
        char* sc = smem + B_TAB + 6144;
        *(f32x2*)(sc + (n & 7) * 256 + (w * 8 + hg * 2 + (n >> 3)) * 8) = (f32x2){(sum[0] + sum[1]) + (sum[2] + sum[3]), (q[0] + q[1]) + (q[2] + q[3])};
        asm volatile("s_waitcnt lgkmcnt(0)\n\ts_barrier" ::: "memory");
        if (w == 0) {
            const f32x4 p0 = *(const f32x4*)(sc + lane * 32), p1 = *(const f32x4*)(sc + lane * 32 + 16);
            f32x2 v = {(p0[0] + p0[2]) + (p1[0] + p1[2]), (p0[1] + p0[3]) + (p1[1] + p1[3])};
            v = dpp_add2<0xb1>(v);
            v = dpp_add2<0x4e>(v);
            v = dpp_add2<0x141>(v);
            const float mean = v.x * (1.0f / D_);
            const float var = fmaxf(__builtin_fmaf(-mean, mean, v.y * (1.0f / D_)), 0.f);
            const float m1 = rb(mean), d1 = rb(__builtin_amdgcn_sqrtf(rb(rb(var) + 1e-5f)));
            const float i1 = __builtin_amdgcn_rcpf(d1);
            const float m2 = rb((mean - m1) * i1), d2 = rb(__builtin_amdgcn_sqrtf(rb(rb(var * i1 * i1) + 1e-5f)));
            const float al = rb(i1 * __builtin_amdgcn_rcpf(d2));
            const float be = -__builtin_fmaf(m2, d1, m1) * al;
            asm volatile("buffer_store_dwordx2 %0, %1, %2, %3 offen sc1"
                         :: "v"((u32x2){__float_as_uint(be), __float_as_uint(al) | 1u}), "v"((lane & 7) * (L_ * 8) + lane), "s"(srdG), "s"(blockIdx.x * 64) : "memory");
            STAMP(2, 2);
        }
    }
    f32x4 acc[4][5];
    u32x4 wf[2][4];
    if (g1) {
        g1_groups<PRE, PPOS>(c);
        u32x4 g[4];
        asm volatile("s_waitcnt vmcnt(59)\n\t"
                     "buffer_load_dwordx4 %0, %4, %5, %6 offen sc1\n\tbuffer_load_dwordx4 %1, %4, %5, %6 offen offset:16 sc1\n\t"
                     "buffer_load_dwordx4 %2, %4, %5, %6 offen offset:32 sc1\n\tbuffer_load_dwordx4 %3, %4, %5, %6 offen offset:48 sc1"
                     : "=&v"(g[0]), "=&v"(g[1]), "=&v"(g[2]), "=&v"(g[3]) : "v"(lane * 64), "s"(srdG), "s"(w * 4096) : "memory");
        g1_groups<PPOS, 8>(c);
        STAMP(2, 3);
        asm volatile("s_waitcnt vmcnt(%5)" : "+v"(g[0]), "+v"(g[1]), "+v"(g[2]), "+v"(g[3]), "+v"(bb) : "n"(9 * (8 - PPOS)) : "memory");
        STAMP(2, 4);
        unsigned spins = 0;
#pragma unroll 1
        for (; spins < (1u << 16); ++spins) {
            if (__all(g[0].y & g[0].w & g[1].y & g[1].w & g[2].y & g[2].w & g[3].y & g[3].w & 1u)) break;
            asm volatile("buffer_load_dwordx4 %0, %4, %5, %6 offen sc1\n\tbuffer_load_dwordx4 %1, %4, %5, %6 offen offset:16 sc1\n\t"
                         "buffer_load_dwordx4 %2, %4, %5, %6 offen offset:32 sc1\n\tbuffer_load_dwordx4 %3, %4, %5, %6 offen offset:48 sc1\n\t"
                         "s_waitcnt vmcnt(0)"
                         : "=&v"(g[0]), "=&v"(g[1]), "=&v"(g[2]), "=&v"(g[3]) : "v"(lane * 64), "s"(srdG), "s"(w * 4096) : "memory");
        }
        STAMP(2, 5);
        {
            char* tw = ring + B_TAB + lane * 64;
#pragma unroll
            for (int k = 0; k < 4; ++k) {
                const unsigned lo = g[k].y >> 16, hi = g[k].w & 0xffff0000u;
                *(u32x4*)(tw + k * 16) = (k & 1) ? (u32x4){0u, lo, 0u, hi} : (u32x4){lo, 0u, hi, 0u};
            }
            *(u32x4*)(ring + B_TAB + 4096 + lane * 32) = (u32x4){g[0].x, g[0].z, g[1].x, g[1].z};
            *(u32x4*)(ring + B_TAB + 4096 + lane * 32 + 16) = (u32x4){g[2].x, g[2].z, g[3].x, g[3].z};
            asm volatile("" ::: "memory");
        }
        Nrm t[2];
        u32x4 nf[2][5];
        nrm_read(t[0], c, 0);
        nrm_read(t[1], c, 1);
        {
            c.bias = (f32x4){bflo(bb.x), bfhi(bb.x), bflo(bb.y), bfhi(bb.y)};
            asm volatile("" : "+v"(c.bias));
            f32x4 d[10];
            g1_mid<0, 0>(wf[0], d, t[0], c);
            asm volatile("s_nop 4");
            SB();
#pragma unroll
            for (int k = 0; k < 20; ++k) nf[0][k >> 2][k & 3] = pk2(d[k >> 1][(k & 1) * 2], d[k >> 1][(k & 1) * 2 + 1]);
            SB();
        }
        STAMP(3, 1);
        g1_step<0>(acc, nf, wf, t, c);
        STAMP(3, 2);
        f32x4 r[5];
        kb_reduce(r, acc, smem, w, lane);
        STAMP(3, 3);
        const unsigned go = WS_GB + ((mt1 * 2 + (w >> 1)) * 40 + dt1 * 5) * 1024;
        const unsigned gv = (((w & 1) * 2 + (hg >> 1)) * 16 + n) * 16 + (hg & 1) * 8;
#pragma unroll
        for (int j = 0; j < 5; ++j) {
            u32x2 o;
            o.x = pk2(gelu_tanh(r[j][0]), gelu_tanh(r[j][1]));
            o.y = pk2(gelu_tanh(r[j][2]), gelu_tanh(r[j][3]));
            __builtin_amdgcn_raw_buffer_store_b64(o, c.srdA, gv + j * 1024, go, 16);
        }
        STAMP(3, 4);
        asm volatile("s_waitcnt vmcnt(0)\n\ts_barrier" ::: "memory");
        if (tid == 0) __hip_atomic_store((gu32*)(ws + WS_FL + (dt1 * 32 + mt1) * 128), 1u, __ATOMIC_RELAXED, __HIP_MEMORY_SCOPE_AGENT);
        STAMP(3, 5);
    } else {
#pragma unroll
        for (int s = 0; s < 6; ++s) w_issue(c.srdW2, c.wv2, c.w2o + s * (32 * L_ * 2), c.ring + s * 4096);
        warm_w<1>(w2, b2, xcd, (idx - 27) * 4 + w, 20, c.ring + 9 * 4096, lane);
        vm_wait<0>();
        STAMP(3, 6);
    }
#pragma unroll
    for (int s = 6; s < 10; ++s) w_issue(c.srdW2, c.wv2, c.w2o + s * (32 * L_ * 2), c.ring + s * 4096);
    unsigned bq;
    asm volatile("global_load_ushort %0, %1, %2" : "=v"(bq) : "v"(n * 2), "s"(b2 + mt2 * 64 + 16 * w) : "memory");
    {
        const int nf = w == 3 ? 6 : 7;
        const unsigned fo = (lane < nf ? lane : 0) * 128;
        const char* fp = ws + WS_FL + (dt2 * 32 + 7 * w) * 128;
        unsigned spins = 0;
#pragma unroll 1
        for (; spins < (1u << 16); ++spins) {
            unsigned f;
            asm volatile("global_load_dword %0, %2, %3 sc1\n\ts_waitcnt vmcnt(0)" : "=&v"(f), "+v"(bq) : "v"(fo), "s"(fp) : "memory");
            if (__all(f != 0)) break;
        }
    }
    STAMP(4, 0);
    g2_first<0>(c);
    w_read<2>(wf[0], c, 0); w_read<3>(wf[0], c, 0); w_read<4>(wf[0], c, 0); w_read<5>(wf[0], c, 0);
    w_read<6>(wf[0], c, 0); w_read<7>(wf[0], c, 0); w_read<0>(wf[0], c, 0); w_read<1>(wf[0], c, 0);
    {
        const float b = bflo(bq);
        c.bias = (f32x4){b, b, b, b};
        asm volatile("" : "+v"(c.bias));
    }
    SB();
    g2_step<0>(acc, wf, c);
    STAMP(4, 2);
    f32x4 r[5];
    kb_reduce(r, acc, smem, w, lane);
    STAMP(4, 3);
    {
        u16* op = out + (size_t)(mt2 * 64 + 16 * w + n) * D_ + dt2 * 80 + 20 * hg;
        typedef unsigned u32x4u __attribute__((ext_vector_type(4), aligned(8)));
        u32x4u o0 = {pk2(r[0][0], r[0][1]), pk2(r[0][2], r[0][3]), pk2(r[1][0], r[1][1]), pk2(r[1][2], r[1][3])};
        u32x4u o1 = {pk2(r[2][0], r[2][1]), pk2(r[2][2], r[2][3]), pk2(r[3][0], r[3][1]), pk2(r[3][2], r[3][3])};
        u32x2 o2 = {pk2(r[4][0], r[4][1]), pk2(r[4][2], r[4][3])};
        *(u32x4u*)op = o0;
        *(u32x4u*)(op + 8) = o1;
        *(u32x2*)(op + 16) = o2;
    }
    STAMP(4, 4);
}

extern "C" void kernel_launch(void* const* d_in, const int* in_sizes, int n_in, void* d_out, int out_size,
                              void* d_ws, size_t ws_size, hipStream_t stream) {
    const u16* x = (const u16*)d_in[0];
    const u16* logA = (const u16*)d_in[1];
    const u16* Aim = (const u16*)d_in[2];
    const u16* Cre = (const u16*)d_in[3];
    const u16* Cim = (const u16*)d_in[4];
    const u16* logdt = (const u16*)d_in[5];
    const u16* dskip = (const u16*)d_in[6];
    const u16* w1 = (const u16*)d_in[8];
    const u16* b1 = (const u16*)d_in[9];
    const u16* w2 = (const u16*)d_in[10];
    const u16* b2 = (const u16*)d_in[11];
    char* ws = (char*)d_ws;
    ka_conv<<<256, 512, 0, stream>>>(x, logA, Aim, Cre, Cim, logdt, dskip, ws, w1, b1);
    kb_mlp<<<256, 256, 0, stream>>>(w1, b1, w2, b2, ws, (u16*)d_out);
}
```

```cpp
#include <hip/hip_runtime.h>
#include <hip/hip_bf16.h>
#include <stdint.h>
#pragma clang diagnostic ignored "-Winline-asm"

typedef unsigned short u16;
typedef __bf16 bf16x8 __attribute__((ext_vector_type(8)));
typedef __bf16 bf16x2 __attribute__((ext_vector_type(2)));
typedef short s16x4 __attribute__((ext_vector_type(4)));
typedef float f32x4 __attribute__((ext_vector_type(4)));
typedef float f32x2 __attribute__((ext_vector_type(2)));
typedef unsigned u32x4 __attribute__((ext_vector_type(4)));
typedef unsigned u32x2 __attribute__((ext_vector_type(2)));
#define LDS_AS __attribute__((address_space(3)))

#define STAMP(k, i) do { } while (0)
#define STAMPT(k, i, T) do { } while (0)

#define FSTAMP STAMP
constexpr int L_ = 2048;
constexpr int D_ = 640;
constexpr int N_ = 128;
constexpr int F_ = 1728;
constexpr unsigned SZ_ = L_ * D_ * 2;
constexpr unsigned WS_HB = 0, WS_GB = SZ_, WS_END = 2 * SZ_;
constexpr unsigned WS_ID = 2 * SZ_, WS_GR = WS_ID + 64 * L_ * 2, WS_EP = WS_GR + 32768;
constexpr unsigned WS_G2 = WS_EP + 4096, WS_FL = WS_G2 + L_ * 8;

__device__ __forceinline__ float bf2f(unsigned v) { return __uint_as_float(v << 16); }
__device__ __forceinline__ float bflo(unsigned v) { return __uint_as_float(v << 16); }
__device__ __forceinline__ float bfhi(unsigned v) { return __uint_as_float(v & 0xffff0000u); }
__device__ __forceinline__ unsigned pk2(float lo, float hi) {
    f32x2 f = {lo, hi};
    bf16x2 b = __builtin_convertvector(f, bf16x2);
    return __builtin_bit_cast(unsigned, b);
}
__device__ __forceinline__ float rb(float x) { return bflo(pk2(x, x)); }

__device__ __forceinline__ f32x4 mfma16(u32x4 a, u32x4 b, f32x4 c) {
    return __builtin_amdgcn_mfma_f32_16x16x32_bf16(__builtin_bit_cast(bf16x8, a), __builtin_bit_cast(bf16x8, b), c, 0, 0, 0);
}
__device__ __forceinline__ u32x2 tr_read(const char* p) {
    s16x4 v = __builtin_amdgcn_ds_read_tr16_b64_v4i16((LDS_AS s16x4*)p);
    return __builtin_bit_cast(u32x2, v);
}
template <int CTRL>
__device__ __forceinline__ float dpp_add(float v) {
    int t = __builtin_amdgcn_update_dpp(0, __float_as_int(v), CTRL, 0xf, 0xf, true);
    return v + __int_as_float(t);
}
__device__ __forceinline__ float row16_sum(float v) {
    v = dpp_add<0x128>(v);
    v = dpp_add<0x124>(v);
    v = dpp_add<0x122>(v);
    v = dpp_add<0x121>(v);
    return v;
}

__device__ __forceinline__ void touch4(__amdgpu_buffer_rsrc_t srd, unsigned voff, unsigned soff, unsigned m0v) {
    asm volatile("s_mov_b32 m0, %0\n\ts_nop 0\n\tbuffer_load_dword %1, %2, %3 offen lds"
                 :: "s"(m0v), "v"(voff), "s"(srd), "s"(soff) : "m0", "memory");
}
template <int MODE>
__device__ __forceinline__ void warm_w(const u16* W, const u16* bias, int xcd, int gw, int nw, unsigned lds, int lane) {
    constexpr int LDW = MODE == 0 ? F_ : L_;
    const __amdgpu_buffer_rsrc_t srdW = __builtin_amdgcn_make_buffer_rsrc((void*)W, (short)0, L_ * F_ * 2, 0x00020000);
    if (gw == 0) touch4(__builtin_amdgcn_make_buffer_rsrc((void*)bias, (short)0, LDW * 2, 0x00020000), lane * 128, 0, lds);
    const int mt0 = MODE == 0 ? ((xcd >> 1) * 27) >> 2 : (xcd >> 1) * 8;
    const unsigned voff = ((lane >> 3) * LDW + (lane & 7) * 64) * 2;
    for (int j = gw; j < (MODE == 0 ? 256 : 224); j += nw) {
        const int s = j >> 4, w = (j >> 2) & 3;
        const int ks = MODE == 0 ? w * 16 + s : w * 14 + s;
        if (MODE != 0 && ks >= 54) continue;
        touch4(srdW, voff, ((ks * 32 + (j & 3) * 8) * LDW + mt0 * 64) * 2, lds);
    }
}

constexpr int KA_ROWB = 528;
constexpr int KA_LO = 32 * KA_ROWB;
constexpr int KA_HI0 = 4 * KA_LO;
constexpr int KA_HI = 13312;
constexpr int KA_MB = 20000;
constexpr int CV_COPY = 4160;
constexpr int CV_UB = 4608;
constexpr int KA_U = KA_HI0 + 4 * KA_HI;
#define XC1 4
#define XCS 3
#define GFIRST " sc1"
typedef __attribute__((address_space(1))) unsigned gu32;

__device__ __forceinline__ void cm4(f32x2& r0, f32x2 a0, f32x2 b0, f32x2& r1, f32x2 a1, f32x2 b1,
                                    f32x2& r2, f32x2 a2, f32x2 b2, f32x2& r3, f32x2 a3, f32x2 b3) {
    asm("v_pk_mul_f32 %0, %4, %5 op_sel:[0,0] op_sel_hi:[1,0]\n\t"
        "v_pk_mul_f32 %1, %6, %7 op_sel:[0,0] op_sel_hi:[1,0]\n\t"
        "v_pk_mul_f32 %2, %8, %9 op_sel:[0,0] op_sel_hi:[1,0]\n\t"
        "v_pk_mul_f32 %3, %10, %11 op_sel:[0,0] op_sel_hi:[1,0]\n\t"
        "v_pk_fma_f32 %0, %4, %5, %0 op_sel:[1,1,0] op_sel_hi:[0,1,1] neg_lo:[1,0,0]\n\t"
        "v_pk_fma_f32 %1, %6, %7, %1 op_sel:[1,1,0] op_sel_hi:[0,1,1] neg_lo:[1,0,0]\n\t"
        "v_pk_fma_f32 %2, %8, %9, %2 op_sel:[1,1,0] op_sel_hi:[0,1,1] neg_lo:[1,0,0]\n\t"
        "v_pk_fma_f32 %3, %10, %11, %3 op_sel:[1,1,0] op_sel_hi:[0,1,1] neg_lo:[1,0,0]"
        : "=&v"(r0), "=&v"(r1), "=&v"(r2), "=&v"(r3)
        : "v"(a0), "v"(b0), "v"(a1), "v"(b1), "v"(a2), "v"(b2), "v"(a3), "v"(b3));
}
constexpr int ka_off(int r) { return r < 32 ? r * KA_ROWB : KA_HI0 - KA_MB + (r - 32) * KA_ROWB; }
template <int R0, int R1, int R2, int R3>
__device__ __forceinline__ void put4(const unsigned (&m0v)[2], f32x2 v0, f32x2 v1, f32x2 v2, f32x2 v3) {
    static_assert((R0 < 32) == (R1 < 32) && (R0 < 32) == (R2 < 32) && (R0 < 32) == (R3 < 32), "");
    unsigned t0, t1, t2, t3;
    asm volatile("s_mov_b32 m0, %4\n\t"
                 "v_cvt_pk_bf16_f32 %0, %5, %6\n\tv_cvt_pk_bf16_f32 %1, %7, %8\n\t"
                 "v_cvt_pk_bf16_f32 %2, %9, %10\n\tv_cvt_pk_bf16_f32 %3, %11, %12\n\t"
                 "ds_write_addtid_b32 %0 offset:%13\n\tds_write_addtid_b32 %1 offset:%14\n\t"
                 "ds_write_addtid_b32 %2 offset:%15\n\tds_write_addtid_b32 %3 offset:%16"
                 : "=&v"(t0), "=&v"(t1), "=&v"(t2), "=&v"(t3)
                 : "s"(m0v[R0 >= 32]), "v"(v0.x), "v"(v0.y), "v"(v1.x), "v"(v1.y), "v"(v2.x), "v"(v2.y), "v"(v3.x), "v"(v3.y),
                   "n"(ka_off(R0)), "n"(ka_off(R1)), "n"(ka_off(R2)), "n"(ka_off(R3))
                 : "m0", "memory");
}
__device__ __forceinline__ f32x2 cexp_p(float zr, float zi, float p) {
    const float mag = __builtin_amdgcn_exp2f(zr * p);
    const float rev = __builtin_amdgcn_fractf(zi * p);
    f32x2 r;
    r.x = mag * __builtin_amdgcn_cosf(rev);
    r.y = mag * __builtin_amdgcn_sinf(rev);
    return r;
}
__device__ __forceinline__ float wave_sum(float v) {
    v = row16_sum(v);
    const int i = __float_as_int(v);
    return (__int_as_float(__builtin_amdgcn_readlane(i, 0)) + __int_as_float(__builtin_amdgcn_readlane(i, 16))) +
           (__int_as_float(__builtin_amdgcn_readlane(i, 32)) + __int_as_float(__builtin_amdgcn_readlane(i, 48)));
}
__device__ __forceinline__ unsigned next_lane(unsigned v, unsigned last) {
    return (unsigned)__builtin_amdgcn_update_dpp((int)last, (int)v, 0x130, 0xf, 0xf, false);
}
__device__ __forceinline__ void put_copies(char* p, unsigned r0, unsigned r1, unsigned r2, unsigned r3, unsigned r4, unsigned r5) {
    const unsigned s0 = __builtin_amdgcn_alignbit(r1, r0, 16), s1 = __builtin_amdgcn_alignbit(r2, r1, 16),
                   s2 = __builtin_amdgcn_alignbit(r3, r2, 16), s3 = __builtin_amdgcn_alignbit(r4, r3, 16),
                   s4 = __builtin_amdgcn_alignbit(r5, r4, 16);
    *(u32x4*)(p + 0 * CV_COPY) = (u32x4){r0, r1, r2, r3};
    *(u32x4*)(p + 1 * CV_COPY) = (u32x4){s0, s1, s2, s3};
    *(u32x4*)(p + 2 * CV_COPY) = (u32x4){r1, r2, r3, r4};
    *(u32x4*)(p + 3 * CV_COPY) = (u32x4){s1, s2, s3, s4};
}
template <int O> __device__ __forceinline__ void rd64(u32x2& v, unsigned a) {
    asm volatile("ds_read_b64 %0, %1 offset:%2" : "=v"(v) : "v"(a), "n"(O) : "memory");
}
template <int O> __device__ __forceinline__ void rd128(u32x4& v, unsigned a) {
    asm volatile("ds_read_b128 %0, %1 offset:%2" : "=v"(v) : "v"(a), "n"(O) : "memory");
}
struct CvFrag { u32x2 lo[8], hi[8]; u32x4 b[8]; };
__device__ __forceinline__ void cv_wait(CvFrag& f) {
    asm volatile("s_waitcnt lgkmcnt(0)"
                 : "+v"(f.lo[0]), "+v"(f.lo[1]), "+v"(f.lo[2]), "+v"(f.lo[3]), "+v"(f.lo[4]), "+v"(f.lo[5]), "+v"(f.lo[6]), "+v"(f.lo[7]),
                   "+v"(f.hi[0]), "+v"(f.hi[1]), "+v"(f.hi[2]), "+v"(f.hi[3]), "+v"(f.hi[4]), "+v"(f.hi[5]), "+v"(f.hi[6]), "+v"(f.hi[7]),
                   "+v"(f.b[0]), "+v"(f.b[1]), "+v"(f.b[2]), "+v"(f.b[3]), "+v"(f.b[4]), "+v"(f.b[5]), "+v"(f.b[6]), "+v"(f.b[7])
                 :: "memory");
}
template <int T>
__device__ __forceinline__ void cv_mma(const CvFrag& f, f32x4 (&acc)[8]) {
    constexpr int q = T < 1 ? 0 : T < 3 ? 1 : T < 6 ? 2 : T < 10 ? 3 : T < 15 ? 4 : T < 21 ? 5 : T < 28 ? 6 : 7;
    constexpr int g1 = T - q * (q + 1) / 2;
    const u32x4 a = {f.lo[g1].x, f.lo[g1].y, f.hi[g1].x, f.hi[g1].y};
    acc[q] = mfma16(a, f.b[q - g1], acc[q]);
}
template <int G0, int R>
__device__ __forceinline__ void cv_read(CvFrag& f, unsigned aa, unsigned ba) {
    constexpr int G1 = R / 3;
    if constexpr (R % 3 == 0) rd64<(63 - 8 * G1 - G0) * 64>(f.lo[G1], aa);
    else if constexpr (R % 3 == 1) rd64<(63 - 8 * G1 - G0) * 64 + 8>(f.hi[G1], aa);
    else rd128<(8 * G1 - G0 + 7) * 64>(f.b[G1], ba);
}
template <int G0, int T>
__device__ __forceinline__ void cv_group(const CvFrag& cur, CvFrag& nxt, f32x4 (&acc)[8], unsigned aa, unsigned ba) {
    cv_mma<T>(cur, acc);
    __builtin_amdgcn_sched_barrier(0);
    if constexpr (G0 < 7 && T < 30 && T % 5 != 4) {
        cv_read<G0 + 1, T - T / 5>(nxt, aa, ba);
        __builtin_amdgcn_sched_barrier(0);
    }
    if constexpr (T < 35) cv_group<G0, T + 1>(cur, nxt, acc, aa, ba);
}
template <int G0>
__device__ __forceinline__ void cv_round(CvFrag& cur, CvFrag& nxt, f32x4 (&acc)[8], unsigned aa, unsigned ba) {
    cv_wait(cur);
    if constexpr (G0 == 5) __builtin_amdgcn_s_barrier();
    __builtin_amdgcn_sched_barrier(0);
    cv_group<G0, 0>(cur, nxt, acc, aa, ba);
    if constexpr (G0 < 7) cv_round<G0 + 1>(nxt, cur, acc, aa, ba);
}
template <int I>
__device__ __forceinline__ void cv_first(CvFrag& f, unsigned aa, unsigned ba) {
    cv_read<0, I>(f, aa, ba);
    if constexpr (I < 23) cv_first<I + 1>(f, aa, ba);
}

template <class Hook>
__device__ __forceinline__ void ka_chain(const unsigned (&m0v)[2], float p_la, float p_ai, float p_cr, float p_ci, float p_dt, Hook hook) {
    const float dt = rb(__builtin_amdgcn_exp2f(p_dt * 1.44269504088896341f));
    const float ar = -rb(__builtin_amdgcn_exp2f(p_la * 1.44269504088896341f));
    const float zr = ar * dt * 1.44269504088896341f, zi = p_ai * dt * 0.15915494309189535f;
    f32x2 a[17], b[16], e[8], F1, F2, F4, F8, r0, r1, r2, r3;
    a[0] = (f32x2){2.0f * (p_cr * dt), -2.0f * (p_ci * dt)};
    e[1] = cexp_p(zr, -zi, 1.f);
    b[1] = cexp_p(zr, zi, 8.f);
    F1 = cexp_p(zr, -zi, 128.f);
    cm4(e[2], e[1], e[1], b[2], b[1], b[1], F2, F1, F1, a[1], a[0], F1);
    cm4(e[4], e[2], e[2], b[4], b[2], b[2], F4, F2, F2, e[3], e[2], e[1]);
    put4<41, 42, 43, 45>(m0v, (f32x2){1.f, 0.f}, b[1], b[2], b[4]);
    hook(0);
    cm4(a[2], a[0], F2, a[3], a[1], F2, b[3], b[2], b[1], r0, a[0], e[1]);
    cm4(F8, F4, F4, b[8], b[4], b[4], r1, a[1], e[1], r2, a[0], e[2]);
    put4<16, 20, 1, 9>(m0v, a[2], a[3], r0, r1);
    hook(1);
    cm4(a[4], a[0], F4, a[5], a[1], F4, a[6], a[2], F4, a[7], a[3], F4);
    put4<24, 26, 28, 30>(m0v, a[4], a[5], a[6], a[7]);
    hook(2);
    cm4(e[5], e[4], e[1], e[6], e[4], e[2], e[7], e[4], e[3], r3, a[1], e[2]);
    put4<0, 8, 2, 10>(m0v, a[0], a[1], r2, r3);
    hook(3);
    cm4(b[5], b[4], b[1], b[6], b[4], b[2], b[7], b[4], b[3], r0, a[0], e[3]);
    put4<44, 49, 46, 47>(m0v, b[3], b[8], b[5], b[6]);
    hook(4);
    cm4(a[8], a[0], F8, a[9], a[1], F8, a[10], a[2], F8, a[11], a[3], F8);
    put4<32, 33, 34, 35>(m0v, a[8], a[9], a[10], a[11]);
    hook(5);
    cm4(a[12], a[4], F8, a[13], a[5], F8, a[14], a[6], F8, a[15], a[7], F8);
    put4<36, 37, 38, 39>(m0v, a[12], a[13], a[14], a[15]);
    hook(6);
    cm4(a[16], a[8], F8, r1, a[1], e[3], r2, a[0], e[4], r3, a[1], e[4]);
    put4<3, 11, 4, 12>(m0v, r0, r1, r2, r3);
    hook(7);
    cm4(b[9], b[8], b[1], b[10], b[8], b[2], b[11], b[8], b[3], b[12], b[8], b[4]);
    put4<50, 51, 52, 53>(m0v, b[9], b[10], b[11], b[12]);
    hook(8);
    cm4(b[13], b[8], b[5], b[14], b[8], b[6], b[15], b[8], b[7], r0, a[0], e[5]);
    put4<54, 55, 56, 48>(m0v, b[13], b[14], b[15], b[7]);
    asm volatile("ds_write_addtid_b32 %0 offset:%1" :: "v"(pk2(a[16].x, a[16].y)), "n"(ka_off(40)) : "memory");
    hook(9);
    cm4(r1, a[1], e[5], r2, a[0], e[6], r3, a[1], e[6], F1, a[0], e[7]);
    put4<5, 13, 6, 14>(m0v, r0, r1, r2, r3);
    hook(10);
    cm4(r0, a[1], e[7], r1, a[2], e[2], r2, a[3], e[2], F2, a[2], e[4]);
    put4<7, 15, 17, 21>(m0v, F1, r0, r1, r2);
    hook(11);
    cm4(r3, a[3], e[4], F1, a[2], e[6], r0, a[3], e[6], r1, a[4], e[4]);
    put4<18, 22, 19, 23>(m0v, F2, r3, F1, r0);
    hook(12);
    cm4(r2, a[5], e[4], r3, a[6], e[4], F1, a[7], e[4], F2, a[7], e[4]);
    put4<25, 27, 29, 31>(m0v, r1, r2, r3, F1);
    asm volatile("s_waitcnt lgkmcnt(0)" ::: "memory");
}
__device__ __forceinline__ void unpack4(float (&o)[4], u32x2 v) {
    asm volatile("v_lshlrev_b32 %0, 16, %4\n\tv_and_b32 %1, 0xffff0000, %4\n\tv_lshlrev_b32 %2, 16, %5\n\tv_and_b32 %3, 0xffff0000, %5"
                 : "=&v"(o[0]), "=&v"(o[1]), "=&v"(o[2]), "=&v"(o[3]) : "v"(v.x), "v"(v.y));
}

__global__ __launch_bounds__(512, 2) void ka_conv(const u16* __restrict__ x, const u16* __restrict__ logA,
                                                  const u16* __restrict__ Aim, const u16* __restrict__ Cre,
                                                  const u16* __restrict__ Cim, const u16* __restrict__ logdt,
                                                  const u16* __restrict__ dskip, char* ws,
                                                  const u16* __restrict__ w1, const u16* __restrict__ b1,
                                                  const u16* __restrict__ w2, const u16* __restrict__ b2) {
    __shared__ __attribute__((aligned(16))) char smem[KA_U + 4 * CV_UB];
    const int tid = threadIdx.x, lane = tid & 63;
    const int w = __builtin_amdgcn_readfirstlane(tid >> 6);
    STAMP(0, 0);
    asm volatile("" :: "s"(x), "s"(logA), "s"(Aim), "s"(Cre), "s"(Cim), "s"(logdt), "s"(dskip), "s"(ws));
    const int xcd = blockIdx.x & 7, idx = blockIdx.x >> 3;
    const bool conv = idx < 20;
    const int grp = ((xcd & 1) * 4 + (xcd >> 1)) * 20 + (conv ? idx : 0);
    const int d0 = grp * 4;
    const int row = (xcd * 32 + idx) * 8 + w;
    unsigned sv[5], epoch, pp[5];
    float pf[5];
    asm volatile("global_load_dword %0, %11, %12\n\tglobal_load_dword %1, %11, %12 offset:256\n\t"
                 "global_load_dword %2, %11, %12 offset:512\n\tglobal_load_dword %3, %11, %12 offset:768\n\t"
                 "global_load_dword %4, %11, %12 offset:1024\n\tglobal_load_dword %5, %13, %14\n\t"
                 "global_load_ushort %6, %15, %16\n\tglobal_load_ushort %7, %15, %17\n\t"
                 "global_load_ushort %8, %15, %18\n\tglobal_load_ushort %9, %15, %19\n\t"
                 "global_load_ushort %10, %13, %20"
                 : "=&v"(sv[0]), "=&v"(sv[1]), "=&v"(sv[2]), "=&v"(sv[3]), "=&v"(sv[4]), "=&v"(epoch),
                   "=&v"(pp[0]), "=&v"(pp[1]), "=&v"(pp[2]), "=&v"(pp[3]), "=&v"(pp[4])
                 : "v"(lane * 4), "s"(x + (size_t)row * D_), "v"(0), "s"(ws + WS_EP),
                   "v"((d0 * N_ + tid) * 2), "s"(logA), "s"(Aim), "s"(Cre), "s"(Cim), "s"(logdt + d0 + (w >> 1))
                 : "memory");
    const int t = tid & 255;
    const __amdgpu_buffer_rsrc_t srdG = __builtin_amdgcn_make_buffer_rsrc((void*)(ws + WS_GR), (short)0, L_ * 8, 0x00020000);
    const __amdgpu_buffer_rsrc_t srdX = __builtin_amdgcn_make_buffer_rsrc((void*)x, (short)0, SZ_, 0x00020000);
    const int owner = conv ? w >> 2 : 0;
    u32x2 xc[8], xd;
    asm volatile("s_barrier\n\ts_cmp_eq_u32 %5, 0\n\ts_cbranch_scc1 .Lka_a%=\n\t"
                 "global_load_dwordx2 %4, %6, %7\n\t"
                 "buffer_load_dwordx2 %0, %8, %9, %10 offen\n\tbuffer_load_dwordx2 %1, %8, %9, %10 offen offset:1280\n\t"
                 "buffer_load_dwordx2 %2, %8, %9, %10 offen offset:2560\n\tbuffer_load_dwordx2 %3, %8, %9, %10 offen offset:3840\n\t"
                 "s_waitcnt vmcnt(5)\n\ts_branch .Lka_b%=\n"
                 ".Lka_a%=:\n\ts_waitcnt vmcnt(0)\n"
                 ".Lka_b%=:"
                 : "=&v"(xc[0]), "=&v"(xc[1]), "=&v"(xc[2]), "=&v"(xc[3]), "=&v"(xd)
                 : "s"(owner), "v"(0), "s"(dskip + d0), "v"(t * (8 * D_ * 2)), "s"(srdX), "s"(d0 * 2) : "scc", "memory");
    unsigned etag;
    {
        float v[12];
        unpack4(*(float(*)[4])v, (u32x2){sv[0], sv[1]});
        unpack4(*(float(*)[4])(v + 4), (u32x2){sv[2], sv[3]});
        asm volatile("v_lshlrev_b32 %0, 16, %3\n\tv_and_b32 %1, 0xffff0000, %3\n\tv_xor_b32 %2, 0x9e3779b9, %4"
                     : "=&v"(v[8]), "=&v"(v[9]), "=&v"(etag) : "v"(sv[4]), "v"(epoch));
        const float s = ((v[0] + v[1]) + (v[2] + v[3])) + ((v[4] + v[5]) + (v[6] + v[7])) + (v[8] + v[9]);
        const float mean = wave_sum(s) * (1.0f / D_);
        float q0 = 0.f, q1 = 0.f;
#pragma unroll
        for (int j = 0; j < 5; ++j) {
            const float a = v[2 * j] - mean, b = v[2 * j + 1] - mean;
            q0 = __builtin_fmaf(a, a, q0);
            q1 = __builtin_fmaf(b, b, q1);
        }
        const float q = wave_sum(q0 + q1);
        const float den = __builtin_amdgcn_sqrtf(rb(rb(q * (1.0f / D_)) + 1e-5f));
        asm volatile("buffer_store_dwordx2 %10, %11, %12, %13 offen sc1\n\t"
                     "v_lshlrev_b32 %0, 16, %5\n\tv_lshlrev_b32 %1, 16, %6\n\tv_lshlrev_b32 %2, 16, %7\n\t"
                     "v_lshlrev_b32 %3, 16, %8\n\tv_lshlrev_b32 %4, 16, %9"
                     : "=&v"(pf[0]), "=&v"(pf[1]), "=&v"(pf[2]), "=&v"(pf[3]), "=&v"(pf[4])
                     : "v"(pp[0]), "v"(pp[1]), "v"(pp[2]), "v"(pp[3]), "v"(pp[4]),
                       "v"((u32x2){pk2(mean, den), etag * 0x85ebca6bu}), "v"(lane * (L_ * 8)), "s"(srdG), "s"(row * 8) : "memory");
    }
    STAMP(0, 1);
    STAMPT(5, 4, 448);
    if (!conv) {
        const unsigned lds = (unsigned)(size_t)(LDS_AS char*)(smem + w * 1024);
        warm_w<0>(w1, b1, xcd, (idx - 20) * 8 + w, 96, lds, lane);
        if (idx == 31 && tid < 256) {
            const int r = xcd * 8 + (tid >> 5), c = tid & 31;
            *(unsigned*)(ws + WS_ID + r * (L_ * 2) + 4 * c) = (r >> 1) == c ? ((r & 1) ? 0x3f800000u : 0x3f80u) : 0u;
        }
        if (idx == 30 && tid < 128) *(u32x4*)(ws + WS_G2 + (xcd * 128 + tid) * 16) = (u32x4){0u, 0u, 0u, 0u};
        if (idx == 29 && tid < 32) *(unsigned*)(ws + WS_FL + (xcd * 32 + tid) * 128) = 0u;
        asm volatile("s_waitcnt vmcnt(0)" ::: "memory");
        STAMP(0, 7);
        return;
    }
    const unsigned lds0 = (unsigned)(size_t)(LDS_AS char*)smem;
    const int m = lane & 15, hg = lane >> 4;
    char* mine = smem + (w & 3) * KA_LO;
    char* minh = smem + KA_HI0 + (w & 3) * KA_HI;
    const unsigned m0v[2] = {lds0 + (w >> 1) * KA_LO + (w & 1) * 256, lds0 + KA_MB + (w >> 1) * KA_HI + (w & 1) * 256};
    ka_chain(m0v, pf[0], pf[1], pf[2], pf[3], pf[4], [&](int i) {
        if (i % XCS == 0 && i / XCS < 4)
            asm volatile("s_cmp_eq_u32 %1, 0\n\ts_cbranch_scc1 .Lka_c%=\n\tbuffer_load_dwordx2 %0, %2, %3, %4 offen offset:%5\n.Lka_c%=:"
                         : "=v"(xc[4 + i / XCS])
                         : "s"(owner), "v"(t * (8 * D_ * 2)), "s"(srdX), "s"(d0 * 2 + 4 * D_ * 2), "n"((i / XCS) * (D_ * 2)) : "scc", "memory");
    });
    STAMP(0, 2);
    STAMPT(1, 0, 448);
    __builtin_amdgcn_s_barrier();
    if (w < 4) {
        STAMPT(1, 1, 0);
        u16* kd = (u16*)(minh + 8192);
        {
            const char* fb = mine + m * KA_ROWB + hg * 16;
            const char* fh = minh + m * KA_ROWB + hg * 16;
            u32x4 fa[8][3], fl[8];
#pragma unroll
            for (int ks = 0; ks < 8; ++ks) {
                fl[ks] = *(const u32x4*)(fh + 9 * KA_ROWB + ks * 64);
                fa[ks][0] = *(const u32x4*)(fb + ks * 64);
                fa[ks][1] = *(const u32x4*)(fb + 16 * KA_ROWB + ks * 64);
                fa[ks][2] = *(const u32x4*)(fh + ks * 64);
            }
            __builtin_amdgcn_sched_barrier(0);
            f32x4 acc0 = {0.f, 0.f, 0.f, 0.f}, acc1 = acc0, acc2 = acc0;
#pragma unroll
            for (int ks = 0; ks < 8; ++ks) {
                acc0 = mfma16(fa[ks][0], fl[ks], acc0);
                acc1 = mfma16(fa[ks][1], fl[ks], acc1);
                acc2 = mfma16(fa[ks][2], fl[ks], acc2);
            }
            if (acc0[0] == 1234.5f) STAMPT(1, 7, 0);
            if (acc1[0] == 1234.5f) STAMPT(1, 7, 0);
            if (acc2[0] == 1234.5f) STAMPT(1, 7, 0);
            STAMPT(1, 2, 0);
            const int c = m;
            *(u32x2*)(kd + 128 * (hg >> 1) + 4 * (hg & 1) + 8 * c) = (u32x2){pk2(acc0[0], acc0[1]), pk2(acc0[2], acc0[3])};
            const unsigned p01 = pk2(acc1[0], acc1[1]), p23 = pk2(acc1[2], acc1[3]);
            const int i0 = hg < 2 ? 256 + 64 * hg + 4 * c : 256 + 64 * hg + 2 * c;
            *(unsigned*)(kd + i0) = p01;
            *(unsigned*)(kd + i0 + (hg < 2 ? 2 : 32)) = p23;
            const unsigned q01 = pk2(acc2[0], acc2[1]), q23 = pk2(acc2[2], acc2[3]);
            kd[512 + 64 * hg + c] = (u16)(q01 & 0xffff);
            kd[528 + 64 * hg + c] = (u16)(q01 >> 16);
            kd[544 + 64 * hg + c] = (u16)(q23 & 0xffff);
            kd[560 + 64 * hg + c] = (u16)(q23 >> 16);
            asm volatile("" ::: "memory");
        }
        const unsigned* kw = (const unsigned*)kd;
        const unsigned dw0 = kw[319 - lane], dw1 = kw[320 - lane];
        const unsigned cw0 = kw[255 - lane], cw1 = kw[256 - lane];
        const bool isA = lane >= 32;
        const char* bp = (const char*)kd + (isA ? 16 * (63 - lane) : 256 + 8 * (63 - lane));
        const u32x2 b01 = *(const u32x2*)bp, b23 = *(const u32x2*)(bp + 8);
        if (dw0 + dw1 + cw0 + cw1 + b01.x + b23.x == 1234) STAMPT(1, 7, 0);
        STAMPT(1, 3, 0);
        const unsigned P = __builtin_amdgcn_perm(0, dw1, 0x01000100), Q = __builtin_amdgcn_perm(0, dw0, 0x03020302),
                       M = __builtin_amdgcn_perm(0, dw0, 0x01000302), Z = __builtin_amdgcn_perm(0, dw0, 0x01000100);
        const unsigned c0 = __builtin_amdgcn_perm(0, cw1, 0x01000100), c1 = __builtin_amdgcn_perm(0, cw0, 0x03020302),
                       c2 = __builtin_amdgcn_perm(0, cw0, 0x01000302), c3 = __builtin_amdgcn_perm(0, cw0, 0x01000100);
        const unsigned o0 = __builtin_amdgcn_perm(b23.x, isA ? b23.y : b01.y, isA ? 0x01000302u : 0x03020504u),
                       o1 = __builtin_amdgcn_perm(b23.x, b01.y, isA ? 0x05040706u : 0x01000100u),
                       o2 = __builtin_amdgcn_perm(b01.y, b01.x, isA ? 0x05040706u : 0x03020504u),
                       o3 = __builtin_amdgcn_perm(0, b01.x, isA ? 0x01000302u : 0x01000100u);
        put_copies(mine + 3072 + lane * 16, o0, o1, o2, o3, next_lane(o0, 0), next_lane(o1, 0));
        put_copies(mine + 2048 + lane * 16, c0, c1, c2, c3, next_lane(c0, __builtin_amdgcn_readlane(o0, 0)),
                   next_lane(c1, __builtin_amdgcn_readlane(o1, 0)));
        put_copies(mine + lane * 32, P, P, Q, Q, Q, M);
        put_copies(mine + lane * 32 + 16, Q, M, Z, Z, next_lane(P, __builtin_amdgcn_readlane(c0, 0)),
                   next_lane(P, __builtin_amdgcn_readlane(c1, 0)));
        if (lane < 8) *(u32x4*)(mine + (lane >> 1) * CV_COPY + 4096 + (lane & 1) * 16) = (u32x4){0u, 0u, 0u, 0u};
        STAMP(0, 3);
        __syncthreads();
        STAMP(0, 4);
        const unsigned aa = (unsigned)(size_t)(LDS_AS char*)(mine + (3 - (m & 3)) * CV_COPY + 2 * (2028 - 32 * 63 - 4 * (m >> 2) + 8 * hg));
        const unsigned ba = (unsigned)(size_t)(LDS_AS char*)(smem + KA_U + w * CV_UB + (2 * m + hg) * 16);
        f32x4 acc[8];
#pragma unroll
        for (int q = 0; q < 8; ++q) acc[q] = (f32x4){0.f, 0.f, 0.f, 0.f};
        CvFrag f0, f1;
        cv_first<0>(f0, aa, ba);
        cv_round<0>(f0, f1, acc, aa, ba);
        STAMP(0, 5);
        const char* rp = minh + (16 * m + 4 * hg) * 4;
        char* hp = mine + (16 * m + 4 * hg) * 2;
#pragma unroll
        for (int q = 0; q < 8; ++q) {
            const f32x4 h = acc[q] + *(const f32x4*)(rp + q * 1024);
            *(u32x2*)(hp + q * 512) = (u32x2){pk2(h[0], h[1]), pk2(h[2], h[3])};
        }
    } else {
        const unsigned tag = etag * 0x85ebca6bu;
        u32x4 g[4];
        asm volatile("buffer_load_dwordx4 %0, %4, %5, 0 offen" GFIRST "\n\tbuffer_load_dwordx4 %1, %4, %5, 0 offen offset:16" GFIRST "\n\t"
                     "buffer_load_dwordx4 %2, %4, %5, 0 offen offset:32" GFIRST "\n\tbuffer_load_dwordx4 %3, %4, %5, 0 offen offset:48" GFIRST "\n\t"
                     "s_waitcnt vmcnt(4)"
                     : "=&v"(g[0]), "=&v"(g[1]), "=&v"(g[2]), "=&v"(g[3]) : "v"(t * 64), "s"(srdG) : "memory");
        float xf[8][4], dk[4];
#pragma unroll
        for (int k = 0; k < 8; ++k) unpack4(xf[k], xc[k]);
        unpack4(dk, xd);
        if (lane < 30) *(u32x4*)(smem + KA_U + (w - 4) * CV_UB + lane * 16) = (u32x4){0u, 0u, 0u, 0u};
        STAMPT(1, 4, 256);
        asm volatile("s_waitcnt vmcnt(0)" : "+v"(g[0]), "+v"(g[1]), "+v"(g[2]), "+v"(g[3]) :: "memory");
        unsigned spins = 0;
#pragma unroll 1
        for (; spins < (1u << 16); ++spins) {
            const bool bad = ((g[0].y ^ tag) | (g[0].w ^ tag) | (g[1].y ^ tag) | (g[1].w ^ tag) |
                              (g[2].y ^ tag) | (g[2].w ^ tag) | (g[3].y ^ tag) | (g[3].w ^ tag)) != 0;
            if (!__any(bad)) break;
            if (bad)
                asm volatile("buffer_load_dwordx4 %0, %4, %5, 0 offen sc1\n\tbuffer_load_dwordx4 %1, %4, %5, 0 offen offset:16 sc1\n\t"
                             "buffer_load_dwordx4 %2, %4, %5, 0 offen offset:32 sc1\n\tbuffer_load_dwordx4 %3, %4, %5, 0 offen offset:48 sc1\n\t"
                             "s_waitcnt vmcnt(0)"
                             : "+v"(g[0]), "+v"(g[1]), "+v"(g[2]), "+v"(g[3]) : "v"(t * 64), "s"(srdG) : "memory");
        }
        STAMPT(1, 5, 256);
        unsigned up[4][4];
#pragma unroll
        for (int k = 0; k < 4; ++k) {
            const float i0 = __builtin_amdgcn_rcpf(bfhi(g[k].x)), i1 = __builtin_amdgcn_rcpf(bfhi(g[k].z));
            const float m0 = bflo(g[k].x), m1 = bflo(g[k].z);
#pragma unroll
            for (int c = 0; c < 4; ++c) up[c][k] = pk2((xf[2 * k][c] - m0) * i0, (xf[2 * k + 1][c] - m1) * i1);
        }
#pragma unroll
        for (int c = 0; c < 4; ++c)
            *(u32x4*)(smem + KA_U + c * CV_UB + 480 + t * 16) = (u32x4){up[c][0], up[c][1], up[c][2], up[c][3]};
        STAMPT(1, 6, 256);
        __syncthreads();
        if (blockIdx.x == 0 && tid == 256)
            __hip_atomic_store((gu32*)(ws + WS_EP), (etag ^ 0x9e3779b9u) + 1u, __ATOMIC_RELAXED, __HIP_MEMORY_SCOPE_AGENT);
#pragma unroll
        for (int c = 0; c < 4; ++c) {
            f32x4 r0, r1;
#pragma unroll
            for (int k = 0; k < 2; ++k) {
                r0[2 * k] = __builtin_fmaf(bflo(up[c][k]), dk[c], xf[2 * k][c]);
                r0[2 * k + 1] = __builtin_fmaf(bfhi(up[c][k]), dk[c], xf[2 * k + 1][c]);
                r1[2 * k] = __builtin_fmaf(bflo(up[c][2 + k]), dk[c], xf[4 + 2 * k][c]);
                r1[2 * k + 1] = __builtin_fmaf(bfhi(up[c][2 + k]), dk[c], xf[5 + 2 * k][c]);
            }
            *(f32x4*)(smem + KA_HI0 + c * KA_HI + t * 32) = r0;
            *(f32x4*)(smem + KA_HI0 + c * KA_HI + t * 32 + 16) = r1;
        }
        asm volatile("s_waitcnt lgkmcnt(0)" ::: "memory");
        __builtin_amdgcn_s_barrier();
    }
    __syncthreads();
    {
        const int ch = tid & 3;
        const int dt = grp / 20, rem = grp % 20, mq = rem / 5, j = rem % 5;
#pragma unroll
        for (int i = 0; i < 2; ++i) {
            const int piece = (tid >> 2) + 128 * i;
            const int unit = ((piece >> 2) * 40 + dt * 5 + j) * 64 + (piece & 3) * 16 + 4 * mq + ch;
            *(u32x4*)(ws + WS_HB + (size_t)unit * 16) = *(const u32x4*)(smem + ch * KA_LO + piece * 16);
        }
    }
    STAMP(0, 6);
}

constexpr int B_REG = 40960;
constexpr int B_RED = 24576;
constexpr int B_TAB = 32768;
#define PPOS 6
#define NPRO 8
#define RSTEPS 10
#define PLATE 2
#define EPW 2
#define G2D 4
#define PRE 2

__device__ __forceinline__ float gelu_tanh(float x) {
    const float p = __builtin_fmaf(x * x, -2.302208198f * 0.044715f, -2.302208198f);
    const float e = __builtin_amdgcn_exp2f(p * x);
    return x * __builtin_amdgcn_rcpf(1.0f + e);
}
template <int I>
__device__ __forceinline__ void w_piece(__amdgpu_buffer_rsrc_t srd, unsigned v, unsigned so, unsigned m0v) {
    if (I == 0) asm volatile("s_mov_b32 m0, %0\n\ts_nop 0\n\tbuffer_load_dwordx4 %1, %2, %3 offen lds"
                             :: "s"(m0v), "v"(v), "s"(srd), "s"(so) : "m0", "memory");
    else asm volatile("buffer_load_dwordx4 %0, %1, %2 offen offset:%3 lds"
                      :: "v"(v), "s"(srd), "s"(so), "n"(I * 1024) : "memory");
}
__device__ __forceinline__ void w_issue(__amdgpu_buffer_rsrc_t srd, const unsigned (&v)[4], unsigned so, unsigned m0v) {
    m0v = __builtin_amdgcn_readfirstlane(m0v);
    asm volatile("s_mov_b32 m0, %0\n\ts_nop 0\n\t"
                 "buffer_load_dwordx4 %1, %5, %6 offen lds\n\tbuffer_load_dwordx4 %2, %5, %6 offen offset:1024 lds\n\t"
                 "buffer_load_dwordx4 %3, %5, %6 offen offset:2048 lds\n\tbuffer_load_dwordx4 %4, %5, %6 offen offset:3072 lds"
                 :: "s"(m0v), "v"(v[0]), "v"(v[1]), "v"(v[2]), "v"(v[3]), "s"(srd), "s"(so) : "m0", "memory");
}
template <int SC, int SL, int J>
__device__ __forceinline__ void a_piece(__amdgpu_buffer_rsrc_t srd, unsigned av0, unsigned av1, unsigned so) {
    if (SC) asm volatile("buffer_load_dwordx4 a[%c0:%c1], %2, %3, %4 offen offset:%5 sc1"
                         :: "i"(SL * 20 + J * 4), "i"(SL * 20 + J * 4 + 3), "v"(J < 4 ? av0 : av1), "s"(srd), "s"(so), "n"((J & 3) * 1024) : "memory");
    else asm volatile("buffer_load_dwordx4 a[%c0:%c1], %2, %3, %4 offen offset:%5"
                      :: "i"(SL * 20 + J * 4), "i"(SL * 20 + J * 4 + 3), "v"(J < 4 ? av0 : av1), "s"(srd), "s"(so), "n"((J & 3) * 1024) : "memory");
}
template <int SC, int SL>
__device__ __forceinline__ void a_issue(__amdgpu_buffer_rsrc_t srd, unsigned av0, unsigned av1, unsigned so) {
    a_piece<SC, SL, 0>(srd, av0, av1, so); a_piece<SC, SL, 1>(srd, av0, av1, so); a_piece<SC, SL, 2>(srd, av0, av1, so);
    a_piece<SC, SL, 3>(srd, av0, av1, so); a_piece<SC, SL, 4>(srd, av0, av1, so);
}
template <int N> __device__ __forceinline__ void vm_wait() { asm volatile("s_waitcnt vmcnt(%0)" :: "n"(N) : "memory"); }
template <int R>
__device__ __forceinline__ void affine4(f32x4& d, u32x2 al, f32x4 be) {
    asm volatile("v_mfma_f32_4x4x4_16b_bf16 %0, %1, a[%c2:%c3], %4" : "=&v"(d) : "v"(al), "i"(R), "i"(R + 1), "v"(be));
}
template <int INIT>
__device__ __forceinline__ void mma_vv(f32x4& c, u32x4 a, u32x4 b, f32x4 c0) {
    if (INIT == 0) asm volatile("v_mfma_f32_16x16x32_bf16 %0, %1, %2, %0" : "+v"(c) : "v"(a), "v"(b));
    if (INIT == 1) asm volatile("v_mfma_f32_16x16x32_bf16 %0, %1, %2, 0" : "=&v"(c) : "v"(a), "v"(b));
    if (INIT == 2) asm volatile("v_mfma_f32_16x16x32_bf16 %0, %1, %2, %3" : "=&v"(c) : "v"(a), "v"(b), "v"(c0));
}
template <int INIT, int R>
__device__ __forceinline__ void mma_av(f32x4& c, u32x4 b, f32x4 c0) {
    if (INIT == 0) asm volatile("v_mfma_f32_16x16x32_bf16 %0, a[%c1:%c2], %3, %0" : "+v"(c) : "i"(R), "i"(R + 3), "v"(b));
    if (INIT == 1) asm volatile("v_mfma_f32_16x16x32_bf16 %0, a[%c1:%c2], %3, 0" : "=&v"(c) : "i"(R), "i"(R + 3), "v"(b));
    if (INIT == 2) asm volatile("v_mfma_f32_16x16x32_bf16 %0, a[%c1:%c2], %3, %4" : "=&v"(c) : "i"(R), "i"(R + 3), "v"(b), "v"(c0));
}
template <int INIT, int R, int Q>
__device__ __forceinline__ void mma_aa(f32x4& c, f32x4 c0) {
    if (INIT == 0) asm volatile("v_mfma_f32_16x16x32_bf16 %0, a[%c1:%c2], a[%c3:%c4], %0" : "+v"(c) : "i"(R), "i"(R + 3), "i"(Q), "i"(Q + 3));
    if (INIT == 1) asm volatile("v_mfma_f32_16x16x32_bf16 %0, a[%c1:%c2], a[%c3:%c4], 0" : "=&v"(c) : "i"(R), "i"(R + 3), "i"(Q), "i"(Q + 3));
    if (INIT == 2) asm volatile("v_mfma_f32_16x16x32_bf16 %0, a[%c1:%c2], a[%c3:%c4], %5" : "=&v"(c) : "i"(R), "i"(R + 3), "i"(Q), "i"(Q + 3), "v"(c0));
}
#define SB() __builtin_amdgcn_sched_barrier(0)
struct Nrm { u32x2 a0, a1; f32x4 b0, b1; };
struct KbCtx {
    __amdgpu_buffer_rsrc_t srdW1, srdW2, srdL, srdA, srdH;
    unsigned wv1[4], wv2[4], av0, av1, ring, w1o, a1o, w2o, a2o, wlo[2], alo[2];
    const char* tp[4];
    unsigned tpa[4];
    const char* ta;
    const char* tb;
    char* red;
    f32x4 bias;
};
__device__ __forceinline__ unsigned w1_off(const KbCtx& c, int g) { return c.w1o + g * (32 * F_ * 2); }
__device__ __forceinline__ unsigned a1_off(const KbCtx& c, int g) { return c.a1o + g * 40960; }
__device__ __forceinline__ unsigned w2_off(const KbCtx& c, int g) { return g >= 12 ? c.wlo[g & 1] : c.w2o + g * (32 * L_ * 2); }
__device__ __forceinline__ unsigned a2_off(const KbCtx& c, int g) { return g >= 12 ? c.alo[g & 1] : c.a2o + g * 40960; }
template <int R>
__device__ __forceinline__ void w_read(u32x4 (&wf)[4], const KbCtx& c, int SL) {
    const u32x2 v = tr_read(c.tp[R >> 1] + SL * 4096 + (R & 1) * 512);
    if (R & 1) { wf[R >> 1].z = v.x; wf[R >> 1].w = v.y; } else { wf[R >> 1].x = v.x; wf[R >> 1].y = v.y; }
}
template <int I>
__device__ __forceinline__ void nrm_read(Nrm& t, const KbCtx& c, int S) {
    if (I == 0) { t.a0 = *(const u32x2*)(c.ta + S * 256); t.a1 = *(const u32x2*)(c.ta + S * 256 + 32); }
    if (I == 1) t.b0 = *(const f32x4*)(c.tb + S * 128);
    if (I == 2) t.b1 = *(const f32x4*)(c.tb + S * 128 + 16);
}
template <int T>
__device__ __forceinline__ void red_put(const f32x4 (&acc)[4][5], const KbCtx& c) {
    if constexpr (T >= 8) *(f32x4*)(c.red + (T - 8) * 1024) = acc[(T - 8) / 5 + 1][(T - 8) % 5];
    if constexpr (T == 19) {
        SB();
#pragma unroll
        for (int j = 2; j < 5; ++j) *(f32x4*)(c.red + (10 + j) * 1024) = acc[3][j];
    }
}
constexpr int G1Q = (16 - NPRO) * 9;
constexpr int g1_cum(int s) {
    return s <= RSTEPS ? (G1Q * s + RSTEPS - 1) / RSTEPS : G1Q + (s >= 15 ? 24 : 24 * (s - RSTEPS) / (15 - RSTEPS));
}
template <int S, int P>
__device__ __forceinline__ void g1_req(const KbCtx& c) {
    constexpr int q = g1_cum(S) + P;
    static_assert(g1_cum(S + 1) - g1_cum(S) <= 9, "");
    if constexpr (q < g1_cum(S + 1)) {
        if constexpr (q < G1Q) {
            constexpr int g = NPRO + q / 9, r = q % 9;
            static_assert(g - 8 <= S && g >= S + 2, "");
            if constexpr (r < 4) w_piece<r>(c.srdW1, c.wv1[r], w1_off(c, g), c.ring + (g & 7) * 4096);
            else a_piece<0, g & 7, r - 4>(c.srdH, c.av0, c.av1, a1_off(c, g));
        } else {
            constexpr int g = (q - G1Q) / 4, r = (q - G1Q) % 4;
            static_assert(g + 8 <= S, "");
            w_piece<r>(c.srdW2, c.wv2[r], w2_off(c, g), c.ring + g * 4096);
        }
    }
}
template <int SL, int R>
__device__ __forceinline__ void w_park(const KbCtx& c) {
    asm volatile("ds_read_b64_tr_b16 a[%c0:%c1], %2 offset:%3"
                 :: "i"(160 + SL * 16 + R * 2), "i"(161 + SL * 16 + R * 2), "v"(c.tpa[R >> 1]), "n"(SL * 4096 + (R & 1) * 512) : "memory");
}
template <int SL> __device__ __forceinline__ void w_park8(const KbCtx& c) {
    w_park<SL, 0>(c); w_park<SL, 1>(c); w_park<SL, 2>(c); w_park<SL, 3>(c); w_park<SL, 4>(c); w_park<SL, 5>(c); w_park<SL, 6>(c); w_park<SL, 7>(c);
}
template <int S, int T>
__device__ __forceinline__ void g1_mma(f32x4 (&acc)[4][5], u32x4 (&n)[2][5], u32x4 (&wf)[2][4], const KbCtx& c) {
    mma_vv<S ? 0 : (T & 3) ? 1 : 2>(acc[T & 3][T >> 2], wf[S & 1][T & 3], n[S & 1][T >> 2], c.bias);
    SB();
}
constexpr int g1_aux(int t) {
    constexpr int a[20] = {0, 1, 2, 3, 4, -1, 5, -2, 6, -3, 7, -4, 8, 9, 10, -5, -6, -7, -8, -9};
    return a[t];
}
template <int S, int T>
__device__ __forceinline__ void g1_body(f32x4 (&acc)[4][5], u32x4 (&n)[2][5], u32x4 (&wf)[2][4], f32x4 (&d)[10], Nrm& t, const KbCtx& c) {
    g1_mma<S, T>(acc, n, wf, c);
    n[(S + 1) & 1][T >> 2][T & 3] = pk2(d[T >> 1][(T & 1) * 2], d[T >> 1][(T & 1) * 2 + 1]);
    constexpr int a = g1_aux(T);
    if constexpr (a < 0) g1_req<S, -1 - a>(c);
    if constexpr (a >= 3) w_read<a - 3>(wf[(S + 1) & 1], c, (S + 1) & 7);
    if constexpr (a >= 0 && a < 3 && S < 14) nrm_read<a>(t, c, S + 2);
    SB();
    if constexpr (T < 19) g1_body<S, T + 1>(acc, n, wf, d, t, c);
}
template <int S, int T>
__device__ __forceinline__ void g1_mid(f32x4 (&d)[10], const Nrm& t) {
    affine4<(S & 7) * 20 + T * 2>(d[T], (T & 1) ? t.a1 : t.a0, (T & 1) ? t.b1 : t.b0);
    SB();
    if constexpr (T < 9) g1_mid<S, T + 1>(d, t);
}
template <int T>
__device__ __forceinline__ void g1_last(f32x4 (&acc)[4][5], u32x4 (&n)[5], u32x4 (&wf)[4], const KbCtx& c) {
    constexpr int i = (T / 5 + 1) & 3, j = T % 5;
    mma_vv<0>(acc[i][j], wf[i], n[j], c.bias);
    SB();
    if constexpr (T < 8) { w_park<T / 4, (T & 3) * 2>(c); w_park<T / 4, (T & 3) * 2 + 1>(c); }
    red_put<T>(acc, c);
    SB();
    if constexpr (T < 19) g1_last<T + 1>(acc, n, wf, c);
}
template <int N> __device__ __forceinline__ void all_wait() {
    asm volatile("" ::: "memory");
    __builtin_amdgcn_s_waitcnt((N & 15) | 0x70 | ((N >> 4) << 14));
    asm volatile("" ::: "memory");
    SB();
}
template <int S>
__device__ __forceinline__ void g1_step(f32x4 (&acc)[4][5], u32x4 (&n)[2][5], u32x4 (&wf)[2][4], Nrm& t, const KbCtx& c) {
    if constexpr (S == 15) {
        all_wait<16>();
        g1_last<0>(acc, n[1], wf[1], c);
    } else {
        constexpr int young = NPRO * 9 + g1_cum(S) - (S + 2) * 9;
        static_assert(young >= 0 && young + g1_cum(S + 1) - g1_cum(S) <= 63, "");
        all_wait<young>();
        f32x4 d[10];
        g1_mid<S + 1, 0>(d, t);
        g1_body<S, 0>(acc, n, wf, d, t, c);
        g1_step<S + 1>(acc, n, wf, t, c);
    }
}
constexpr int g2_slot(int s) { return s < 6 ? s : s < 10 ? s - 6 : s - 4; }
template <int S, int P>
__device__ __forceinline__ void g2_req(const KbCtx& c) {
    if constexpr (S < 14) a_piece<1, S & 7, P>(c.srdA, c.av0, c.av1, a2_off(c, S));
}
template <int S, int T>
__device__ __forceinline__ void g2_body(f32x4 (&acc)[4][5], u32x4 (&wf)[2][4], const KbCtx& c) {
    constexpr int i = (T / 5 + 1) & 3, j = T % 5;
    if constexpr (S < 4) mma_aa<S ? 0 : i ? 1 : 2, (S & 7) * 20 + j * 4, 160 + S * 16 + i * 4>(acc[i][j], c.bias);
    else mma_av<0, (S & 7) * 20 + j * 4>(acc[i][j], wf[S & 1][i], c.bias);
    SB();
    if constexpr (S == 13) red_put<T>(acc, c);
    else {
        if constexpr (T % 4 == 0) g2_req<S + G2D, T / 4>(c);
        else if constexpr (S >= 3 && T > 5 && T < 17) w_read<(T - T / 4 - 3) & 7>(wf[(S + 1) & 1], c, g2_slot(S + 1));
    }
    SB();
    if constexpr (T < 19) g2_body<S, T + 1>(acc, wf, c);
}
template <int S>
__device__ __forceinline__ void g2_step(f32x4 (&acc)[4][5], u32x4 (&wf)[2][4], const KbCtx& c) {
    vm_wait<5 * (S + G2D > 14 ? 13 - S : G2D - 1)>();
    if (S == 0) STAMP(4, 1);
    SB();
    g2_body<S, 0>(acc, wf, c);
    if constexpr (S < 13) g2_step<S + 1>(acc, wf, c);
}
__device__ __forceinline__ void kb_reduce(f32x4 (&r)[5], const f32x4 (&acc)[4][5], char* smem, int w, int lane) {
    asm volatile("s_waitcnt lgkmcnt(0)\n\ts_barrier" ::: "memory");
    f32x4 p[5][3];
#pragma unroll
    for (int j = 0; j < 5; ++j)
#pragma unroll
        for (int i = 1; i < 4; ++i)
            p[j][i - 1] = *(const f32x4*)(smem + ((w - i) & 3) * B_REG + B_RED + lane * 16 + (i - 1) * 5120 + j * 1024);
    SB();
#pragma unroll
    for (int j = 0; j < 5; ++j) r[j] = (acc[0][j] + p[j][0]) + (p[j][1] + p[j][2]);
}
template <int S0, int S1>
__device__ __forceinline__ void g1_groups(const KbCtx& c) {
    if constexpr (S0 < S1) {
        if (S0 >= 3) vm_wait<54>();
        w_issue(c.srdW1, c.wv1, w1_off(c, S0), c.ring + S0 * 4096);
        a_issue<0, S0>(c.srdH, c.av0, c.av1, a1_off(c, S0));
        g1_groups<S0 + 1, S1>(c);
    }
}
template <int S>
__device__ __forceinline__ void g2_first(const KbCtx& c) {
    a_issue<1, S>(c.srdA, c.av0, c.av1, a2_off(c, S));
    if constexpr (S < G2D - 1) g2_first<S + 1>(c);
}
template <int CTRL> __device__ __forceinline__ f32x2 dpp_add2(f32x2 v) { return (f32x2){dpp_add<CTRL>(v.x), dpp_add<CTRL>(v.y)}; }

__global__ __launch_bounds__(256, 1) void kb_mlp(const u16* __restrict__ w1, const u16* __restrict__ b1, const u16* __restrict__ w2,
                                                 const u16* __restrict__ b2, char* ws, u16* __restrict__ out) {
    __shared__ __attribute__((aligned(16))) char smem[4 * B_REG];
    const int tid = threadIdx.x, lane = tid & 63;
    const int w = __builtin_amdgcn_readfirstlane(tid >> 6);
    STAMP(2, 0);
    const int n = lane & 15, hg = lane >> 4;
    KbCtx c;
    c.srdA = __builtin_amdgcn_make_buffer_rsrc((void*)ws, (short)0, WS_END, 0x00020000);
    u32x4 hs[3];
    {
        const unsigned v = hg * 1024 + n * 16;
        asm volatile("buffer_load_dwordx4 %0, %3, %5, %6 offen\n\tbuffer_load_dwordx4 %1, %3, %5, %7 offen\n\tbuffer_load_dwordx4 %2, %4, %5, %7 offen"
                     : "=&v"(hs[0]), "=&v"(hs[1]), "=&v"(hs[2])
                     : "v"(v), "v"(v + 4096 + (lane >> 5) * WS_END), "s"(c.srdA),
                       "s"(WS_HB + ((blockIdx.x >> 2) * 40 + 10 * w) * 1024 + (blockIdx.x & 3) * 256),
                       "s"(WS_HB + ((blockIdx.x >> 2) * 40 + 10 * w + 4) * 1024 + (blockIdx.x & 3) * 256) : "memory");
    }
    const int xcd = blockIdx.x & 7, idx = blockIdx.x >> 3;
    const bool g1 = idx < 27;
    const int t1 = (xcd >> 1) * 27 + (g1 ? idx : 0), t2 = (xcd >> 1) * 32 + idx;
    const int mt1 = t1 >> 2, dt1 = (xcd & 1) * 4 + (t1 & 3), mt2 = t2 >> 2, dt2 = (xcd & 1) * 4 + (t2 & 3);
    char* ring = smem + w * B_REG;
    c.ring = (unsigned)(size_t)(LDS_AS char*)ring;
    c.srdW1 = __builtin_amdgcn_make_buffer_rsrc((void*)w1, (short)0, g1 ? L_ * F_ * 2 : 0, 0x00020000);
    c.srdH = __builtin_amdgcn_make_buffer_rsrc((void*)ws, (short)0, g1 ? WS_END : 0, 0x00020000);
    c.srdW2 = __builtin_amdgcn_make_buffer_rsrc((void*)w2, (short)0, L_ * F_ * 2, 0x00020000);
    const bool idw = w == 3;
    c.srdL = __builtin_amdgcn_make_buffer_rsrc((void*)(idw ? ws + WS_ID : (const char*)w2), (short)0, L_ * F_ * 2, 0x00020000);
    {
        const int wr = lane >> 3, wslot = lane & 7;
        const unsigned s0 = ((((wslot >> 1) ^ ((wr >> 1) & 1)) * 2 + (wslot & 1)) * 8) * 2;
        const unsigned s1 = ((((wslot >> 1) ^ (((wr >> 1) & 1) | 2)) * 2 + (wslot & 1)) * 8) * 2;
        c.wv1[0] = wr * F_ * 2 + s0; c.wv1[1] = (wr + 8) * F_ * 2 + s1 - 1024;
        c.wv1[2] = c.wv1[0] + 16 * F_ * 2 - 2048; c.wv1[3] = c.wv1[1] + 16 * F_ * 2 - 2048;
        c.wv2[0] = wr * L_ * 2 + s0; c.wv2[1] = (wr + 8) * L_ * 2 + s1 - 1024;
        c.wv2[2] = c.wv2[0] + 16 * L_ * 2 - 2048; c.wv2[3] = c.wv2[1] + 16 * L_ * 2 - 2048;
    }
    c.av0 = lane * 16; c.av1 = lane * 16 + 4096;
    c.w1o = (w * 16 * 32 * F_ + mt1 * 64) * 2;
    c.a1o = WS_HB + (w * 16 * 40 + dt1 * 5) * 1024;
    c.w2o = (w * 14 * 32 * L_ + mt2 * 64) * 2;
    c.a2o = WS_GB + (w * 14 * 40 + dt2 * 5) * 1024;
#pragma unroll
    for (int i = 0; i < 2; ++i) {
        c.wlo[i] = idw ? i * (32 * L_ * 2) : c.w2o + (12 + i) * (32 * L_ * 2);
        c.alo[i] = idw ? WS_HB + ((mt2 * 2 + i) * 40 + dt2 * 5) * 1024 : c.a2o + (12 + i) * 40960;
    }
    u32x2 bb;
    asm volatile("global_load_dwordx2 %0, %1, %2" : "=v"(bb) : "v"(hg * 8), "s"(b1 + mt1 * 64 + 16 * w) : "memory");
    asm volatile("" ::: "a0", "a223");
    g1_groups<0, PRE>(c);
    {
        const int q4 = n >> 2, p4 = n & 3;
        const int fsw = ((q4 >> 1) & 1) | ((hg & 1) << 1);
        const char* tb = ring + (8 * hg + q4) * 128 + p4 * 8;
#pragma unroll
        for (int i = 0; i < 4; ++i) {
            c.tp[i] = tb + ((((i + w) & 3) ^ fsw) * 32);
            c.tpa[i] = (unsigned)(size_t)(LDS_AS const char*)c.tp[i];
        }
        c.ta = ring + B_TAB + (8 * hg + (lane & 3)) * 8;
        c.tb = ring + B_TAB + 4096 + hg * 32;
        c.red = ring + B_RED + lane * 16;
    }
    const __amdgpu_buffer_rsrc_t srdG = __builtin_amdgcn_make_buffer_rsrc((void*)(ws + WS_G2), (short)0, L_ * 8, 0x00020000);
    {
        const unsigned one = (n & 1) ? 0x3f800000u : 0x3f80u;
        const int dw = (hg & 1) == (n >> 3) ? (n >> 1) & 3 : 4;
        const u32x4 sel = {dw == 0 ? one : 0u, dw == 1 ? one : 0u, dw == 2 ? one : 0u, dw == 3 ? one : 0u};
        const u32x4 z4 = {0u, 0u, 0u, 0u};
        const u32x4 selA = hg < 2 ? sel : z4, selB = hg < 2 ? z4 : sel;
        f32x4 sum, x0, x1, x2, x3, x4;
        asm volatile("s_waitcnt vmcnt(%12)\n\t"
                     "v_mfma_f32_16x16x32_bf16 %1, %6, %9, 0\n\tv_mfma_f32_16x16x32_bf16 %2, %6, %10, 0\n\t"
                     "v_mfma_f32_16x16x32_bf16 %3, %7, %9, 0\n\tv_mfma_f32_16x16x32_bf16 %4, %7, %10, 0\n\t"
                     "v_mfma_f32_16x16x32_bf16 %5, %8, %9, 0\n\tv_mfma_f32_16x16x32_bf16 %0, %6, %11, 0\n\t"
                     "v_mfma_f32_16x16x32_bf16 %0, %7, %11, %0\n\tv_mfma_f32_16x16x32_bf16 %0, %8, %11, %0\n\ts_nop 4"
                     : "=&v"(sum), "=&v"(x0), "=&v"(x1), "=&v"(x2), "=&v"(x3), "=&v"(x4)
                     : "v"(hs[0]), "v"(hs[1]), "v"(hs[2]), "v"(selA), "v"(selB), "v"(sel), "n"(9 * PRE + 1) : "memory");
        STAMP(2, 1);
        f32x4 q = (x0 * x0 + x1 * x1) + (x2 * x2 + x3 * x3) + x4 * x4;
        asm volatile("s_nop 2" : "+v"(sum), "+v"(q));
        char* sc = smem + B_TAB + 6144;
        *(f32x2*)(sc + (n & 7) * 256 + (w * 8 + hg * 2 + (n >> 3)) * 8) = (f32x2){(sum[0] + sum[1]) + (sum[2] + sum[3]), (q[0] + q[1]) + (q[2] + q[3])};
        asm volatile("s_waitcnt lgkmcnt(0)\n\ts_barrier" ::: "memory");
        if (w == 0) {
            const f32x4 p0 = *(const f32x4*)(sc + lane * 32), p1 = *(const f32x4*)(sc + lane * 32 + 16);
            f32x2 v = {(p0[0] + p0[2]) + (p1[0] + p1[2]), (p0[1] + p0[3]) + (p1[1] + p1[3])};
            v = dpp_add2<0xb1>(v);
            v = dpp_add2<0x4e>(v);
            v = dpp_add2<0x141>(v);
            const float mean = v.x * (1.0f / D_);
            const float var = fmaxf(__builtin_fmaf(-mean, mean, v.y * (1.0f / D_)), 0.f);
            const float m1 = rb(mean), d1 = rb(__builtin_amdgcn_sqrtf(rb(rb(var) + 1e-5f)));
            const float i1 = __builtin_amdgcn_rcpf(d1);
            const float m2 = rb((mean - m1) * i1), d2 = rb(__builtin_amdgcn_sqrtf(rb(rb(var * i1 * i1) + 1e-5f)));
            const float al = rb(i1 * __builtin_amdgcn_rcpf(d2));
            const float be = -__builtin_fmaf(m2, d1, m1) * al;
            asm volatile("buffer_store_dwordx2 %0, %1, %2, %3 offen sc1"
                         :: "v"((u32x2){__float_as_uint(be), __float_as_uint(al) | 1u}), "v"((lane & 7) * (L_ * 8) + lane), "s"(srdG), "s"(blockIdx.x * 64) : "memory");
            STAMP(2, 2);
        }
    }
    f32x4 acc[4][5];
    u32x4 wf[2][4];
    if (g1) {
        g1_groups<PRE, PPOS>(c);
        u32x4 g[4];
        const unsigned gvo = lane * 64;
        asm volatile("s_waitcnt vmcnt(59)\n\t"
                     "buffer_load_dwordx4 %0, %4, %5, %6 offen sc1\n\tbuffer_load_dwordx4 %1, %4, %5, %6 offen offset:16 sc1\n\t"
                     "buffer_load_dwordx4 %2, %4, %5, %6 offen offset:32 sc1\n\tbuffer_load_dwordx4 %3, %4, %5, %6 offen offset:48 sc1"
                     : "=&v"(g[0]), "=&v"(g[1]), "=&v"(g[2]), "=&v"(g[3]) : "v"(gvo), "s"(srdG), "s"(w * 4096) : "memory");
        g1_groups<PPOS, NPRO>(c);
        STAMP(2, 3);
        asm volatile("s_waitcnt vmcnt(%5)" : "+v"(g[0]), "+v"(g[1]), "+v"(g[2]), "+v"(g[3]), "+v"(bb) : "n"(9 * (NPRO - PPOS)) : "memory");
        STAMP(2, 4);
        unsigned spins = 0;
#pragma unroll 1
        for (; spins < (1u << 16); ++spins) {
            if (__all(g[0].y & g[0].w & g[1].y & g[1].w & g[2].y & g[2].w & g[3].y & g[3].w & 1u)) break;
            asm volatile("buffer_load_dwordx4 %0, %4, %5, %6 offen sc1\n\tbuffer_load_dwordx4 %1, %4, %5, %6 offen offset:16 sc1\n\t"
                         "buffer_load_dwordx4 %2, %4, %5, %6 offen offset:32 sc1\n\tbuffer_load_dwordx4 %3, %4, %5, %6 offen offset:48 sc1\n\t"
                         "s_waitcnt vmcnt(0)"
                         : "=&v"(g[0]), "=&v"(g[1]), "=&v"(g[2]), "=&v"(g[3]) : "v"(gvo), "s"(srdG), "s"(w * 4096) : "memory");
        }
        STAMP(2, 5);
        {
            char* tw = ring + B_TAB + lane * 64;
#pragma unroll
            for (int k = 0; k < 4; ++k) {
                const unsigned lo = g[k].y >> 16, hi = g[k].w & 0xffff0000u;
                *(u32x4*)(tw + k * 16) = (k & 1) ? (u32x4){0u, lo, 0u, hi} : (u32x4){lo, 0u, hi, 0u};
            }
            *(u32x4*)(ring + B_TAB + 4096 + lane * 32) = (u32x4){g[0].x, g[0].z, g[1].x, g[1].z};
            *(u32x4*)(ring + B_TAB + 4096 + lane * 32 + 16) = (u32x4){g[2].x, g[2].z, g[3].x, g[3].z};
            asm volatile("" ::: "memory");
        }
        Nrm t;
        u32x4 nf[2][5];
        nrm_read<0>(t, c, 0); nrm_read<1>(t, c, 0); nrm_read<2>(t, c, 0);
        w_read<0>(wf[0], c, 0); w_read<1>(wf[0], c, 0); w_read<2>(wf[0], c, 0); w_read<3>(wf[0], c, 0);
        w_read<4>(wf[0], c, 0); w_read<5>(wf[0], c, 0); w_read<6>(wf[0], c, 0); w_read<7>(wf[0], c, 0);
        {
            c.bias = (f32x4){bflo(bb.x), bfhi(bb.x), bflo(bb.y), bfhi(bb.y)};
            asm volatile("" : "+v"(c.bias));
            f32x4 d[10];
            SB();
            g1_mid<0, 0>(d, t);
            nrm_read<0>(t, c, 1); nrm_read<1>(t, c, 1); nrm_read<2>(t, c, 1);
            asm volatile("s_nop 4");
            SB();
#pragma unroll
            for (int k = 0; k < 20; ++k) nf[0][k >> 2][k & 3] = pk2(d[k >> 1][(k & 1) * 2], d[k >> 1][(k & 1) * 2 + 1]);
            SB();
        }
        STAMP(3, 1);
        g1_step<0>(acc, nf, wf, t, c);
        STAMP(3, 2);
        f32x4 r[5];
        kb_reduce(r, acc, smem, w, lane);
        STAMP(3, 3);
        const unsigned go = WS_GB + ((mt1 * 2 + (w >> 1)) * 40 + dt1 * 5) * 1024;
        const unsigned gv = (((w & 1) * 2 + (hg >> 1)) * 16 + n) * 16 + (hg & 1) * 8;
        vm_wait<8>();
        w_park8<2>(c);
        w_park8<3>(c);
#pragma unroll
        for (int j = 0; j < 5; ++j) {
            if (j == 2) asm volatile("s_waitcnt lgkmcnt(0)" ::: "memory");
            if (j < EPW) w_issue(c.srdW2, c.wv2, w2_off(c, 6 + j), c.ring + j * 4096);
            SB();
            u32x2 o;
            o.x = pk2(gelu_tanh(r[j][0]), gelu_tanh(r[j][1]));
            o.y = pk2(gelu_tanh(r[j][2]), gelu_tanh(r[j][3]));
            __builtin_amdgcn_raw_buffer_store_b64(o, c.srdA, gv + j * 1024, go, 16);
            SB();
        }
        STAMP(3, 4);
        asm volatile("s_waitcnt vmcnt(0)\n\ts_barrier" ::: "memory");
        if (tid == 0) __hip_atomic_store((gu32*)(ws + WS_FL + (dt1 * 32 + mt1) * 128), 1u, __ATOMIC_RELAXED, __HIP_MEMORY_SCOPE_AGENT);
        STAMP(3, 5);
    } else {
#pragma unroll
        for (int s = 0; s < 6; ++s) w_issue(c.srdW2, c.wv2, w2_off(c, s), c.ring + s * 4096);
        warm_w<1>(w2, b2, xcd, (idx - 27) * 4 + w, 20, c.ring + 9 * 4096, lane);
        vm_wait<0>();
        w_park8<0>(c); w_park8<1>(c); w_park8<2>(c); w_park8<3>(c);
        asm volatile("s_waitcnt lgkmcnt(0)" ::: "memory");
#pragma unroll
        for (int s = 0; s < EPW; ++s) w_issue(c.srdW2, c.wv2, w2_off(c, 6 + s), c.ring + s * 4096);
        STAMP(3, 6);
    }
    unsigned bq;
    {
        const int nf = w == 3 ? 6 : 7;
        const unsigned fo = (lane < nf ? lane : 0) * 128;
        const char* fp = ws + WS_FL + (dt2 * 32 + 7 * w) * 128;
        unsigned f;
#pragma unroll
        for (int s = 6 + EPW; s < 14; ++s) {
            if (s == 14 - PLATE)
                asm volatile("global_load_ushort %1, %4, %5\n\tglobal_load_dword %0, %2, %3 sc1"
                             : "=&v"(f), "=&v"(bq) : "v"(fo), "s"(fp), "v"(n * 2), "s"(b2 + mt2 * 64 + 16 * w) : "memory");
            if (s < 12) w_issue(c.srdW2, c.wv2, w2_off(c, s), c.ring + g2_slot(s) * 4096);
            else w_issue(c.srdL, c.wv2, w2_off(c, s), c.ring + g2_slot(s) * 4096);
        }
        asm volatile("s_waitcnt vmcnt(%2)" : "+v"(f), "+v"(bq) : "n"(4 * PLATE) : "memory");
        unsigned spins = 0;
#pragma unroll 1
        for (; spins < (1u << 16); ++spins) {
            if (__all(f != 0)) break;
            asm volatile("global_load_dword %0, %1, %2 sc1\n\ts_waitcnt vmcnt(0)" : "=&v"(f) : "v"(fo), "s"(fp) : "memory");
        }
    }
    STAMP(4, 0);
    g2_first<0>(c);
    {
        const float b = bflo(bq);
        c.bias = (f32x4){b, b, b, b};
        asm volatile("" : "+v"(c.bias));
    }
    SB();
    g2_step<0>(acc, wf, c);
    STAMP(4, 2);
    f32x4 r[5];
    kb_reduce(r, acc, smem, w, lane);
    STAMP(4, 3);
    {
        u16* op = out + (size_t)(mt2 * 64 + 16 * w + n) * D_ + dt2 * 80 + 20 * hg;
        typedef unsigned u32x4u __attribute__((ext_vector_type(4), aligned(8)));
        u32x4u o0 = {pk2(r[0][0], r[0][1]), pk2(r[0][2], r[0][3]), pk2(r[1][0], r[1][1]), pk2(r[1][2], r[1][3])};
        u32x4u o1 = {pk2(r[2][0], r[2][1]), pk2(r[2][2], r[2][3]), pk2(r[3][0], r[3][1]), pk2(r[3][2], r[3][3])};
        u32x2 o2 = {pk2(r[4][0], r[4][1]), pk2(r[4][2], r[4][3])};
        *(u32x4u*)op = o0;
        *(u32x4u*)(op + 8) = o1;
        *(u32x2*)(op + 16) = o2;
    }
    STAMP(4, 4);
}

extern "C" void kernel_launch(void* const* d_in, const int* in_sizes, int n_in, void* d_out, int out_size,
                              void* d_ws, size_t ws_size, hipStream_t stream) {
    const u16* x = (const u16*)d_in[0];
    const u16* logA = (const u16*)d_in[1];
    const u16* Aim = (const u16*)d_in[2];
    const u16* Cre = (const u16*)d_in[3];
    const u16* Cim = (const u16*)d_in[4];
    const u16* logdt = (const u16*)d_in[5];
    const u16* dskip = (const u16*)d_in[6];
    const u16* w1 = (const u16*)d_in[8];
    const u16* b1 = (const u16*)d_in[9];
    const u16* w2 = (const u16*)d_in[10];
    const u16* b2 = (const u16*)d_in[11];
    char* ws = (char*)d_ws;
    ka_conv<<<256, 512, 0, stream>>>(x, logA, Aim, Cre, Cim, logdt, dskip, ws, w1, b1, w2, b2);
    kb_mlp<<<256, 256, 0, stream>>>(w1, b1, w2, b2, ws, (u16*)d_out);
}
```

```cpp
#include <hip/hip_runtime.h>
#include <hip/hip_bf16.h>
#include <stdint.h>
#pragma clang diagnostic ignored "-Winline-asm"

typedef unsigned short u16;
typedef __bf16 bf16x8 __attribute__((ext_vector_type(8)));
typedef __bf16 bf16x2 __attribute__((ext_vector_type(2)));
typedef short s16x4 __attribute__((ext_vector_type(4)));
typedef float f32x4 __attribute__((ext_vector_type(4)));
typedef float f32x2 __attribute__((ext_vector_type(2)));
typedef unsigned u32x4 __attribute__((ext_vector_type(4)));
typedef unsigned u32x2 __attribute__((ext_vector_type(2)));
#define LDS_AS __attribute__((address_space(3)))

#define STAMP(k, i) do { } while (0)
#define STAMPT(k, i, T) do { } while (0)

#define TS(i) do { } while (0)
#define TSPUT(k, T) do { } while (0)
#define STAMPX(k, i) do { } while (0)
#define FSTAMP STAMP
constexpr int L_ = 2048;
constexpr int D_ = 640;
constexpr int N_ = 128;
constexpr int F_ = 1728;
constexpr unsigned SZ_ = L_ * D_ * 2;
constexpr unsigned WS_HB = 0, WS_GB = SZ_, WS_END = 2 * SZ_;
constexpr unsigned WS_ID = 2 * SZ_, WS_GR = WS_ID + 64 * L_ * 2, WS_EP = WS_GR + 32768;
constexpr unsigned WS_G2 = WS_EP + 4096, WS_FL = WS_G2 + L_ * 8;

__device__ __forceinline__ float bf2f(unsigned v) { return __uint_as_float(v << 16); }
__device__ __forceinline__ float bflo(unsigned v) { return __uint_as_float(v << 16); }
__device__ __forceinline__ float bfhi(unsigned v) { return __uint_as_float(v & 0xffff0000u); }
__device__ __forceinline__ unsigned pk2(float lo, float hi) {
    f32x2 f = {lo, hi};
    bf16x2 b = __builtin_convertvector(f, bf16x2);
    return __builtin_bit_cast(unsigned, b);
}
__device__ __forceinline__ float rb(float x) { return bflo(pk2(x, x)); }

__device__ __forceinline__ f32x4 mfma16(u32x4 a, u32x4 b, f32x4 c) {
    return __builtin_amdgcn_mfma_f32_16x16x32_bf16(__builtin_bit_cast(bf16x8, a), __builtin_bit_cast(bf16x8, b), c, 0, 0, 0);
}
__device__ __forceinline__ u32x2 tr_read(const char* p) {
    s16x4 v = __builtin_amdgcn_ds_read_tr16_b64_v4i16((LDS_AS s16x4*)p);
    return __builtin_bit_cast(u32x2, v);
}
template <int CTRL>
__device__ __forceinline__ float dpp_add(float v) {
    int t = __builtin_amdgcn_update_dpp(0, __float_as_int(v), CTRL, 0xf, 0xf, true);
    return v + __int_as_float(t);
}
__device__ __forceinline__ float row16_sum(float v) {
    v = dpp_add<0x128>(v);
    v = dpp_add<0x124>(v);
    v = dpp_add<0x122>(v);
    v = dpp_add<0x121>(v);
    return v;
}

template <int NT = 0>
__device__ __forceinline__ void touch4(__amdgpu_buffer_rsrc_t srd, unsigned voff, unsigned soff, unsigned m0v) {
    if (NT) asm volatile("s_mov_b32 m0, %0\n\ts_nop 0\n\tbuffer_load_dword %1, %2, %3 offen nt lds"
                 :: "s"(m0v), "v"(voff), "s"(srd), "s"(soff) : "m0", "memory");
    else asm volatile("s_mov_b32 m0, %0\n\ts_nop 0\n\tbuffer_load_dword %1, %2, %3 offen lds"
                 :: "s"(m0v), "v"(voff), "s"(srd), "s"(soff) : "m0", "memory");
}
#define MT2S 1
__device__ __forceinline__ int mt2_of(int p, int i) { return MT2S == 1 ? 8 * p + i : MT2S == 2 ? 16 * (p >> 1) + 2 * i + (p & 1) : 4 * i + p; }
#define W2M 1
#define W2N 864
#define XT 1
#define W1SL 3
#define KT 300
#define W1X 8
#define BAL 1
#define W2C 320
#define CW 3
#define CODE_SRD(srd, sym) do { unsigned lo_, hi_, n_; \
    asm("s_getpc_b64 vcc\n\ts_add_u32 %0, vcc_lo, " sym "@rel32@lo+4\n\ts_addc_u32 %1, vcc_hi, " sym "@rel32@hi+12\n\t" \
        "s_mov_b32 %2, .L" sym "_last-" sym : "=s"(lo_), "=s"(hi_), "=s"(n_) :: "vcc", "scc"); \
    srd = __builtin_amdgcn_make_buffer_rsrc((void*)(((unsigned long long)hi_ << 32) | lo_), (short)0, n_, 0x00020000); } while (0)
__device__ __forceinline__ void pause_until(unsigned t0, unsigned ticks) {
#pragma unroll 1
    for (int i = 0; i < 64 && (unsigned)__builtin_amdgcn_s_memrealtime() - t0 < ticks; ++i) __builtin_amdgcn_s_sleep(4);
}
__device__ __forceinline__ void fetch_w2(const u16* W, int j, int j1, int nw, unsigned lds, int lane) {
    const __amdgpu_buffer_rsrc_t srdW = __builtin_amdgcn_make_buffer_rsrc((void*)W, (short)0, L_ * F_ * 2, 0x00020000);
    for (; j < j1; j += nw) touch4(srdW, lane * 128, j * 8192, lds);
}
template <int MODE>
__device__ __forceinline__ void warm_w(const u16* W, const u16* bias, int xcd, int gw, int nw, unsigned lds, int lane) {
    constexpr int LDW = MODE == 0 ? F_ : L_;
    const __amdgpu_buffer_rsrc_t srdW = __builtin_amdgcn_make_buffer_rsrc((void*)W, (short)0, L_ * F_ * 2, 0x00020000);
    if (gw == 0) touch4(__builtin_amdgcn_make_buffer_rsrc((void*)bias, (short)0, LDW * 2, 0x00020000), lane * 128, 0, lds);
    const int mt0 = MODE == 0 ? ((xcd >> 1) * 27) >> 2 : mt2_of(xcd >> 1, 0);
    const unsigned voff = ((lane >> 3) * LDW + (lane & 7) * (MODE == 0 ? 64 : 64 * MT2S)) * 2;
    int r = 0;
    for (int j = gw; j < (MODE == 0 ? 256 : 224); j += nw) {
        if (MODE == 0 && r++ < W1SL) asm volatile("s_waitcnt vmcnt(0)" ::: "memory");
        const int s = MODE == 0 ? (j >> 4) ^ ((xcd & 1) * W1X) : j >> 4, w = (j >> 2) & 3;
        const int ks = MODE == 0 ? w * 16 + s : w * 14 + s;
        if (MODE != 0 && ks >= 54) continue;
        touch4(srdW, voff, ((ks * 32 + (j & 3) * 8) * LDW + mt0 * 64) * 2, lds);
    }
}

constexpr int KA_ROWB = 528;
constexpr int KA_LO = 32 * KA_ROWB;
constexpr int KA_HI0 = 4 * KA_LO;
constexpr int KA_HI = 13312;
constexpr int KA_MB = 20000;
constexpr int CV_COPY = 4160;
constexpr int CV_UB = 4608;
constexpr int KA_U = KA_HI0 + 4 * KA_HI;
#define XC1 4
#define XCS 3
#define GFIRST " sc1"
typedef __attribute__((address_space(1))) unsigned gu32;

__device__ __forceinline__ void cm4(f32x2& r0, f32x2 a0, f32x2 b0, f32x2& r1, f32x2 a1, f32x2 b1,
                                    f32x2& r2, f32x2 a2, f32x2 b2, f32x2& r3, f32x2 a3, f32x2 b3) {
    asm("v_pk_mul_f32 %0, %4, %5 op_sel:[0,0] op_sel_hi:[1,0]\n\t"
        "v_pk_mul_f32 %1, %6, %7 op_sel:[0,0] op_sel_hi:[1,0]\n\t"
        "v_pk_mul_f32 %2, %8, %9 op_sel:[0,0] op_sel_hi:[1,0]\n\t"
        "v_pk_mul_f32 %3, %10, %11 op_sel:[0,0] op_sel_hi:[1,0]\n\t"
        "v_pk_fma_f32 %0, %4, %5, %0 op_sel:[1,1,0] op_sel_hi:[0,1,1] neg_lo:[1,0,0]\n\t"
        "v_pk_fma_f32 %1, %6, %7, %1 op_sel:[1,1,0] op_sel_hi:[0,1,1] neg_lo:[1,0,0]\n\t"
        "v_pk_fma_f32 %2, %8, %9, %2 op_sel:[1,1,0] op_sel_hi:[0,1,1] neg_lo:[1,0,0]\n\t"
        "v_pk_fma_f32 %3, %10, %11, %3 op_sel:[1,1,0] op_sel_hi:[0,1,1] neg_lo:[1,0,0]"
        : "=&v"(r0), "=&v"(r1), "=&v"(r2), "=&v"(r3)
        : "v"(a0), "v"(b0), "v"(a1), "v"(b1), "v"(a2), "v"(b2), "v"(a3), "v"(b3));
}
constexpr int ka_off(int r) { return r < 32 ? r * KA_ROWB : KA_HI0 - KA_MB + (r - 32) * KA_ROWB; }
template <int R0, int R1, int R2, int R3>
__device__ __forceinline__ void put4(const unsigned (&m0v)[2], f32x2 v0, f32x2 v1, f32x2 v2, f32x2 v3) {
    static_assert((R0 < 32) == (R1 < 32) && (R0 < 32) == (R2 < 32) && (R0 < 32) == (R3 < 32), "");
    unsigned t0, t1, t2, t3;
    asm volatile("s_mov_b32 m0, %4\n\t"
                 "v_cvt_pk_bf16_f32 %0, %5, %6\n\tv_cvt_pk_bf16_f32 %1, %7, %8\n\t"
                 "v_cvt_pk_bf16_f32 %2, %9, %10\n\tv_cvt_pk_bf16_f32 %3, %11, %12\n\t"
                 "ds_write_addtid_b32 %0 offset:%13\n\tds_write_addtid_b32 %1 offset:%14\n\t"
                 "ds_write_addtid_b32 %2 offset:%15\n\tds_write_addtid_b32 %3 offset:%16"
                 : "=&v"(t0), "=&v"(t1), "=&v"(t2), "=&v"(t3)
                 : "s"(m0v[R0 >= 32]), "v"(v0.x), "v"(v0.y), "v"(v1.x), "v"(v1.y), "v"(v2.x), "v"(v2.y), "v"(v3.x), "v"(v3.y),
                   "n"(ka_off(R0)), "n"(ka_off(R1)), "n"(ka_off(R2)), "n"(ka_off(R3))
                 : "m0", "memory");
}
__device__ __forceinline__ f32x2 cexp_p(float zr, float zi, float p) {
    const float mag = __builtin_amdgcn_exp2f(zr * p);
    const float rev = __builtin_amdgcn_fractf(zi * p);
    f32x2 r;
    r.x = mag * __builtin_amdgcn_cosf(rev);
    r.y = mag * __builtin_amdgcn_sinf(rev);
    return r;
}
__device__ __forceinline__ float wave_sum(float v) {
    v = row16_sum(v);
    const int i = __float_as_int(v);
    return (__int_as_float(__builtin_amdgcn_readlane(i, 0)) + __int_as_float(__builtin_amdgcn_readlane(i, 16))) +
           (__int_as_float(__builtin_amdgcn_readlane(i, 32)) + __int_as_float(__builtin_amdgcn_readlane(i, 48)));
}
__device__ __forceinline__ unsigned next_lane(unsigned v, unsigned last) {
    return (unsigned)__builtin_amdgcn_update_dpp((int)last, (int)v, 0x130, 0xf, 0xf, false);
}
__device__ __forceinline__ void put_copies(char* p, unsigned r0, unsigned r1, unsigned r2, unsigned r3, unsigned r4, unsigned r5) {
    const unsigned s0 = __builtin_amdgcn_alignbit(r1, r0, 16), s1 = __builtin_amdgcn_alignbit(r2, r1, 16),
                   s2 = __builtin_amdgcn_alignbit(r3, r2, 16), s3 = __builtin_amdgcn_alignbit(r4, r3, 16),
                   s4 = __builtin_amdgcn_alignbit(r5, r4, 16);
    *(u32x4*)(p + 0 * CV_COPY) = (u32x4){r0, r1, r2, r3};
    *(u32x4*)(p + 1 * CV_COPY) = (u32x4){s0, s1, s2, s3};
    *(u32x4*)(p + 2 * CV_COPY) = (u32x4){r1, r2, r3, r4};
    *(u32x4*)(p + 3 * CV_COPY) = (u32x4){s1, s2, s3, s4};
}
template <int O> __device__ __forceinline__ void rd64(u32x2& v, unsigned a) {
    asm volatile("ds_read_b64 %0, %1 offset:%2" : "=v"(v) : "v"(a), "n"(O) : "memory");
}
template <int O> __device__ __forceinline__ void rd128(u32x4& v, unsigned a) {
    asm volatile("ds_read_b128 %0, %1 offset:%2" : "=v"(v) : "v"(a), "n"(O) : "memory");
}
struct CvFrag { u32x2 lo[8], hi[8]; u32x4 b[8]; };
__device__ __forceinline__ void cv_wait(CvFrag& f) {
    asm volatile("s_waitcnt lgkmcnt(0)"
                 : "+v"(f.lo[0]), "+v"(f.lo[1]), "+v"(f.lo[2]), "+v"(f.lo[3]), "+v"(f.lo[4]), "+v"(f.lo[5]), "+v"(f.lo[6]), "+v"(f.lo[7]),
                   "+v"(f.hi[0]), "+v"(f.hi[1]), "+v"(f.hi[2]), "+v"(f.hi[3]), "+v"(f.hi[4]), "+v"(f.hi[5]), "+v"(f.hi[6]), "+v"(f.hi[7]),
                   "+v"(f.b[0]), "+v"(f.b[1]), "+v"(f.b[2]), "+v"(f.b[3]), "+v"(f.b[4]), "+v"(f.b[5]), "+v"(f.b[6]), "+v"(f.b[7])
                 :: "memory");
}
template <int T>
__device__ __forceinline__ void cv_mma(const CvFrag& f, f32x4 (&acc)[8]) {
    constexpr int q = T < 1 ? 0 : T < 3 ? 1 : T < 6 ? 2 : T < 10 ? 3 : T < 15 ? 4 : T < 21 ? 5 : T < 28 ? 6 : 7;
    constexpr int g1 = T - q * (q + 1) / 2;
    const u32x4 a = {f.lo[g1].x, f.lo[g1].y, f.hi[g1].x, f.hi[g1].y};
    acc[q] = mfma16(a, f.b[q - g1], acc[q]);
}
template <int G0, int R>
__device__ __forceinline__ void cv_read(CvFrag& f, unsigned aa, unsigned ba) {
    constexpr int G1 = R / 3;
    if constexpr (R % 3 == 0) rd64<(63 - 8 * G1 - G0) * 64>(f.lo[G1], aa);
    else if constexpr (R % 3 == 1) rd64<(63 - 8 * G1 - G0) * 64 + 8>(f.hi[G1], aa);
    else rd128<(8 * G1 - G0 + 7) * 64>(f.b[G1], ba);
}
template <int G0, int T>
__device__ __forceinline__ void cv_group(const CvFrag& cur, CvFrag& nxt, f32x4 (&acc)[8], unsigned aa, unsigned ba) {
    cv_mma<T>(cur, acc);
    __builtin_amdgcn_sched_barrier(0);
    if constexpr (G0 < 7 && T < 30 && T % 5 != 4) {
        cv_read<G0 + 1, T - T / 5>(nxt, aa, ba);
        __builtin_amdgcn_sched_barrier(0);
    }
    if constexpr (T < 35) cv_group<G0, T + 1>(cur, nxt, acc, aa, ba);
}
template <int G0>
__device__ __forceinline__ void cv_round(CvFrag& cur, CvFrag& nxt, f32x4 (&acc)[8], unsigned aa, unsigned ba) {
    cv_wait(cur);
    if constexpr (G0 == 5) __builtin_amdgcn_s_barrier();
    __builtin_amdgcn_sched_barrier(0);
    cv_group<G0, 0>(cur, nxt, acc, aa, ba);
    if constexpr (G0 < 7) cv_round<G0 + 1>(nxt, cur, acc, aa, ba);
}
template <int I>
__device__ __forceinline__ void cv_first(CvFrag& f, unsigned aa, unsigned ba) {
    cv_read<0, I>(f, aa, ba);
    if constexpr (I < 23) cv_first<I + 1>(f, aa, ba);
}

template <class Hook>
__device__ __forceinline__ void ka_chain(const unsigned (&m0v)[2], float p_la, float p_ai, float p_cr, float p_ci, float p_dt, Hook hook) {
    const float dt = rb(__builtin_amdgcn_exp2f(p_dt * 1.44269504088896341f));
    const float ar = -rb(__builtin_amdgcn_exp2f(p_la * 1.44269504088896341f));
    const float zr = ar * dt * 1.44269504088896341f, zi = p_ai * dt * 0.15915494309189535f;
    f32x2 a[17], b[16], e[8], F1, F2, F4, F8, r0, r1, r2, r3;
    a[0] = (f32x2){2.0f * (p_cr * dt), -2.0f * (p_ci * dt)};
    e[1] = cexp_p(zr, -zi, 1.f);
    b[1] = cexp_p(zr, zi, 8.f);
    F1 = cexp_p(zr, -zi, 128.f);
    cm4(e[2], e[1], e[1], b[2], b[1], b[1], F2, F1, F1, a[1], a[0], F1);
    cm4(e[4], e[2], e[2], b[4], b[2], b[2], F4, F2, F2, e[3], e[2], e[1]);
    put4<41, 42, 43, 45>(m0v, (f32x2){1.f, 0.f}, b[1], b[2], b[4]);
    hook(0);
    cm4(a[2], a[0], F2, a[3], a[1], F2, b[3], b[2], b[1], r0, a[0], e[1]);
    cm4(F8, F4, F4, b[8], b[4], b[4], r1, a[1], e[1], r2, a[0], e[2]);
    put4<16, 20, 1, 9>(m0v, a[2], a[3], r0, r1);
    hook(1);
    cm4(a[4], a[0], F4, a[5], a[1], F4, a[6], a[2], F4, a[7], a[3], F4);
    put4<24, 26, 28, 30>(m0v, a[4], a[5], a[6], a[7]);
    hook(2);
    cm4(e[5], e[4], e[1], e[6], e[4], e[2], e[7], e[4], e[3], r3, a[1], e[2]);
    put4<0, 8, 2, 10>(m0v, a[0], a[1], r2, r3);
    hook(3);
    cm4(b[5], b[4], b[1], b[6], b[4], b[2], b[7], b[4], b[3], r0, a[0], e[3]);
    put4<44, 49, 46, 47>(m0v, b[3], b[8], b[5], b[6]);
    hook(4);
    cm4(a[8], a[0], F8, a[9], a[1], F8, a[10], a[2], F8, a[11], a[3], F8);
    put4<32, 33, 34, 35>(m0v, a[8], a[9], a[10], a[11]);
    hook(5);
    cm4(a[12], a[4], F8, a[13], a[5], F8, a[14], a[6], F8, a[15], a[7], F8);
    put4<36, 37, 38, 39>(m0v, a[12], a[13], a[14], a[15]);
    hook(6);
    cm4(a[16], a[8], F8, r1, a[1], e[3], r2, a[0], e[4], r3, a[1], e[4]);
    put4<3, 11, 4, 12>(m0v, r0, r1, r2, r3);
    hook(7);
    cm4(b[9], b[8], b[1], b[10], b[8], b[2], b[11], b[8], b[3], b[12], b[8], b[4]);
    put4<50, 51, 52, 53>(m0v, b[9], b[10], b[11], b[12]);
    hook(8);
    cm4(b[13], b[8], b[5], b[14], b[8], b[6], b[15], b[8], b[7], r0, a[0], e[5]);
    put4<54, 55, 56, 48>(m0v, b[13], b[14], b[15], b[7]);
    asm volatile("ds_write_addtid_b32 %0 offset:%1" :: "v"(pk2(a[16].x, a[16].y)), "n"(ka_off(40)) : "memory");
    hook(9);
    cm4(r1, a[1], e[5], r2, a[0], e[6], r3, a[1], e[6], F1, a[0], e[7]);
    put4<5, 13, 6, 14>(m0v, r0, r1, r2, r3);
    hook(10);
    cm4(r0, a[1], e[7], r1, a[2], e[2], r2, a[3], e[2], F2, a[2], e[4]);
    put4<7, 15, 17, 21>(m0v, F1, r0, r1, r2);
    hook(11);
    cm4(r3, a[3], e[4], F1, a[2], e[6], r0, a[3], e[6], r1, a[4], e[4]);
    put4<18, 22, 19, 23>(m0v, F2, r3, F1, r0);
    hook(12);
    cm4(r2, a[5], e[4], r3, a[6], e[4], F1, a[7], e[4], F2, a[7], e[4]);
    put4<25, 27, 29, 31>(m0v, r1, r2, r3, F1);
    asm volatile("s_waitcnt lgkmcnt(0)" ::: "memory");
}
__device__ __forceinline__ void unpack4(float (&o)[4], u32x2 v) {
    asm volatile("v_lshlrev_b32 %0, 16, %4\n\tv_and_b32 %1, 0xffff0000, %4\n\tv_lshlrev_b32 %2, 16, %5\n\tv_and_b32 %3, 0xffff0000, %5"
                 : "=&v"(o[0]), "=&v"(o[1]), "=&v"(o[2]), "=&v"(o[3]) : "v"(v.x), "v"(v.y));
}

extern "C" __global__ __launch_bounds__(512, 2) void ka_conv(const u16* __restrict__ x, const u16* __restrict__ logA,
                                                  const u16* __restrict__ Aim, const u16* __restrict__ Cre,
                                                  const u16* __restrict__ Cim, const u16* __restrict__ logdt,
                                                  const u16* __restrict__ dskip, char* ws,
                                                  const u16* __restrict__ w1, const u16* __restrict__ b1,
                                                  const u16* __restrict__ w2, const u16* __restrict__ b2) {
    __shared__ __attribute__((aligned(16))) char smem[KA_U + 4 * CV_UB + 256];
    const int tid = threadIdx.x, lane = tid & 63;
    const int w = __builtin_amdgcn_readfirstlane(tid >> 6);
    STAMP(0, 0);
    if ((CW & 1) && w >= 6) {
        __amdgpu_buffer_rsrc_t srdC;
        CODE_SRD(srdC, "ka_conv");
        touch4(srdC, lane * 128, (w - 6) * 8192, (unsigned)(size_t)(LDS_AS char*)smem + w * 256);
    }
    asm volatile("" :: "s"(x), "s"(logA), "s"(Aim), "s"(Cre), "s"(Cim), "s"(logdt), "s"(dskip), "s"(ws));
    const int xcd = blockIdx.x & 7, idx = blockIdx.x >> 3;
    const bool conv = idx < 20;
    const int grp = ((xcd & 1) * 4 + (xcd >> 1)) * 20 + (conv ? idx : 0);
    const int d0 = grp * 4;
    const int row = (xcd * 32 + idx) * 8 + w;
    unsigned sv[5], epoch, pp[5];
    float pf[5];
    asm volatile("global_load_dword %0, %11, %12\n\tglobal_load_dword %1, %11, %12 offset:256\n\t"
                 "global_load_dword %2, %11, %12 offset:512\n\tglobal_load_dword %3, %11, %12 offset:768\n\t"
                 "global_load_dword %4, %11, %12 offset:1024\n\tglobal_load_dword %5, %13, %14\n\t"
                 "global_load_ushort %6, %15, %16\n\tglobal_load_ushort %7, %15, %17\n\t"
                 "global_load_ushort %8, %15, %18\n\tglobal_load_ushort %9, %15, %19\n\t"
                 "global_load_ushort %10, %13, %20"
                 : "=&v"(sv[0]), "=&v"(sv[1]), "=&v"(sv[2]), "=&v"(sv[3]), "=&v"(sv[4]), "=&v"(epoch),
                   "=&v"(pp[0]), "=&v"(pp[1]), "=&v"(pp[2]), "=&v"(pp[3]), "=&v"(pp[4])
                 : "v"(lane * 4), "s"(x + (size_t)row * D_), "v"(0), "s"(ws + WS_EP),
                   "v"((d0 * N_ + tid) * 2), "s"(logA), "s"(Aim), "s"(Cre), "s"(Cim), "s"(logdt + d0 + (w >> 1))
                 : "memory");
    const int t = tid & 255;
    const __amdgpu_buffer_rsrc_t srdG = __builtin_amdgcn_make_buffer_rsrc((void*)(ws + WS_GR), (short)0, L_ * 8, 0x00020000);
    const __amdgpu_buffer_rsrc_t srdX = __builtin_amdgcn_make_buffer_rsrc((void*)x, (short)0, SZ_, 0x00020000);
    {
        const int tw = XT == 1 ? (BAL ? (conv ? 64 : (7 - w) * 12 + idx - 20) : (idx - 20) * 8 + w) : XT == 2 ? (w < 4 ? idx * 4 + w : 64) : XT == 3 ? (w < 2 ? idx * 2 + w : 64) : 64;
        touch4(srdX, (lane >> 1) * (D_ * 2) + (lane & 1) * 156, (unsigned)tw < 64u ? tw * (32 * D_ * 2) + (grp / 20) * 160 : SZ_,
               (unsigned)(size_t)(LDS_AS char*)(smem + KA_U + 4 * CV_UB));
    }
    const int owner = conv ? w >> 2 : 0;
    u32x2 xc[8], xd;
    asm volatile("s_barrier\n\ts_cmp_eq_u32 %5, 0\n\ts_cbranch_scc1 .Lka_a%=\n\t"
                 "global_load_dwordx2 %4, %6, %7\n\t"
                 "buffer_load_dwordx2 %0, %8, %9, %10 offen\n\tbuffer_load_dwordx2 %1, %8, %9, %10 offen offset:1280\n\t"
                 "buffer_load_dwordx2 %2, %8, %9, %10 offen offset:2560\n\tbuffer_load_dwordx2 %3, %8, %9, %10 offen offset:3840\n\t"
                 "s_waitcnt vmcnt(6)\n\ts_branch .Lka_b%=\n"
                 ".Lka_a%=:\n\ts_waitcnt vmcnt(1)\n"
                 ".Lka_b%=:"
                 : "=&v"(xc[0]), "=&v"(xc[1]), "=&v"(xc[2]), "=&v"(xc[3]), "=&v"(xd)
                 : "s"(owner), "v"(0), "s"(dskip + d0), "v"(t * (8 * D_ * 2)), "s"(srdX), "s"(d0 * 2) : "scc", "memory");
    unsigned etag;
    {
        float v[12];
        unpack4(*(float(*)[4])v, (u32x2){sv[0], sv[1]});
        unpack4(*(float(*)[4])(v + 4), (u32x2){sv[2], sv[3]});
        asm volatile("v_lshlrev_b32 %0, 16, %3\n\tv_and_b32 %1, 0xffff0000, %3\n\tv_xor_b32 %2, 0x9e3779b9, %4"
                     : "=&v"(v[8]), "=&v"(v[9]), "=&v"(etag) : "v"(sv[4]), "v"(epoch));
        const float s = ((v[0] + v[1]) + (v[2] + v[3])) + ((v[4] + v[5]) + (v[6] + v[7])) + (v[8] + v[9]);
        const float mean = wave_sum(s) * (1.0f / D_);
        float q0 = 0.f, q1 = 0.f;
#pragma unroll
        for (int j = 0; j < 5; ++j) {
            const float a = v[2 * j] - mean, b = v[2 * j + 1] - mean;
            q0 = __builtin_fmaf(a, a, q0);
            q1 = __builtin_fmaf(b, b, q1);
        }
        const float q = wave_sum(q0 + q1);
        const float den = __builtin_amdgcn_sqrtf(rb(rb(q * (1.0f / D_)) + 1e-5f));
        asm volatile("buffer_store_dwordx2 %10, %11, %12, %13 offen sc1\n\t"
                     "v_lshlrev_b32 %0, 16, %5\n\tv_lshlrev_b32 %1, 16, %6\n\tv_lshlrev_b32 %2, 16, %7\n\t"
                     "v_lshlrev_b32 %3, 16, %8\n\tv_lshlrev_b32 %4, 16, %9"
                     : "=&v"(pf[0]), "=&v"(pf[1]), "=&v"(pf[2]), "=&v"(pf[3]), "=&v"(pf[4])
                     : "v"(pp[0]), "v"(pp[1]), "v"(pp[2]), "v"(pp[3]), "v"(pp[4]),
                       "v"((u32x2){pk2(mean, den), etag * 0x85ebca6bu}), "v"(lane * (L_ * 8)), "s"(srdG), "s"(row * 8) : "memory");
    }
    TS(0);
    if (!conv) {
        const unsigned lds = (unsigned)(size_t)(LDS_AS char*)(smem + w * 1024);
        if (XT == 4 && idx < 28) touch4(srdX, (lane >> 1) * (D_ * 2) + (lane & 1) * 156, ((idx - 20) * 8 + w) * (32 * D_ * 2) + (grp / 20) * 160, lds);
        if ((CW & 2) && idx == 28 && w < 3) {
            __amdgpu_buffer_rsrc_t srdC;
            CODE_SRD(srdC, "kb_mlp");
            touch4(srdC, lane * 128, w * 8192, lds);
        }
        const int gw = BAL ? w * 12 + idx - 20 : (idx - 20) * 8 + w;
        warm_w<0>(w1, b1, xcd, gw, 96, lds, lane);
        if (W2M & 1) asm volatile("s_waitcnt vmcnt(0)" ::: "memory");
        fetch_w2(w2, W2C + (95 - gw) * 8 + xcd, W2N, 768, lds, lane);
        if (idx == 31 && tid < 256) {
            const int r = xcd * 8 + (tid >> 5), c = tid & 31;
            *(unsigned*)(ws + WS_ID + r * (L_ * 2) + 4 * c) = (r >> 1) == c ? ((r & 1) ? 0x3f800000u : 0x3f80u) : 0u;
        }
        if (idx == 30 && tid < 128) *(u32x4*)(ws + WS_G2 + (xcd * 128 + tid) * 16) = (u32x4){0u, 0u, 0u, 0u};
        if (idx == 29 && tid < 32) *(unsigned*)(ws + WS_FL + (xcd * 32 + tid) * 128) = 0u;
        asm volatile("s_waitcnt vmcnt(0)" ::: "memory");
        STAMP(0, 7);
        return;
    }
    const unsigned lds0 = (unsigned)(size_t)(LDS_AS char*)smem;
    const int m = lane & 15, hg = lane >> 4;
    char* mine = smem + (w & 3) * KA_LO;
    char* minh = smem + KA_HI0 + (w & 3) * KA_HI;
    const unsigned m0v[2] = {lds0 + (w >> 1) * KA_LO + (w & 1) * 256, lds0 + KA_MB + (w >> 1) * KA_HI + (w & 1) * 256};
    ka_chain(m0v, pf[0], pf[1], pf[2], pf[3], pf[4], [&](int i) {
        if (i % XCS == 0 && i / XCS < 4)
            asm volatile("s_cmp_eq_u32 %1, 0\n\ts_cbranch_scc1 .Lka_c%=\n\tbuffer_load_dwordx2 %0, %2, %3, %4 offen offset:%5\n.Lka_c%=:"
                         : "=v"(xc[4 + i / XCS])
                         : "s"(owner), "v"(t * (8 * D_ * 2)), "s"(srdX), "s"(d0 * 2 + 4 * D_ * 2), "n"((i / XCS) * (D_ * 2)) : "scc", "memory");
    });
    TS(1);
    __builtin_amdgcn_s_barrier();
    if (w < 4) {
        TS(2);
        u16* kd = (u16*)(minh + 8192);
        {
            const char* fb = mine + m * KA_ROWB + hg * 16;
            const char* fh = minh + m * KA_ROWB + hg * 16;
            u32x4 fa[8][3], fl[8];
#pragma unroll
            for (int ks = 0; ks < 8; ++ks) {
                fl[ks] = *(const u32x4*)(fh + 9 * KA_ROWB + ks * 64);
                fa[ks][0] = *(const u32x4*)(fb + ks * 64);
                fa[ks][1] = *(const u32x4*)(fb + 16 * KA_ROWB + ks * 64);
                fa[ks][2] = *(const u32x4*)(fh + ks * 64);
            }
            __builtin_amdgcn_sched_barrier(0);
            f32x4 acc0 = {0.f, 0.f, 0.f, 0.f}, acc1 = acc0, acc2 = acc0;
#pragma unroll
            for (int ks = 0; ks < 8; ++ks) {
                acc0 = mfma16(fa[ks][0], fl[ks], acc0);
                acc1 = mfma16(fa[ks][1], fl[ks], acc1);
                acc2 = mfma16(fa[ks][2], fl[ks], acc2);
            }
            const int c = m;
            *(u32x2*)(kd + 128 * (hg >> 1) + 4 * (hg & 1) + 8 * c) = (u32x2){pk2(acc0[0], acc0[1]), pk2(acc0[2], acc0[3])};
            const unsigned p01 = pk2(acc1[0], acc1[1]), p23 = pk2(acc1[2], acc1[3]);
            const int i0 = hg < 2 ? 256 + 64 * hg + 4 * c : 256 + 64 * hg + 2 * c;
            *(unsigned*)(kd + i0) = p01;
            *(unsigned*)(kd + i0 + (hg < 2 ? 2 : 32)) = p23;
            const unsigned q01 = pk2(acc2[0], acc2[1]), q23 = pk2(acc2[2], acc2[3]);
            kd[512 + 64 * hg + c] = (u16)(q01 & 0xffff);
            kd[528 + 64 * hg + c] = (u16)(q01 >> 16);
            kd[544 + 64 * hg + c] = (u16)(q23 & 0xffff);
            kd[560 + 64 * hg + c] = (u16)(q23 >> 16);
            asm volatile("" ::: "memory");
        }
        const unsigned* kw = (const unsigned*)kd;
        const unsigned dw0 = kw[319 - lane], dw1 = kw[320 - lane];
        const unsigned cw0 = kw[255 - lane], cw1 = kw[256 - lane];
        const bool isA = lane >= 32;
        const char* bp = (const char*)kd + (isA ? 16 * (63 - lane) : 256 + 8 * (63 - lane));
        const u32x2 b01 = *(const u32x2*)bp, b23 = *(const u32x2*)(bp + 8);
        const unsigned P = __builtin_amdgcn_perm(0, dw1, 0x01000100), Q = __builtin_amdgcn_perm(0, dw0, 0x03020302),
                       M = __builtin_amdgcn_perm(0, dw0, 0x01000302), Z = __builtin_amdgcn_perm(0, dw0, 0x01000100);
        const unsigned c0 = __builtin_amdgcn_perm(0, cw1, 0x01000100), c1 = __builtin_amdgcn_perm(0, cw0, 0x03020302),
                       c2 = __builtin_amdgcn_perm(0, cw0, 0x01000302), c3 = __builtin_amdgcn_perm(0, cw0, 0x01000100);
        const unsigned o0 = __builtin_amdgcn_perm(b23.x, isA ? b23.y : b01.y, isA ? 0x01000302u : 0x03020504u),
                       o1 = __builtin_amdgcn_perm(b23.x, b01.y, isA ? 0x05040706u : 0x01000100u),
                       o2 = __builtin_amdgcn_perm(b01.y, b01.x, isA ? 0x05040706u : 0x03020504u),
                       o3 = __builtin_amdgcn_perm(0, b01.x, isA ? 0x01000302u : 0x01000100u);
        put_copies(mine + 3072 + lane * 16, o0, o1, o2, o3, next_lane(o0, 0), next_lane(o1, 0));
        put_copies(mine + 2048 + lane * 16, c0, c1, c2, c3, next_lane(c0, __builtin_amdgcn_readlane(o0, 0)),
                   next_lane(c1, __builtin_amdgcn_readlane(o1, 0)));
        put_copies(mine + lane * 32, P, P, Q, Q, Q, M);
        put_copies(mine + lane * 32 + 16, Q, M, Z, Z, next_lane(P, __builtin_amdgcn_readlane(c0, 0)),
                   next_lane(P, __builtin_amdgcn_readlane(c1, 0)));
        if (lane < 8) *(u32x4*)(mine + (lane >> 1) * CV_COPY + 4096 + (lane & 1) * 16) = (u32x4){0u, 0u, 0u, 0u};
        asm volatile("s_waitcnt vmcnt(0)" ::: "memory");
        TS(5);
        __syncthreads();
        TS(6);
        const unsigned aa = (unsigned)(size_t)(LDS_AS char*)(mine + (3 - (m & 3)) * CV_COPY + 2 * (2028 - 32 * 63 - 4 * (m >> 2) + 8 * hg));
        const unsigned ba = (unsigned)(size_t)(LDS_AS char*)(smem + KA_U + w * CV_UB + (2 * m + hg) * 16);
        f32x4 acc[8];
#pragma unroll
        for (int q = 0; q < 8; ++q) acc[q] = (f32x4){0.f, 0.f, 0.f, 0.f};
        CvFrag f0, f1;
        cv_first<0>(f0, aa, ba);
        cv_round<0>(f0, f1, acc, aa, ba);
        TS(7);
        const char* rp = minh + (16 * m + 4 * hg) * 4;
        char* hp = mine + (16 * m + 4 * hg) * 2;
#pragma unroll
        for (int q = 0; q < 8; ++q) {
            const f32x4 h = acc[q] + *(const f32x4*)(rp + q * 1024);
            *(u32x2*)(hp + q * 512) = (u32x2){pk2(h[0], h[1]), pk2(h[2], h[3])};
        }
    } else {
        const unsigned tag = etag * 0x85ebca6bu;
        u32x4 g[4];
        asm volatile("buffer_load_dwordx4 %0, %4, %5, 0 offen" GFIRST "\n\tbuffer_load_dwordx4 %1, %4, %5, 0 offen offset:16" GFIRST "\n\t"
                     "buffer_load_dwordx4 %2, %4, %5, 0 offen offset:32" GFIRST "\n\tbuffer_load_dwordx4 %3, %4, %5, 0 offen offset:48" GFIRST "\n\t"
                     "s_waitcnt vmcnt(4)"
                     : "=&v"(g[0]), "=&v"(g[1]), "=&v"(g[2]), "=&v"(g[3]) : "v"(t * 64), "s"(srdG) : "memory");
        float xf[8][4], dk[4];
#pragma unroll
        for (int k = 0; k < 8; ++k) unpack4(xf[k], xc[k]);
        unpack4(dk, xd);
        if (lane < 30) *(u32x4*)(smem + KA_U + (w - 4) * CV_UB + lane * 16) = (u32x4){0u, 0u, 0u, 0u};
        TS(3);
        asm volatile("s_waitcnt vmcnt(0)" : "+v"(g[0]), "+v"(g[1]), "+v"(g[2]), "+v"(g[3]) :: "memory");
        unsigned spins = 0;
#pragma unroll 1
        for (; spins < (1u << 16); ++spins) {
            const bool bad = ((g[0].y ^ tag) | (g[0].w ^ tag) | (g[1].y ^ tag) | (g[1].w ^ tag) |
                              (g[2].y ^ tag) | (g[2].w ^ tag) | (g[3].y ^ tag) | (g[3].w ^ tag)) != 0;
            if (!__any(bad)) break;
            if (bad)
                asm volatile("buffer_load_dwordx4 %0, %4, %5, 0 offen sc1\n\tbuffer_load_dwordx4 %1, %4, %5, 0 offen offset:16 sc1\n\t"
                             "buffer_load_dwordx4 %2, %4, %5, 0 offen offset:32 sc1\n\tbuffer_load_dwordx4 %3, %4, %5, 0 offen offset:48 sc1\n\t"
                             "s_waitcnt vmcnt(0)"
                             : "+v"(g[0]), "+v"(g[1]), "+v"(g[2]), "+v"(g[3]) : "v"(t * 64), "s"(srdG) : "memory");
        }
        TS(4);
        unsigned up[4][4];
#pragma unroll
        for (int k = 0; k < 4; ++k) {
            const float i0 = __builtin_amdgcn_rcpf(bfhi(g[k].x)), i1 = __builtin_amdgcn_rcpf(bfhi(g[k].z));
            const float m0 = bflo(g[k].x), m1 = bflo(g[k].z);
#pragma unroll
            for (int c = 0; c < 4; ++c) up[c][k] = pk2((xf[2 * k][c] - m0) * i0, (xf[2 * k + 1][c] - m1) * i1);
        }
#pragma unroll
        for (int c = 0; c < 4; ++c)
            *(u32x4*)(smem + KA_U + c * CV_UB + 480 + t * 16) = (u32x4){up[c][0], up[c][1], up[c][2], up[c][3]};
        TS(5);
        __syncthreads();
        if (blockIdx.x == 0 && tid == 256)
            __hip_atomic_store((gu32*)(ws + WS_EP), (etag ^ 0x9e3779b9u) + 1u, __ATOMIC_RELAXED, __HIP_MEMORY_SCOPE_AGENT);
#pragma unroll
        for (int c = 0; c < 4; ++c) {
            f32x4 r0, r1;
#pragma unroll
            for (int k = 0; k < 2; ++k) {
                r0[2 * k] = __builtin_fmaf(bflo(up[c][k]), dk[c], xf[2 * k][c]);
                r0[2 * k + 1] = __builtin_fmaf(bfhi(up[c][k]), dk[c], xf[2 * k + 1][c]);
                r1[2 * k] = __builtin_fmaf(bflo(up[c][2 + k]), dk[c], xf[4 + 2 * k][c]);
                r1[2 * k + 1] = __builtin_fmaf(bfhi(up[c][2 + k]), dk[c], xf[5 + 2 * k][c]);
            }
            *(f32x4*)(smem + KA_HI0 + c * KA_HI + t * 32) = r0;
            *(f32x4*)(smem + KA_HI0 + c * KA_HI + t * 32 + 16) = r1;
        }
        fetch_w2(w2, (w - 4) * 160 + blockIdx.x, W2C, 640, lds0 + KA_U + 4 * CV_UB, lane);
        asm volatile("s_waitcnt lgkmcnt(0)" ::: "memory");
        __builtin_amdgcn_s_barrier();
        asm volatile("s_waitcnt vmcnt(0)" ::: "memory");
    }
    __syncthreads();
    {
        const int ch = tid & 3;
        const int dt = grp / 20, rem = grp % 20, mq = rem / 5, j = rem % 5;
#pragma unroll
        for (int i = 0; i < 2; ++i) {
            const int piece = (tid >> 2) + 128 * i;
            const int unit = ((piece >> 2) * 40 + dt * 5 + j) * 64 + (piece & 3) * 16 + 4 * mq + ch;
            *(u32x4*)(ws + WS_HB + (size_t)unit * 16) = *(const u32x4*)(smem + ch * KA_LO + piece * 16);
        }
    }
    STAMP(0, 6);
    TSPUT(1, 0);
    TSPUT(5, 448);
    asm volatile(".Lka_conv_last:");
}

constexpr int B_REG = 40960;
constexpr int B_RED = 24576;
constexpr int B_TAB = 32768;
#define PPOS 5
#define NP1 6
#define NPRO 8
#define WL 4
#define PK23 1
#define TQ1 4
#define TQ2 9
#define RSTEPS 11
#define PLATE 2
#define EPW 1
#define G2D 4
#define PRE 2

__device__ __forceinline__ float gelu_tanh(float x) {
    const float p = __builtin_fmaf(x * x, -2.302208198f * 0.044715f, -2.302208198f);
    const float e = __builtin_amdgcn_exp2f(p * x);
    return x * __builtin_amdgcn_rcpf(1.0f + e);
}
template <int I>
__device__ __forceinline__ void w_piece(__amdgpu_buffer_rsrc_t srd, unsigned v, unsigned so, unsigned m0v) {
    if (I == 0) asm volatile("s_mov_b32 m0, %0\n\ts_nop 0\n\tbuffer_load_dwordx4 %1, %2, %3 offen lds"
                             :: "s"(m0v), "v"(v), "s"(srd), "s"(so) : "m0", "memory");
    else asm volatile("buffer_load_dwordx4 %0, %1, %2 offen offset:%3 lds"
                      :: "v"(v), "s"(srd), "s"(so), "n"(I * 1024) : "memory");
}
__device__ __forceinline__ void w_issue(__amdgpu_buffer_rsrc_t srd, const unsigned (&v)[4], unsigned so, unsigned m0v) {
    m0v = __builtin_amdgcn_readfirstlane(m0v);
    asm volatile("s_mov_b32 m0, %0\n\ts_nop 0\n\t"
                 "buffer_load_dwordx4 %1, %5, %6 offen lds\n\tbuffer_load_dwordx4 %2, %5, %6 offen offset:1024 lds\n\t"
                 "buffer_load_dwordx4 %3, %5, %6 offen offset:2048 lds\n\tbuffer_load_dwordx4 %4, %5, %6 offen offset:3072 lds"
                 :: "s"(m0v), "v"(v[0]), "v"(v[1]), "v"(v[2]), "v"(v[3]), "s"(srd), "s"(so) : "m0", "memory");
}
template <int SC, int SL, int J>
__device__ __forceinline__ void a_piece(__amdgpu_buffer_rsrc_t srd, unsigned av0, unsigned av1, unsigned so) {
    if (SC) asm volatile("buffer_load_dwordx4 a[%c0:%c1], %2, %3, %4 offen offset:%5 sc1"
                         :: "i"(SL * 20 + J * 4), "i"(SL * 20 + J * 4 + 3), "v"(J < 4 ? av0 : av1), "s"(srd), "s"(so), "n"((J & 3) * 1024) : "memory");
    else asm volatile("buffer_load_dwordx4 a[%c0:%c1], %2, %3, %4 offen offset:%5"
                      :: "i"(SL * 20 + J * 4), "i"(SL * 20 + J * 4 + 3), "v"(J < 4 ? av0 : av1), "s"(srd), "s"(so), "n"((J & 3) * 1024) : "memory");
}
template <int SC, int SL>
__device__ __forceinline__ void a_issue(__amdgpu_buffer_rsrc_t srd, unsigned av0, unsigned av1, unsigned so) {
    a_piece<SC, SL, 0>(srd, av0, av1, so); a_piece<SC, SL, 1>(srd, av0, av1, so); a_piece<SC, SL, 2>(srd, av0, av1, so);
    a_piece<SC, SL, 3>(srd, av0, av1, so); a_piece<SC, SL, 4>(srd, av0, av1, so);
}
template <int N> __device__ __forceinline__ void vm_wait() { asm volatile("s_waitcnt vmcnt(%0)" :: "n"(N) : "memory"); }
template <int R>
__device__ __forceinline__ void affine4(f32x4& d, u32x2 al, f32x4 be) {
    asm volatile("v_mfma_f32_4x4x4_16b_bf16 %0, %1, a[%c2:%c3], %4" : "=&v"(d) : "v"(al), "i"(R), "i"(R + 1), "v"(be));
}
template <int INIT>
__device__ __forceinline__ void mma_vv(f32x4& c, u32x4 a, u32x4 b, f32x4 c0) {
    if (INIT == 0) asm volatile("v_mfma_f32_16x16x32_bf16 %0, %1, %2, %0" : "+v"(c) : "v"(a), "v"(b));
    if (INIT == 1) asm volatile("v_mfma_f32_16x16x32_bf16 %0, %1, %2, 0" : "=&v"(c) : "v"(a), "v"(b));
    if (INIT == 2) asm volatile("v_mfma_f32_16x16x32_bf16 %0, %1, %2, %3" : "=&v"(c) : "v"(a), "v"(b), "v"(c0));
}
template <int INIT, int R>
__device__ __forceinline__ void mma_av(f32x4& c, u32x4 b, f32x4 c0) {
    if (INIT == 0) asm volatile("v_mfma_f32_16x16x32_bf16 %0, a[%c1:%c2], %3, %0" : "+v"(c) : "i"(R), "i"(R + 3), "v"(b));
    if (INIT == 1) asm volatile("v_mfma_f32_16x16x32_bf16 %0, a[%c1:%c2], %3, 0" : "=&v"(c) : "i"(R), "i"(R + 3), "v"(b));
    if (INIT == 2) asm volatile("v_mfma_f32_16x16x32_bf16 %0, a[%c1:%c2], %3, %4" : "=&v"(c) : "i"(R), "i"(R + 3), "v"(b), "v"(c0));
}
template <int INIT, int R, int Q>
__device__ __forceinline__ void mma_aa(f32x4& c, f32x4 c0) {
    if (INIT == 0) asm volatile("v_mfma_f32_16x16x32_bf16 %0, a[%c1:%c2], a[%c3:%c4], %0" : "+v"(c) : "i"(R), "i"(R + 3), "i"(Q), "i"(Q + 3));
    if (INIT == 1) asm volatile("v_mfma_f32_16x16x32_bf16 %0, a[%c1:%c2], a[%c3:%c4], 0" : "=&v"(c) : "i"(R), "i"(R + 3), "i"(Q), "i"(Q + 3));
    if (INIT == 2) asm volatile("v_mfma_f32_16x16x32_bf16 %0, a[%c1:%c2], a[%c3:%c4], %5" : "=&v"(c) : "i"(R), "i"(R + 3), "i"(Q), "i"(Q + 3), "v"(c0));
}
#define SB() __builtin_amdgcn_sched_barrier(0)
struct Nrm { u32x2 a0, a1; f32x4 b0, b1; };
struct KbCtx {
    __amdgpu_buffer_rsrc_t srdW1, srdW2, srdL, srdA, srdH;
    unsigned wv1[4], wv2[4], av0, av1, ring, w1o, a1o, w2o, a2o, wlo[2], alo[2];
    const char* tp[4];
    unsigned tpa[4];
    const char* ta;
    const char* tb;
    char* red;
    f32x4 bias;
};
__device__ __forceinline__ unsigned w1_off(const KbCtx& c, int g) { return c.w1o + g * (32 * F_ * 2); }
__device__ __forceinline__ unsigned a1_off(const KbCtx& c, int g) { return c.a1o + g * 40960; }
__device__ __forceinline__ unsigned w2_off(const KbCtx& c, int g) { return g >= 12 ? c.wlo[g & 1] : c.w2o + g * (32 * L_ * 2); }
__device__ __forceinline__ unsigned a2_off(const KbCtx& c, int g) { return g >= 12 ? c.alo[g & 1] : c.a2o + g * 40960; }
template <int R>
__device__ __forceinline__ void w_read(u32x4 (&wf)[4], const KbCtx& c, int SL) {
    const u32x2 v = tr_read(c.tp[R >> 1] + SL * 4096 + (R & 1) * 512);
    if (R & 1) { wf[R >> 1].z = v.x; wf[R >> 1].w = v.y; } else { wf[R >> 1].x = v.x; wf[R >> 1].y = v.y; }
}
template <int I>
__device__ __forceinline__ void nrm_read(Nrm& t, const KbCtx& c, int S) {
    if (I == 0) { t.a0 = *(const u32x2*)(c.ta + S * 256); t.a1 = *(const u32x2*)(c.ta + S * 256 + 32); }
    if (I == 1) t.b0 = *(const f32x4*)(c.tb + S * 128);
    if (I == 2) t.b1 = *(const f32x4*)(c.tb + S * 128 + 16);
}
template <int T>
__device__ __forceinline__ void red_put(const f32x4 (&acc)[4][5], const KbCtx& c) {
    if constexpr (T >= 8) *(f32x4*)(c.red + (T - 8) * 1024) = acc[(T - 8) / 5 + 1][(T - 8) % 5];
    if constexpr (T == 19) {
        SB();
#pragma unroll
        for (int j = 2; j < 5; ++j) *(f32x4*)(c.red + (10 + j) * 1024) = acc[3][j];
    }
}
constexpr int G1Q = (16 - NPRO) * 9;
constexpr int g1_cum(int s) {
    return s <= RSTEPS ? (G1Q * s + RSTEPS - 1) / RSTEPS : G1Q + (s >= 15 ? 4 * WL : 4 * WL * (s - RSTEPS) / (15 - RSTEPS));
}
template <int S, int P>
__device__ __forceinline__ void g1_req(const KbCtx& c) {
    constexpr int q = g1_cum(S) + P;
    static_assert(g1_cum(S + 1) - g1_cum(S) <= 9, "");
    if constexpr (q < g1_cum(S + 1)) {
        if constexpr (q < G1Q) {
            constexpr int g = NPRO + q / 9, r = q % 9;
            static_assert(g - 8 <= S && g >= S + 2, "");
            if constexpr (r < 4) w_piece<r>(c.srdW1, c.wv1[r], w1_off(c, g), c.ring + (g & 7) * 4096);
            else a_piece<0, g & 7, r - 4>(c.srdH, c.av0, c.av1, a1_off(c, g));
        } else {
            constexpr int g = (q - G1Q) / 4, r = (q - G1Q) % 4;
            static_assert(g + 8 <= S, "");
            w_piece<r>(c.srdW2, c.wv2[r], w2_off(c, g), c.ring + g * 4096);
        }
    }
}
template <int SL, int R>
__device__ __forceinline__ void w_park(const KbCtx& c) {
    asm volatile("ds_read_b64_tr_b16 a[%c0:%c1], %2 offset:%3"
                 :: "i"(160 + SL * 16 + R * 2), "i"(161 + SL * 16 + R * 2), "v"(c.tpa[R >> 1]), "n"(SL * 4096 + (R & 1) * 512) : "memory");
}
template <int SL> __device__ __forceinline__ void w_park8(const KbCtx& c) {
    w_park<SL, 0>(c); w_park<SL, 1>(c); w_park<SL, 2>(c); w_park<SL, 3>(c); w_park<SL, 4>(c); w_park<SL, 5>(c); w_park<SL, 6>(c); w_park<SL, 7>(c);
}
template <int S, int T>
__device__ __forceinline__ void g1_mma(f32x4 (&acc)[4][5], u32x4 (&n)[2][5], u32x4 (&wf)[2][4], const KbCtx& c) {
    mma_vv<S ? 0 : (T & 3) ? 1 : 2>(acc[T & 3][T >> 2], wf[S & 1][T & 3], n[S & 1][T >> 2], c.bias);
    SB();
}
constexpr int g1_aux(int t) {
    constexpr int a[20] = {0, 1, 2, 3, 4, -1, 5, -2, 6, -3, 7, -4, 8, 9, 10, -5, -6, -7, -8, -9};
    return a[t];
}
template <int S, int T>
__device__ __forceinline__ void g1_body(f32x4 (&acc)[4][5], u32x4 (&n)[2][5], u32x4 (&wf)[2][4], f32x4 (&d)[10], Nrm& t, const KbCtx& c) {
    g1_mma<S, T>(acc, n, wf, c);
    n[(S + 1) & 1][T >> 2][T & 3] = pk2(d[T >> 1][(T & 1) * 2], d[T >> 1][(T & 1) * 2 + 1]);
    constexpr int a = g1_aux(T);
    if constexpr (a < 0) g1_req<S, -1 - a>(c);
    if constexpr (a >= 3) w_read<a - 3>(wf[(S + 1) & 1], c, (S + 1) & 7);
    if constexpr (a >= 0 && a < 3 && S < 14) nrm_read<a>(t, c, S + 2);
    SB();
    if constexpr (T < 19) g1_body<S, T + 1>(acc, n, wf, d, t, c);
}
template <int S, int T>
__device__ __forceinline__ void g1_mid(f32x4 (&d)[10], const Nrm& t) {
    affine4<(S & 7) * 20 + T * 2>(d[T], (T & 1) ? t.a1 : t.a0, (T & 1) ? t.b1 : t.b0);
    SB();
    if constexpr (T < 9) g1_mid<S, T + 1>(d, t);
}
template <int T>
__device__ __forceinline__ void g1_last(f32x4 (&acc)[4][5], u32x4 (&n)[5], u32x4 (&wf)[4], const KbCtx& c) {
    constexpr int i = (T / 5 + 1) & 3, j = T % 5;
    mma_vv<0>(acc[i][j], wf[i], n[j], c.bias);
    SB();
    if constexpr (T < 8) { w_park<T / 4, (T & 3) * 2>(c); w_park<T / 4, (T & 3) * 2 + 1>(c); }
    red_put<T>(acc, c);
    constexpr int q0 = (24 - 4 * WL) * T / 20, q1 = (24 - 4 * WL) * (T + 1) / 20;
    if constexpr (q1 > q0) w_piece<q0 & 3>(c.srdW2, c.wv2[q0 & 3], w2_off(c, WL + q0 / 4), c.ring + (WL + q0 / 4) * 4096);
    SB();
    if constexpr (T < 19) g1_last<T + 1>(acc, n, wf, c);
}
template <int N> __device__ __forceinline__ void all_wait() {
    asm volatile("" ::: "memory");
    __builtin_amdgcn_s_waitcnt((N & 15) | 0x70 | ((N >> 4) << 14));
    asm volatile("" ::: "memory");
    SB();
}
template <int S>
__device__ __forceinline__ void g1_step(f32x4 (&acc)[4][5], u32x4 (&n)[2][5], u32x4 (&wf)[2][4], Nrm& t, const KbCtx& c) {
    if constexpr (S == 15) {
        all_wait<4 * WL - 8>();
        g1_last<0>(acc, n[1], wf[1], c);
    } else {
        constexpr int young = NPRO * 9 + g1_cum(S) - (S + 2) * 9;
        static_assert(young >= 0 && young + g1_cum(S + 1) - g1_cum(S) <= 63, "");
        all_wait<young>();
        f32x4 d[10];
        g1_mid<S + 1, 0>(d, t);
        g1_body<S, 0>(acc, n, wf, d, t, c);
        g1_step<S + 1>(acc, n, wf, t, c);
    }
}
constexpr int g2_slot(int s) { return s < 6 ? s : s < 10 ? s - 6 : s - 4; }
template <int S, int P>
__device__ __forceinline__ void g2_req(const KbCtx& c) {
    if constexpr (S < 14) a_piece<1, S & 7, P>(c.srdA, c.av0, c.av1, a2_off(c, S));
}
template <int S, int T>
__device__ __forceinline__ void g2_body(f32x4 (&acc)[4][5], u32x4 (&wf)[2][4], const KbCtx& c) {
    constexpr int i = (T / 5 + 1) & 3, j = T % 5;
    if constexpr (S < 4) mma_aa<S ? 0 : i ? 1 : 2, (S & 7) * 20 + j * 4, 160 + S * 16 + i * 4>(acc[i][j], c.bias);
    else mma_av<0, (S & 7) * 20 + j * 4>(acc[i][j], wf[S & 1][i], c.bias);
    SB();
    if constexpr (S == 13) red_put<T>(acc, c);
    else {
        if constexpr (T % 4 == 0) g2_req<S + G2D, T / 4>(c);
        else if constexpr (S >= 3 && T > 5 && T < 17) w_read<(T - T / 4 - 3) & 7>(wf[(S + 1) & 1], c, g2_slot(S + 1));
    }
    SB();
    if constexpr (T < 19) g2_body<S, T + 1>(acc, wf, c);
}
template <int S>
__device__ __forceinline__ void g2_step(f32x4 (&acc)[4][5], u32x4 (&wf)[2][4], const KbCtx& c) {
    vm_wait<5 * (S + G2D > 14 ? 13 - S : G2D - 1)>();
    if (S == 0) STAMPX(4, 1);
    SB();
    g2_body<S, 0>(acc, wf, c);
    if constexpr (S < 13) g2_step<S + 1>(acc, wf, c);
}
__device__ __forceinline__ void kb_reduce(f32x4 (&r)[5], const f32x4 (&acc)[4][5], char* smem, int w, int lane) {
    asm volatile("s_waitcnt lgkmcnt(0)\n\ts_barrier" ::: "memory");
    f32x4 p[5][3];
#pragma unroll
    for (int j = 0; j < 5; ++j)
#pragma unroll
        for (int i = 1; i < 4; ++i)
            p[j][i - 1] = *(const f32x4*)(smem + ((w - i) & 3) * B_REG + B_RED + lane * 16 + (i - 1) * 5120 + j * 1024);
    SB();
#pragma unroll
    for (int j = 0; j < 5; ++j) r[j] = (acc[0][j] + p[j][0]) + (p[j][1] + p[j][2]);
}
template <int S0, int S1>
__device__ __forceinline__ void g1_groups(const KbCtx& c) {
    if constexpr (S0 < S1) {
        if (S0 >= 3) vm_wait<54>();
        w_issue(c.srdW1, c.wv1, w1_off(c, S0), c.ring + S0 * 4096);
        a_issue<0, S0>(c.srdH, c.av0, c.av1, a1_off(c, S0));
        g1_groups<S0 + 1, S1>(c);
    }
}
template <int Q0, int Q1>
__device__ __forceinline__ void g1_tail(const KbCtx& c) {
    if constexpr (Q0 < Q1 && Q0 < (NPRO - NP1) * 9) {
        constexpr int g = NP1 + Q0 / 9, r = Q0 % 9;
        if constexpr (r < 4) w_piece<r>(c.srdW1, c.wv1[r], w1_off(c, g), c.ring + g * 4096);
        else a_piece<0, g, r - 4>(c.srdH, c.av0, c.av1, a1_off(c, g));
        g1_tail<Q0 + 1, Q1>(c);
    }
    SB();
}
template <int T>
__device__ __forceinline__ void g1_mid0(f32x4 (&d)[10], const Nrm& t, const KbCtx& c) {
    affine4<T * 2>(d[T], (T & 1) ? t.a1 : t.a0, (T & 1) ? t.b1 : t.b0);
    SB();
    if constexpr (T & 1) g1_tail<TQ2 + T / 2, TQ2 + T / 2 + 1>(c);
    if constexpr (T < 9) g1_mid0<T + 1>(d, t, c);
}
template <int S>
__device__ __forceinline__ void g2_first(const KbCtx& c) {
    a_issue<1, S>(c.srdA, c.av0, c.av1, a2_off(c, S));
    if constexpr (S < G2D - 1) g2_first<S + 1>(c);
}
template <int CTRL> __device__ __forceinline__ f32x2 dpp_add2(f32x2 v) { return (f32x2){dpp_add<CTRL>(v.x), dpp_add<CTRL>(v.y)}; }

extern "C" __global__ __launch_bounds__(256, 1) void kb_mlp(const u16* __restrict__ w1, const u16* __restrict__ b1, const u16* __restrict__ w2,
                                                 const u16* __restrict__ b2, char* ws, u16* __restrict__ out) {
    __shared__ __attribute__((aligned(16))) char smem[4 * B_REG];
    const int tid = threadIdx.x, lane = tid & 63;
    const int w = __builtin_amdgcn_readfirstlane(tid >> 6);
    STAMP(2, 0);
    const int n = lane & 15, hg = lane >> 4;
    KbCtx c;
    c.srdA = __builtin_amdgcn_make_buffer_rsrc((void*)ws, (short)0, WS_END, 0x00020000);
    u32x4 hs[3];
    {
        const unsigned v = hg * 1024 + n * 16;
        asm volatile("buffer_load_dwordx4 %0, %3, %5, %6 offen\n\tbuffer_load_dwordx4 %1, %3, %5, %7 offen\n\tbuffer_load_dwordx4 %2, %4, %5, %7 offen"
                     : "=&v"(hs[0]), "=&v"(hs[1]), "=&v"(hs[2])
                     : "v"(v), "v"(v + 4096 + (lane >> 5) * WS_END), "s"(c.srdA),
                       "s"(WS_HB + ((blockIdx.x >> 2) * 40 + 10 * w) * 1024 + (blockIdx.x & 3) * 256),
                       "s"(WS_HB + ((blockIdx.x >> 2) * 40 + 10 * w + 4) * 1024 + (blockIdx.x & 3) * 256) : "memory");
    }
    const int xcd = blockIdx.x & 7, idx = blockIdx.x >> 3;
    const bool g1 = idx < 27;
    const int t1 = (xcd >> 1) * 27 + (g1 ? idx : 0), t2 = (xcd >> 1) * 32 + idx;
    const int mt1 = t1 >> 2, dt1 = (xcd & 1) * 4 + (t1 & 3), mt2 = mt2_of(xcd >> 1, idx >> 2), dt2 = (xcd & 1) * 4 + (t2 & 3);
    char* ring = smem + w * B_REG;
    c.ring = (unsigned)(size_t)(LDS_AS char*)ring;
    c.srdW1 = __builtin_amdgcn_make_buffer_rsrc((void*)w1, (short)0, g1 ? L_ * F_ * 2 : 0, 0x00020000);
    c.srdH = __builtin_amdgcn_make_buffer_rsrc((void*)ws, (short)0, g1 ? WS_END : 0, 0x00020000);
    c.srdW2 = __builtin_amdgcn_make_buffer_rsrc((void*)w2, (short)0, L_ * F_ * 2, 0x00020000);
    const bool idw = w == 3;
    c.srdL = __builtin_amdgcn_make_buffer_rsrc((void*)(idw ? ws + WS_ID : (const char*)w2), (short)0, L_ * F_ * 2, 0x00020000);
    {
        const int wr = lane >> 3, wslot = lane & 7;
        const unsigned s0 = ((((wslot >> 1) ^ ((wr >> 1) & 1)) * 2 + (wslot & 1)) * 8) * 2;
        const unsigned s1 = ((((wslot >> 1) ^ (((wr >> 1) & 1) | 2)) * 2 + (wslot & 1)) * 8) * 2;
        c.wv1[0] = wr * F_ * 2 + s0; c.wv1[1] = (wr + 8) * F_ * 2 + s1 - 1024;
        c.wv1[2] = c.wv1[0] + 16 * F_ * 2 - 2048; c.wv1[3] = c.wv1[1] + 16 * F_ * 2 - 2048;
        c.wv2[0] = wr * L_ * 2 + s0; c.wv2[1] = (wr + 8) * L_ * 2 + s1 - 1024;
        c.wv2[2] = c.wv2[0] + 16 * L_ * 2 - 2048; c.wv2[3] = c.wv2[1] + 16 * L_ * 2 - 2048;
    }
    c.av0 = lane * 16; c.av1 = lane * 16 + 4096;
    c.w1o = (w * 16 * 32 * F_ + mt1 * 64) * 2;
    c.a1o = WS_HB + (w * 16 * 40 + dt1 * 5) * 1024;
    c.w2o = (w * 14 * 32 * L_ + mt2 * 64) * 2;
    c.a2o = WS_GB + (w * 14 * 40 + dt2 * 5) * 1024;
#pragma unroll
    for (int i = 0; i < 2; ++i) {
        c.wlo[i] = idw ? i * (32 * L_ * 2) : c.w2o + (12 + i) * (32 * L_ * 2);
        c.alo[i] = idw ? WS_HB + ((mt2 * 2 + i) * 40 + dt2 * 5) * 1024 : c.a2o + (12 + i) * 40960;
    }
    u32x2 bb;
    asm volatile("global_load_dwordx2 %0, %1, %2" : "=v"(bb) : "v"(hg * 8), "s"(b1 + mt1 * 64 + 16 * w) : "memory");
    asm volatile("" ::: "a0", "a223");
    g1_groups<0, PRE>(c);
    {
        const int q4 = n >> 2, p4 = n & 3;
        const int fsw = ((q4 >> 1) & 1) | ((hg & 1) << 1);
        const char* tb = ring + (8 * hg + q4) * 128 + p4 * 8;
#pragma unroll
        for (int i = 0; i < 4; ++i) {
            c.tp[i] = tb + ((((i + w) & 3) ^ fsw) * 32);
            c.tpa[i] = (unsigned)(size_t)(LDS_AS const char*)c.tp[i];
        }
        c.ta = ring + B_TAB + (8 * hg + (lane & 3)) * 8;
        c.tb = ring + B_TAB + 4096 + hg * 32;
        c.red = ring + B_RED + lane * 16;
    }
    const __amdgpu_buffer_rsrc_t srdG = __builtin_amdgcn_make_buffer_rsrc((void*)(ws + WS_G2), (short)0, L_ * 8, 0x00020000);
    {
        const unsigned one = (n & 1) ? 0x3f800000u : 0x3f80u;
        const int dw = (hg & 1) == (n >> 3) ? (n >> 1) & 3 : 4;
        const u32x4 sel = {dw == 0 ? one : 0u, dw == 1 ? one : 0u, dw == 2 ? one : 0u, dw == 3 ? one : 0u};
        const u32x4 z4 = {0u, 0u, 0u, 0u};
        const u32x4 selA = hg < 2 ? sel : z4, selB = hg < 2 ? z4 : sel;
        f32x4 sum, x0, x1, x2, x3, x4;
        asm volatile("s_waitcnt vmcnt(%12)\n\t"
                     "v_mfma_f32_16x16x32_bf16 %1, %6, %9, 0\n\tv_mfma_f32_16x16x32_bf16 %2, %6, %10, 0\n\t"
                     "v_mfma_f32_16x16x32_bf16 %3, %7, %9, 0\n\tv_mfma_f32_16x16x32_bf16 %4, %7, %10, 0\n\t"
                     "v_mfma_f32_16x16x32_bf16 %5, %8, %9, 0\n\tv_mfma_f32_16x16x32_bf16 %0, %6, %11, 0\n\t"
                     "v_mfma_f32_16x16x32_bf16 %0, %7, %11, %0\n\tv_mfma_f32_16x16x32_bf16 %0, %8, %11, %0\n\ts_nop 4"
                     : "=&v"(sum), "=&v"(x0), "=&v"(x1), "=&v"(x2), "=&v"(x3), "=&v"(x4)
                     : "v"(hs[0]), "v"(hs[1]), "v"(hs[2]), "v"(selA), "v"(selB), "v"(sel), "n"(9 * PRE + 1) : "memory");
        STAMPX(2, 1);
        f32x4 q = (x0 * x0 + x1 * x1) + (x2 * x2 + x3 * x3) + x4 * x4;
        asm volatile("s_nop 2" : "+v"(sum), "+v"(q));
        char* sc = smem + B_TAB + 6144;
        *(f32x2*)(sc + (n & 7) * 256 + (w * 8 + hg * 2 + (n >> 3)) * 8) = (f32x2){(sum[0] + sum[1]) + (sum[2] + sum[3]), (q[0] + q[1]) + (q[2] + q[3])};
        asm volatile("s_waitcnt lgkmcnt(0)\n\ts_barrier" ::: "memory");
        if (w == 0) {
            const f32x4 p0 = *(const f32x4*)(sc + lane * 32), p1 = *(const f32x4*)(sc + lane * 32 + 16);
            f32x2 v = {(p0[0] + p0[2]) + (p1[0] + p1[2]), (p0[1] + p0[3]) + (p1[1] + p1[3])};
            v = dpp_add2<0xb1>(v);
            v = dpp_add2<0x4e>(v);
            v = dpp_add2<0x141>(v);
            const float mean = v.x * (1.0f / D_);
            const float var = fmaxf(__builtin_fmaf(-mean, mean, v.y * (1.0f / D_)), 0.f);
            const float m1 = rb(mean), d1 = rb(__builtin_amdgcn_sqrtf(rb(rb(var) + 1e-5f)));
            const float i1 = __builtin_amdgcn_rcpf(d1);
            const float m2 = rb((mean - m1) * i1), d2 = rb(__builtin_amdgcn_sqrtf(rb(rb(var * i1 * i1) + 1e-5f)));
            const float al = rb(i1 * __builtin_amdgcn_rcpf(d2));
            const float be = -__builtin_fmaf(m2, d1, m1) * al;
            asm volatile("buffer_store_dwordx2 %0, %1, %2, %3 offen sc1"
                         :: "v"((u32x2){__float_as_uint(be), __float_as_uint(al) | 1u}), "v"((lane & 7) * (L_ * 8) + lane), "s"(srdG), "s"(blockIdx.x * 64) : "memory");
            STAMP(2, 2);
        }
    }
    f32x4 acc[4][5];
    u32x4 wf[2][4];
    unsigned f, bq;
    const unsigned fo = (lane < (w == 3 ? 6 : 7) ? lane : 0) * 128;
    const char* fp = ws + WS_FL + (dt2 * 32 + 7 * w) * 128;
    if (g1) {
        g1_groups<PRE, PPOS>(c);
        u32x4 g[4];
        const unsigned gvo = lane * 64;
        asm volatile("s_waitcnt vmcnt(59)\n\t"
                     "buffer_load_dwordx4 %0, %4, %5, %6 offen sc1\n\tbuffer_load_dwordx4 %1, %4, %5, %6 offen offset:16 sc1\n\t"
                     "buffer_load_dwordx4 %2, %4, %5, %6 offen offset:32 sc1\n\tbuffer_load_dwordx4 %3, %4, %5, %6 offen offset:48 sc1"
                     : "=&v"(g[0]), "=&v"(g[1]), "=&v"(g[2]), "=&v"(g[3]) : "v"(gvo), "s"(srdG), "s"(w * 4096) : "memory");
        g1_groups<PPOS, NP1>(c);
        STAMPX(2, 3);
        asm volatile("s_waitcnt vmcnt(%5)" : "+v"(g[0]), "+v"(g[1]), "+v"(g[2]), "+v"(g[3]), "+v"(bb) : "n"(9 * (NP1 - PPOS)) : "memory");
        STAMPX(2, 4);
        unsigned spins = 0;
#pragma unroll 1
        for (; spins < (1u << 16); ++spins) {
            if (__all(g[0].y & g[0].w & g[1].y & g[1].w & g[2].y & g[2].w & g[3].y & g[3].w & 1u)) break;
            asm volatile("buffer_load_dwordx4 %0, %4, %5, %6 offen sc1\n\tbuffer_load_dwordx4 %1, %4, %5, %6 offen offset:16 sc1\n\t"
                         "buffer_load_dwordx4 %2, %4, %5, %6 offen offset:32 sc1\n\tbuffer_load_dwordx4 %3, %4, %5, %6 offen offset:48 sc1\n\t"
                         "s_waitcnt vmcnt(0)"
                         : "=&v"(g[0]), "=&v"(g[1]), "=&v"(g[2]), "=&v"(g[3]) : "v"(gvo), "s"(srdG), "s"(w * 4096) : "memory");
        }
        STAMPX(2, 5);
        {
            char* tw = ring + B_TAB + lane * 64;
#pragma unroll
            for (int k = 0; k < 4; ++k) {
                const unsigned lo = g[k].y >> 16, hi = g[k].w & 0xffff0000u;
                *(u32x4*)(tw + k * 16) = (k & 1) ? (u32x4){0u, lo, 0u, hi} : (u32x4){lo, 0u, hi, 0u};
            }
            *(u32x4*)(ring + B_TAB + 4096 + lane * 32) = (u32x4){g[0].x, g[0].z, g[1].x, g[1].z};
            *(u32x4*)(ring + B_TAB + 4096 + lane * 32 + 16) = (u32x4){g[2].x, g[2].z, g[3].x, g[3].z};
            asm volatile("" ::: "memory");
        }
        Nrm t;
        u32x4 nf[2][5];
        nrm_read<0>(t, c, 0); nrm_read<1>(t, c, 0); nrm_read<2>(t, c, 0);
        SB();
        g1_tail<0, TQ1>(c);
        w_read<0>(wf[0], c, 0); w_read<1>(wf[0], c, 0); w_read<2>(wf[0], c, 0); w_read<3>(wf[0], c, 0);
        w_read<4>(wf[0], c, 0); w_read<5>(wf[0], c, 0); w_read<6>(wf[0], c, 0); w_read<7>(wf[0], c, 0);
        SB();
        g1_tail<TQ1, TQ2>(c);
        {
            c.bias = (f32x4){bflo(bb.x), bfhi(bb.x), bflo(bb.y), bfhi(bb.y)};
            asm volatile("" : "+v"(c.bias));
            f32x4 d[10];
            SB();
            g1_mid0<0>(d, t, c);
            nrm_read<0>(t, c, 1); nrm_read<1>(t, c, 1); nrm_read<2>(t, c, 1);
            asm volatile("s_nop 4");
            SB();
#pragma unroll
            for (int k = 0; k < 20; ++k) nf[0][k >> 2][k & 3] = pk2(d[k >> 1][(k & 1) * 2], d[k >> 1][(k & 1) * 2 + 1]);
            SB();
            g1_tail<TQ2 + 5, 27>(c);
        }
        STAMP(3, 1);
        g1_step<0>(acc, nf, wf, t, c);
        STAMP(3, 2);
        f32x4 r[5];
        kb_reduce(r, acc, smem, w, lane);
        STAMPX(3, 3);
        const unsigned go = WS_GB + ((mt1 * 2 + (w >> 1)) * 40 + dt1 * 5) * 1024;
        const unsigned gv = (((w & 1) * 2 + (hg >> 1)) * 16 + n) * 16 + (hg & 1) * 8;
        if (!PK23) {
            vm_wait<8>();
            w_park8<2>(c);
            w_park8<3>(c);
        }
#pragma unroll
        for (int j = 0; j < 5; ++j) {
            if (j == 2) asm volatile("s_waitcnt lgkmcnt(0)" ::: "memory");
            if (j < EPW) w_issue(c.srdW2, c.wv2, w2_off(c, 6 + j), c.ring + j * 4096);
            SB();
            u32x2 o;
            o.x = pk2(gelu_tanh(r[j][0]), gelu_tanh(r[j][1]));
            o.y = pk2(gelu_tanh(r[j][2]), gelu_tanh(r[j][3]));
            __builtin_amdgcn_raw_buffer_store_b64(o, c.srdA, gv + j * 1024, go, 16);
            SB();
        }
        STAMPX(3, 4);
        asm volatile("s_waitcnt vmcnt(0)" ::: "memory");
        if (PK23) {
            w_park8<2>(c);
            w_park8<3>(c);
        }
        asm volatile("s_barrier" ::: "memory");
        if (tid == 0) __hip_atomic_store((gu32*)(ws + WS_FL + (dt1 * 32 + mt1) * 128), 1u, __ATOMIC_RELAXED, __HIP_MEMORY_SCOPE_AGENT);
        STAMP(3, 5);
        asm volatile("s_waitcnt lgkmcnt(0)" ::: "memory");
#pragma unroll
        for (int s = 6 + EPW; s < 14; ++s) {
            if (s == 14 - PLATE)
                asm volatile("global_load_ushort %1, %4, %5\n\tglobal_load_dword %0, %2, %3 sc1"
                             : "=&v"(f), "=&v"(bq) : "v"(fo), "s"(fp), "v"(n * 2), "s"(b2 + mt2 * 64 + 16 * w) : "memory");
            w_issue(s < 12 ? c.srdW2 : c.srdL, c.wv2, w2_off(c, s), c.ring + g2_slot(s) * 4096);
        }
        asm volatile("s_waitcnt vmcnt(%2)" : "+v"(f), "+v"(bq) : "n"(4 * PLATE) : "memory");
    } else {
        const unsigned t0 = (unsigned)__builtin_amdgcn_s_memrealtime();
#pragma unroll
        for (int s = 0; s < 6; ++s) w_issue(c.srdW2, c.wv2, w2_off(c, s), c.ring + s * 4096);
        vm_wait<0>();
        w_park8<0>(c); w_park8<1>(c); w_park8<2>(c); w_park8<3>(c);
        asm volatile("s_waitcnt lgkmcnt(0)" ::: "memory");
#pragma unroll
        for (int s = 6; s < 13; ++s) w_issue(s < 12 ? c.srdW2 : c.srdL, c.wv2, w2_off(c, s), c.ring + g2_slot(s) * 4096);
        fetch_w2(w2, W2N + ((idx - 27) * 4 + w) * 8 + xcd, 864, 160, c.ring + 9 * 4096, lane);
        pause_until(t0, KT);
        warm_w<1>(w2, b2, xcd, (idx - 27) * 4 + w, 20, c.ring + 9 * 4096, lane);
        vm_wait<0>();
        w_issue(c.srdL, c.wv2, w2_off(c, 13), c.ring + g2_slot(13) * 4096);
        asm volatile("global_load_ushort %1, %4, %5\n\tglobal_load_dword %0, %2, %3 sc1\n\ts_waitcnt vmcnt(0)"
                     : "=&v"(f), "=&v"(bq) : "v"(fo), "s"(fp), "v"(n * 2), "s"(b2 + mt2 * 64 + 16 * w) : "memory");
        STAMP(3, 6);
    }
    {
        unsigned spins = 0;
#pragma unroll 1
        for (; spins < (1u << 16); ++spins) {
            if (__all(f != 0)) break;
            asm volatile("global_load_dword %0, %1, %2 sc1\n\ts_waitcnt vmcnt(0)" : "=&v"(f) : "v"(fo), "s"(fp) : "memory");
        }
    }
    STAMP(4, 0);
    g2_first<0>(c);
    {
        const float b = bflo(bq);
        c.bias = (f32x4){b, b, b, b};
        asm volatile("" : "+v"(c.bias));
    }
    SB();
    g2_step<0>(acc, wf, c);
    STAMP(4, 2);
    f32x4 r[5];
    kb_reduce(r, acc, smem, w, lane);
    STAMPX(4, 3);
    {
        u16* op = out + (size_t)(mt2 * 64 + 16 * w + n) * D_ + dt2 * 80 + 20 * hg;
        typedef unsigned u32x4u __attribute__((ext_vector_type(4), aligned(8)));
        u32x4u o0 = {pk2(r[0][0], r[0][1]), pk2(r[0][2], r[0][3]), pk2(r[1][0], r[1][1]), pk2(r[1][2], r[1][3])};
        u32x4u o1 = {pk2(r[2][0], r[2][1]), pk2(r[2][2], r[2][3]), pk2(r[3][0], r[3][1]), pk2(r[3][2], r[3][3])};
        u32x2 o2 = {pk2(r[4][0], r[4][1]), pk2(r[4][2], r[4][3])};
        *(u32x4u*)op = o0;
        *(u32x4u*)(op + 8) = o1;
        *(u32x2*)(op + 16) = o2;
    }
    STAMP(4, 4);
    asm volatile(".Lkb_mlp_last:");
}

extern "C" void kernel_launch(void* const* d_in, const int* in_sizes, int n_in, void* d_out, int out_size,
                              void* d_ws, size_t ws_size, hipStream_t stream) {
    const u16* x = (const u16*)d_in[0];
    const u16* logA = (const u16*)d_in[1];
    const u16* Aim = (const u16*)d_in[2];
    const u16* Cre = (const u16*)d_in[3];
    const u16* Cim = (const u16*)d_in[4];
    const u16* logdt = (const u16*)d_in[5];
    const u16* dskip = (const u16*)d_in[6];
    const u16* w1 = (const u16*)d_in[8];
    const u16* b1 = (const u16*)d_in[9];
    const u16* w2 = (const u16*)d_in[10];
    const u16* b2 = (const u16*)d_in[11];
    char* ws = (char*)d_ws;
    ka_conv<<<256, 512, 0, stream>>>(x, logA, Aim, Cre, Cim, logdt, dskip, ws, w1, b1, w2, b2);
    kb_mlp<<<256, 256, 0, stream>>>(w1, b1, w2, b2, ws, (u16*)d_out);
}
```

```cpp
#include <hip/hip_runtime.h>
#include <hip/hip_bf16.h>
#include <stdint.h>
#pragma clang diagnostic ignored "-Winline-asm"

typedef unsigned short u16;
typedef __bf16 bf16x8 __attribute__((ext_vector_type(8)));
typedef __bf16 bf16x2 __attribute__((ext_vector_type(2)));
typedef short s16x4 __attribute__((ext_vector_type(4)));
typedef float f32x4 __attribute__((ext_vector_type(4)));
typedef float f32x2 __attribute__((ext_vector_type(2)));
typedef unsigned u32x4 __attribute__((ext_vector_type(4)));
typedef unsigned u32x2 __attribute__((ext_vector_type(2)));
#define LDS_AS __attribute__((address_space(3)))

#define STAMP(k, i) do { } while (0)
#define STAMPT(k, i, T) do { } while (0)

#define TS(i) do { } while (0)
#define TSPUT(k, T) do { } while (0)
#define STAMPX(k, i) do { } while (0)
#define FSTAMP STAMP
constexpr int L_ = 2048;
constexpr int D_ = 640;
constexpr int N_ = 128;
constexpr int F_ = 1728;
constexpr unsigned SZ_ = L_ * D_ * 2;
constexpr unsigned WS_HB = 0, WS_GB = SZ_, WS_END = 2 * SZ_;
constexpr unsigned WS_ID = 2 * SZ_, WS_GR = WS_ID + 64 * L_ * 2, WS_EP = WS_GR + 32768;
constexpr unsigned WS_G2 = WS_EP + 4096, WS_FL = WS_G2 + L_ * 8;

__device__ __forceinline__ float bf2f(unsigned v) { return __uint_as_float(v << 16); }
__device__ __forceinline__ float bflo(unsigned v) { return __uint_as_float(v << 16); }
__device__ __forceinline__ float bfhi(unsigned v) { return __uint_as_float(v & 0xffff0000u); }
__device__ __forceinline__ unsigned pk2(float lo, float hi) {
    f32x2 f = {lo, hi};
    bf16x2 b = __builtin_convertvector(f, bf16x2);
    return __builtin_bit_cast(unsigned, b);
}
__device__ __forceinline__ float rb(float x) { return bflo(pk2(x, x)); }

__device__ __forceinline__ f32x4 mfma16(u32x4 a, u32x4 b, f32x4 c) {
    return __builtin_amdgcn_mfma_f32_16x16x32_bf16(__builtin_bit_cast(bf16x8, a), __builtin_bit_cast(bf16x8, b), c, 0, 0, 0);
}
__device__ __forceinline__ u32x2 tr_read(const char* p) {
    s16x4 v = __builtin_amdgcn_ds_read_tr16_b64_v4i16((LDS_AS s16x4*)p);
    return __builtin_bit_cast(u32x2, v);
}
template <int CTRL>
__device__ __forceinline__ float dpp_add(float v) {
    int t = __builtin_amdgcn_update_dpp(0, __float_as_int(v), CTRL, 0xf, 0xf, true);
    return v + __int_as_float(t);
}
__device__ __forceinline__ float row16_sum(float v) {
    v = dpp_add<0x128>(v);
    v = dpp_add<0x124>(v);
    v = dpp_add<0x122>(v);
    v = dpp_add<0x121>(v);
    return v;
}

template <int NT = 0>
__device__ __forceinline__ void touch4(__amdgpu_buffer_rsrc_t srd, unsigned voff, unsigned soff, unsigned m0v) {
    if (NT) asm volatile("s_mov_b32 m0, %0\n\ts_nop 0\n\tbuffer_load_dword %1, %2, %3 offen nt lds"
                 :: "s"(m0v), "v"(voff), "s"(srd), "s"(soff) : "m0", "memory");
    else asm volatile("s_mov_b32 m0, %0\n\ts_nop 0\n\tbuffer_load_dword %1, %2, %3 offen lds"
                 :: "s"(m0v), "v"(voff), "s"(srd), "s"(soff) : "m0", "memory");
}
#define MT2S 1
__device__ __forceinline__ int mt2_of(int p, int i) { return MT2S == 1 ? 8 * p + i : MT2S == 2 ? 16 * (p >> 1) + 2 * i + (p & 1) : 4 * i + p; }
#define W2M 1
#define W2N 416
#define XT 1
#define W1SL 3
#define KT 100
#define W1X 8
#define BAL 1
#define W2C 160
#define CW 3
#define CODE_SRD(srd, sym) do { unsigned lo_, hi_, n_; \
    asm("s_getpc_b64 vcc\n\ts_add_u32 %0, vcc_lo, " sym "@rel32@lo+4\n\ts_addc_u32 %1, vcc_hi, " sym "@rel32@hi+12\n\t" \
        "s_mov_b32 %2, .L" sym "_last-" sym : "=s"(lo_), "=s"(hi_), "=s"(n_) :: "vcc", "scc"); \
    srd = __builtin_amdgcn_make_buffer_rsrc((void*)(((unsigned long long)hi_ << 32) | lo_), (short)0, n_, 0x00020000); } while (0)
__device__ __forceinline__ void pause_until(unsigned t0, unsigned ticks) {
#pragma unroll 1
    for (int i = 0; i < 64 && (unsigned)__builtin_amdgcn_s_memrealtime() - t0 < ticks; ++i) __builtin_amdgcn_s_sleep(4);
}
__device__ __forceinline__ void fetch_w2(const u16* W, int j, int j1, int nw, unsigned lds, int lane) {
    const __amdgpu_buffer_rsrc_t srdW = __builtin_amdgcn_make_buffer_rsrc((void*)W, (short)0, L_ * F_ * 2, 0x00020000);
    for (; j < j1; j += nw) touch4(srdW, lane * 128, j * 8192, lds);
}
template <int MODE>
__device__ __forceinline__ void warm_w(const u16* W, const u16* bias, int xcd, int gw, int nw, unsigned lds, int lane) {
    constexpr int LDW = MODE == 0 ? F_ : L_;
    const __amdgpu_buffer_rsrc_t srdW = __builtin_amdgcn_make_buffer_rsrc((void*)W, (short)0, L_ * F_ * 2, 0x00020000);
    if (gw == 0) touch4(__builtin_amdgcn_make_buffer_rsrc((void*)bias, (short)0, LDW * 2, 0x00020000), lane * 128, 0, lds);
    const int mt0 = MODE == 0 ? ((xcd >> 1) * 27) >> 2 : mt2_of(xcd >> 1, 0);
    const unsigned voff = ((lane >> 3) * LDW + (lane & 7) * (MODE == 0 ? 64 : 64 * MT2S)) * 2;
    int r = 0;
    for (int j = gw; j < (MODE == 0 ? 256 : 224); j += nw) {
        if (MODE == 0 && r++ < W1SL) asm volatile("s_waitcnt vmcnt(0)" ::: "memory");
        const int s = MODE == 0 ? (j >> 4) ^ ((xcd & 1) * W1X) : j >> 4, w = (j >> 2) & 3;
        const int ks = MODE == 0 ? w * 16 + s : w * 14 + s;
        if (MODE != 0 && ks >= 54) continue;
        touch4(srdW, voff, ((ks * 32 + (j & 3) * 8) * LDW + mt0 * 64) * 2, lds);
    }
}

constexpr int KA_ROWB = 528;
constexpr int KA_LO = 32 * KA_ROWB;
constexpr int KA_HI0 = 4 * KA_LO;
constexpr int KA_HI = 13312;
constexpr int KA_MB = 20000;
constexpr int CV_COPY = 4160;
constexpr int CV_UB = 4608;
constexpr int KA_U = KA_HI0 + 4 * KA_HI;
#define XC1 4
#define XCS 3
#define GFIRST " sc1"
typedef __attribute__((address_space(1))) unsigned gu32;

__device__ __forceinline__ void cm4(f32x2& r0, f32x2 a0, f32x2 b0, f32x2& r1, f32x2 a1, f32x2 b1,
                                    f32x2& r2, f32x2 a2, f32x2 b2, f32x2& r3, f32x2 a3, f32x2 b3) {
    asm("v_pk_mul_f32 %0, %4, %5 op_sel:[0,0] op_sel_hi:[1,0]\n\t"
        "v_pk_mul_f32 %1, %6, %7 op_sel:[0,0] op_sel_hi:[1,0]\n\t"
        "v_pk_mul_f32 %2, %8, %9 op_sel:[0,0] op_sel_hi:[1,0]\n\t"
        "v_pk_mul_f32 %3, %10, %11 op_sel:[0,0] op_sel_hi:[1,0]\n\t"
        "v_pk_fma_f32 %0, %4, %5, %0 op_sel:[1,1,0] op_sel_hi:[0,1,1] neg_lo:[1,0,0]\n\t"
        "v_pk_fma_f32 %1, %6, %7, %1 op_sel:[1,1,0] op_sel_hi:[0,1,1] neg_lo:[1,0,0]\n\t"
        "v_pk_fma_f32 %2, %8, %9, %2 op_sel:[1,1,0] op_sel_hi:[0,1,1] neg_lo:[1,0,0]\n\t"
        "v_pk_fma_f32 %3, %10, %11, %3 op_sel:[1,1,0] op_sel_hi:[0,1,1] neg_lo:[1,0,0]"
        : "=&v"(r0), "=&v"(r1), "=&v"(r2), "=&v"(r3)
        : "v"(a0), "v"(b0), "v"(a1), "v"(b1), "v"(a2), "v"(b2), "v"(a3), "v"(b3));
}
constexpr int ka_off(int r) { return r < 32 ? r * KA_ROWB : KA_HI0 - KA_MB + (r - 32) * KA_ROWB; }
template <int R0, int R1, int R2, int R3>
__device__ __forceinline__ void put4(const unsigned (&m0v)[2], f32x2 v0, f32x2 v1, f32x2 v2, f32x2 v3) {
    static_assert((R0 < 32) == (R1 < 32) && (R0 < 32) == (R2 < 32) && (R0 < 32) == (R3 < 32), "");
    unsigned t0, t1, t2, t3;
    asm volatile("s_mov_b32 m0, %4\n\t"
                 "v_cvt_pk_bf16_f32 %0, %5, %6\n\tv_cvt_pk_bf16_f32 %1, %7, %8\n\t"
                 "v_cvt_pk_bf16_f32 %2, %9, %10\n\tv_cvt_pk_bf16_f32 %3, %11, %12\n\t"
                 "ds_write_addtid_b32 %0 offset:%13\n\tds_write_addtid_b32 %1 offset:%14\n\t"
                 "ds_write_addtid_b32 %2 offset:%15\n\tds_write_addtid_b32 %3 offset:%16"
                 : "=&v"(t0), "=&v"(t1), "=&v"(t2), "=&v"(t3)
                 : "s"(m0v[R0 >= 32]), "v"(v0.x), "v"(v0.y), "v"(v1.x), "v"(v1.y), "v"(v2.x), "v"(v2.y), "v"(v3.x), "v"(v3.y),
                   "n"(ka_off(R0)), "n"(ka_off(R1)), "n"(ka_off(R2)), "n"(ka_off(R3))
                 : "m0", "memory");
}
__device__ __forceinline__ f32x2 cexp_p(float zr, float zi, float p) {
    const float mag = __builtin_amdgcn_exp2f(zr * p);
    const float rev = __builtin_amdgcn_fractf(zi * p);
    f32x2 r;
    r.x = mag * __builtin_amdgcn_cosf(rev);
    r.y = mag * __builtin_amdgcn_sinf(rev);
    return r;
}
__device__ __forceinline__ float wave_sum(float v) {
    v = row16_sum(v);
    const int i = __float_as_int(v);
    return (__int_as_float(__builtin_amdgcn_readlane(i, 0)) + __int_as_float(__builtin_amdgcn_readlane(i, 16))) +
           (__int_as_float(__builtin_amdgcn_readlane(i, 32)) + __int_as_float(__builtin_amdgcn_readlane(i, 48)));
}
__device__ __forceinline__ unsigned next_lane(unsigned v, unsigned last) {
    return (unsigned)__builtin_amdgcn_update_dpp((int)last, (int)v, 0x130, 0xf, 0xf, false);
}
__device__ __forceinline__ void put_copies(char* p, unsigned r0, unsigned r1, unsigned r2, unsigned r3, unsigned r4, unsigned r5) {
    const unsigned s0 = __builtin_amdgcn_alignbit(r1, r0, 16), s1 = __builtin_amdgcn_alignbit(r2, r1, 16),
                   s2 = __builtin_amdgcn_alignbit(r3, r2, 16), s3 = __builtin_amdgcn_alignbit(r4, r3, 16),
                   s4 = __builtin_amdgcn_alignbit(r5, r4, 16);
    *(u32x4*)(p + 0 * CV_COPY) = (u32x4){r0, r1, r2, r3};
    *(u32x4*)(p + 1 * CV_COPY) = (u32x4){s0, s1, s2, s3};
    *(u32x4*)(p + 2 * CV_COPY) = (u32x4){r1, r2, r3, r4};
    *(u32x4*)(p + 3 * CV_COPY) = (u32x4){s1, s2, s3, s4};
}
template <int O> __device__ __forceinline__ void rd64(u32x2& v, unsigned a) {
    asm volatile("ds_read_b64 %0, %1 offset:%2" : "=v"(v) : "v"(a), "n"(O) : "memory");
}
template <int O> __device__ __forceinline__ void rd128(u32x4& v, unsigned a) {
    asm volatile("ds_read_b128 %0, %1 offset:%2" : "=v"(v) : "v"(a), "n"(O) : "memory");
}
struct CvFrag { u32x2 lo[8], hi[8]; u32x4 b[8]; };
__device__ __forceinline__ void cv_wait(CvFrag& f) {
    asm volatile("s_waitcnt lgkmcnt(0)"
                 : "+v"(f.lo[0]), "+v"(f.lo[1]), "+v"(f.lo[2]), "+v"(f.lo[3]), "+v"(f.lo[4]), "+v"(f.lo[5]), "+v"(f.lo[6]), "+v"(f.lo[7]),
                   "+v"(f.hi[0]), "+v"(f.hi[1]), "+v"(f.hi[2]), "+v"(f.hi[3]), "+v"(f.hi[4]), "+v"(f.hi[5]), "+v"(f.hi[6]), "+v"(f.hi[7]),
                   "+v"(f.b[0]), "+v"(f.b[1]), "+v"(f.b[2]), "+v"(f.b[3]), "+v"(f.b[4]), "+v"(f.b[5]), "+v"(f.b[6]), "+v"(f.b[7])
                 :: "memory");
}
template <int T>
__device__ __forceinline__ void cv_mma(const CvFrag& f, f32x4 (&acc)[8]) {
    constexpr int q = T < 1 ? 0 : T < 3 ? 1 : T < 6 ? 2 : T < 10 ? 3 : T < 15 ? 4 : T < 21 ? 5 : T < 28 ? 6 : 7;
    constexpr int g1 = T - q * (q + 1) / 2;
    const u32x4 a = {f.lo[g1].x, f.lo[g1].y, f.hi[g1].x, f.hi[g1].y};
    acc[q] = mfma16(a, f.b[q - g1], acc[q]);
}
template <int G0, int R>
__device__ __forceinline__ void cv_read(CvFrag& f, unsigned aa, unsigned ba) {
    constexpr int G1 = R / 3;
    if constexpr (R % 3 == 0) rd64<(63 - 8 * G1 - G0) * 64>(f.lo[G1], aa);
    else if constexpr (R % 3 == 1) rd64<(63 - 8 * G1 - G0) * 64 + 8>(f.hi[G1], aa);
    else rd128<(8 * G1 - G0 + 7) * 64>(f.b[G1], ba);
}
template <int G0, int T>
__device__ __forceinline__ void cv_group(const CvFrag& cur, CvFrag& nxt, f32x4 (&acc)[8], unsigned aa, unsigned ba) {
    cv_mma<T>(cur, acc);
    __builtin_amdgcn_sched_barrier(0);
    if constexpr (G0 < 7 && T < 30 && T % 5 != 4) {
        cv_read<G0 + 1, T - T / 5>(nxt, aa, ba);
        __builtin_amdgcn_sched_barrier(0);
    }
    if constexpr (T < 35) cv_group<G0, T + 1>(cur, nxt, acc, aa, ba);
}
template <int G0>
__device__ __forceinline__ void cv_round(CvFrag& cur, CvFrag& nxt, f32x4 (&acc)[8], unsigned aa, unsigned ba) {
    cv_wait(cur);
    if constexpr (G0 == 5) __builtin_amdgcn_s_barrier();
    __builtin_amdgcn_sched_barrier(0);
    cv_group<G0, 0>(cur, nxt, acc, aa, ba);
    if constexpr (G0 < 7) cv_round<G0 + 1>(nxt, cur, acc, aa, ba);
}
template <int I>
__device__ __forceinline__ void cv_first(CvFrag& f, unsigned aa, unsigned ba) {
    cv_read<0, I>(f, aa, ba);
    if constexpr (I < 23) cv_first<I + 1>(f, aa, ba);
}

template <class Hook>
__device__ __forceinline__ void ka_chain(const unsigned (&m0v)[2], float p_la, float p_ai, float p_cr, float p_ci, float p_dt, Hook hook) {
    const float dt = rb(__builtin_amdgcn_exp2f(p_dt * 1.44269504088896341f));
    const float ar = -rb(__builtin_amdgcn_exp2f(p_la * 1.44269504088896341f));
    const float zr = ar * dt * 1.44269504088896341f, zi = p_ai * dt * 0.15915494309189535f;
    f32x2 a[17], b[16], e[8], F1, F2, F4, F8, r0, r1, r2, r3;
    a[0] = (f32x2){2.0f * (p_cr * dt), -2.0f * (p_ci * dt)};
    e[1] = cexp_p(zr, -zi, 1.f);
    b[1] = cexp_p(zr, zi, 8.f);
    F1 = cexp_p(zr, -zi, 128.f);
    cm4(e[2], e[1], e[1], b[2], b[1], b[1], F2, F1, F1, a[1], a[0], F1);
    cm4(e[4], e[2], e[2], b[4], b[2], b[2], F4, F2, F2, e[3], e[2], e[1]);
    put4<41, 42, 43, 45>(m0v, (f32x2){1.f, 0.f}, b[1], b[2], b[4]);
    hook(0);
    cm4(a[2], a[0], F2, a[3], a[1], F2, b[3], b[2], b[1], r0, a[0], e[1]);
    cm4(F8, F4, F4, b[8], b[4], b[4], r1, a[1], e[1], r2, a[0], e[2]);
    put4<16, 20, 1, 9>(m0v, a[2], a[3], r0, r1);
    hook(1);
    cm4(a[4], a[0], F4, a[5], a[1], F4, a[6], a[2], F4, a[7], a[3], F4);
    put4<24, 26, 28, 30>(m0v, a[4], a[5], a[6], a[7]);
    hook(2);
    cm4(e[5], e[4], e[1], e[6], e[4], e[2], e[7], e[4], e[3], r3, a[1], e[2]);
    put4<0, 8, 2, 10>(m0v, a[0], a[1], r2, r3);
    hook(3);
    cm4(b[5], b[4], b[1], b[6], b[4], b[2], b[7], b[4], b[3], r0, a[0], e[3]);
    put4<44, 49, 46, 47>(m0v, b[3], b[8], b[5], b[6]);
    hook(4);
    cm4(a[8], a[0], F8, a[9], a[1], F8, a[10], a[2], F8, a[11], a[3], F8);
    put4<32, 33, 34, 35>(m0v, a[8], a[9], a[10], a[11]);
    hook(5);
    cm4(a[12], a[4], F8, a[13], a[5], F8, a[14], a[6], F8, a[15], a[7], F8);
    put4<36, 37, 38, 39>(m0v, a[12], a[13], a[14], a[15]);
    hook(6);
    cm4(a[16], a[8], F8, r1, a[1], e[3], r2, a[0], e[4], r3, a[1], e[4]);
    put4<3, 11, 4, 12>(m0v, r0, r1, r2, r3);
    hook(7);
    cm4(b[9], b[8], b[1], b[10], b[8], b[2], b[11], b[8], b[3], b[12], b[8], b[4]);
    put4<50, 51, 52, 53>(m0v, b[9], b[10], b[11], b[12]);
    hook(8);
    cm4(b[13], b[8], b[5], b[14], b[8], b[6], b[15], b[8], b[7], r0, a[0], e[5]);
    put4<54, 55, 56, 48>(m0v, b[13], b[14], b[15], b[7]);
    asm volatile("ds_write_addtid_b32 %0 offset:%1" :: "v"(pk2(a[16].x, a[16].y)), "n"(ka_off(40)) : "memory");
    hook(9);
    cm4(r1, a[1], e[5], r2, a[0], e[6], r3, a[1], e[6], F1, a[0], e[7]);
    put4<5, 13, 6, 14>(m0v, r0, r1, r2, r3);
    hook(10);
    cm4(r0, a[1], e[7], r1, a[2], e[2], r2, a[3], e[2], F2, a[2], e[4]);
    put4<7, 15, 17, 21>(m0v, F1, r0, r1, r2);
    hook(11);
    cm4(r3, a[3], e[4], F1, a[2], e[6], r0, a[3], e[6], r1, a[4], e[4]);
    put4<18, 22, 19, 23>(m0v, F2, r3, F1, r0);
    hook(12);
    cm4(r2, a[5], e[4], r3, a[6], e[4], F1, a[7], e[4], F2, a[7], e[4]);
    put4<25, 27, 29, 31>(m0v, r1, r2, r3, F1);
    asm volatile("s_waitcnt lgkmcnt(0)" ::: "memory");
}
__device__ __forceinline__ void unpack4(float (&o)[4], u32x2 v) {
    asm volatile("v_lshlrev_b32 %0, 16, %4\n\tv_and_b32 %1, 0xffff0000, %4\n\tv_lshlrev_b32 %2, 16, %5\n\tv_and_b32 %3, 0xffff0000, %5"
                 : "=&v"(o[0]), "=&v"(o[1]), "=&v"(o[2]), "=&v"(o[3]) : "v"(v.x), "v"(v.y));
}

extern "C" __global__ __launch_bounds__(512, 2) void ka_conv(const u16* __restrict__ x, const u16* __restrict__ logA,
                                                  const u16* __restrict__ Aim, const u16* __restrict__ Cre,
                                                  const u16* __restrict__ Cim, const u16* __restrict__ logdt,
                                                  const u16* __restrict__ dskip, char* ws,
                                                  const u16* __restrict__ w1, const u16* __restrict__ b1,
                                                  const u16* __restrict__ w2, const u16* __restrict__ b2) {
    __shared__ __attribute__((aligned(16))) char smem[KA_U + 4 * CV_UB + 256];
    const int tid = threadIdx.x, lane = tid & 63;
    const int w = __builtin_amdgcn_readfirstlane(tid >> 6);
    STAMP(0, 0);
    if ((CW & 1) && w >= 6) {
        __amdgpu_buffer_rsrc_t srdC;
        CODE_SRD(srdC, "ka_conv");
        touch4(srdC, lane * 128, (w - 6) * 8192, (unsigned)(size_t)(LDS_AS char*)smem + w * 256);
    }
    asm volatile("" :: "s"(x), "s"(logA), "s"(Aim), "s"(Cre), "s"(Cim), "s"(logdt), "s"(dskip), "s"(ws));
    const int xcd = blockIdx.x & 7, idx = blockIdx.x >> 3;
    const bool conv = idx < 20;
    const int grp = ((xcd & 1) * 4 + (xcd >> 1)) * 20 + (conv ? idx : 0);
    const int d0 = grp * 4;
    const int row = (xcd * 32 + idx) * 8 + w;
    unsigned sv[5], epoch, pp[5];
    float pf[5];
    asm volatile("global_load_dword %0, %11, %12\n\tglobal_load_dword %1, %11, %12 offset:256\n\t"
                 "global_load_dword %2, %11, %12 offset:512\n\tglobal_load_dword %3, %11, %12 offset:768\n\t"
                 "global_load_dword %4, %11, %12 offset:1024\n\tglobal_load_dword %5, %13, %14\n\t"
                 "global_load_ushort %6, %15, %16\n\tglobal_load_ushort %7, %15, %17\n\t"
                 "global_load_ushort %8, %15, %18\n\tglobal_load_ushort %9, %15, %19\n\t"
                 "global_load_ushort %10, %13, %20"
                 : "=&v"(sv[0]), "=&v"(sv[1]), "=&v"(sv[2]), "=&v"(sv[3]), "=&v"(sv[4]), "=&v"(epoch),
                   "=&v"(pp[0]), "=&v"(pp[1]), "=&v"(pp[2]), "=&v"(pp[3]), "=&v"(pp[4])
                 : "v"(lane * 4), "s"(x + (size_t)row * D_), "v"(0), "s"(ws + WS_EP),
                   "v"((d0 * N_ + tid) * 2), "s"(logA), "s"(Aim), "s"(Cre), "s"(Cim), "s"(logdt + d0 + (w >> 1))
                 : "memory");
    const int t = tid & 255;
    const __amdgpu_buffer_rsrc_t srdG = __builtin_amdgcn_make_buffer_rsrc((void*)(ws + WS_GR), (short)0, L_ * 8, 0x00020000);
    const __amdgpu_buffer_rsrc_t srdX = __builtin_amdgcn_make_buffer_rsrc((void*)x, (short)0, SZ_, 0x00020000);
    {
        const int tw = XT == 1 ? (BAL ? (conv ? 64 : (7 - w) * 12 + idx - 20) : (idx - 20) * 8 + w) : XT == 2 ? (w < 4 ? idx * 4 + w : 64) : XT == 3 ? (w < 2 ? idx * 2 + w : 64) : 64;
        touch4(srdX, (lane >> 1) * (D_ * 2) + (lane & 1) * 156, (unsigned)tw < 64u ? tw * (32 * D_ * 2) + (grp / 20) * 160 : SZ_,
               (unsigned)(size_t)(LDS_AS char*)(smem + KA_U + 4 * CV_UB));
    }
    const int owner = conv ? w >> 2 : 0;
    u32x2 xc[8], xd;
    asm volatile("s_barrier\n\ts_cmp_eq_u32 %5, 0\n\ts_cbranch_scc1 .Lka_a%=\n\t"
                 "global_load_dwordx2 %4, %6, %7\n\t"
                 "buffer_load_dwordx2 %0, %8, %9, %10 offen\n\tbuffer_load_dwordx2 %1, %8, %9, %10 offen offset:1280\n\t"
                 "buffer_load_dwordx2 %2, %8, %9, %10 offen offset:2560\n\tbuffer_load_dwordx2 %3, %8, %9, %10 offen offset:3840\n\t"
                 "s_waitcnt vmcnt(6)\n\ts_branch .Lka_b%=\n"
                 ".Lka_a%=:\n\ts_waitcnt vmcnt(1)\n"
                 ".Lka_b%=:"
                 : "=&v"(xc[0]), "=&v"(xc[1]), "=&v"(xc[2]), "=&v"(xc[3]), "=&v"(xd)
                 : "s"(owner), "v"(0), "s"(dskip + d0), "v"(t * (8 * D_ * 2)), "s"(srdX), "s"(d0 * 2) : "scc", "memory");
    unsigned etag;
    {
        float v[12];
        unpack4(*(float(*)[4])v, (u32x2){sv[0], sv[1]});
        unpack4(*(float(*)[4])(v + 4), (u32x2){sv[2], sv[3]});
        asm volatile("v_lshlrev_b32 %0, 16, %3\n\tv_and_b32 %1, 0xffff0000, %3\n\tv_xor_b32 %2, 0x9e3779b9, %4"
                     : "=&v"(v[8]), "=&v"(v[9]), "=&v"(etag) : "v"(sv[4]), "v"(epoch));
        const float s = ((v[0] + v[1]) + (v[2] + v[3])) + ((v[4] + v[5]) + (v[6] + v[7])) + (v[8] + v[9]);
        const float mean = wave_sum(s) * (1.0f / D_);
        float q0 = 0.f, q1 = 0.f;
#pragma unroll
        for (int j = 0; j < 5; ++j) {
            const float a = v[2 * j] - mean, b = v[2 * j + 1] - mean;
            q0 = __builtin_fmaf(a, a, q0);
            q1 = __builtin_fmaf(b, b, q1);
        }
        const float q = wave_sum(q0 + q1);
        const float den = __builtin_amdgcn_sqrtf(rb(rb(q * (1.0f / D_)) + 1e-5f));
        asm volatile("buffer_store_dwordx2 %10, %11, %12, %13 offen sc1\n\t"
                     "v_lshlrev_b32 %0, 16, %5\n\tv_lshlrev_b32 %1, 16, %6\n\tv_lshlrev_b32 %2, 16, %7\n\t"
                     "v_lshlrev_b32 %3, 16, %8\n\tv_lshlrev_b32 %4, 16, %9"
                     : "=&v"(pf[0]), "=&v"(pf[1]), "=&v"(pf[2]), "=&v"(pf[3]), "=&v"(pf[4])
                     : "v"(pp[0]), "v"(pp[1]), "v"(pp[2]), "v"(pp[3]), "v"(pp[4]),
                       "v"((u32x2){pk2(mean, den), etag * 0x85ebca6bu}), "v"(lane * (L_ * 8)), "s"(srdG), "s"(row * 8) : "memory");
    }
    TS(0);
    if (!conv) {
        const unsigned lds = (unsigned)(size_t)(LDS_AS char*)(smem + w * 1024);
        if (XT == 4 && idx < 28) touch4(srdX, (lane >> 1) * (D_ * 2) + (lane & 1) * 156, ((idx - 20) * 8 + w) * (32 * D_ * 2) + (grp / 20) * 160, lds);
        if ((CW & 2) && idx == 28 && w < 3) {
            __amdgpu_buffer_rsrc_t srdC;
            CODE_SRD(srdC, "kb_mlp");
            touch4(srdC, lane * 128, w * 8192, lds);
        }
        const int gw = BAL ? w * 12 + idx - 20 : (idx - 20) * 8 + w;
        warm_w<0>(w1, b1, xcd, gw, 96, lds, lane);
        if (W2M & 1) asm volatile("s_waitcnt vmcnt(0)" ::: "memory");
        fetch_w2(w2, W2C + (95 - gw) * 8 + xcd, W2N, 768, lds, lane);
        if (idx == 31 && tid < 256) {
            const int r = xcd * 8 + (tid >> 5), c = tid & 31;
            *(unsigned*)(ws + WS_ID + r * (L_ * 2) + 4 * c) = (r >> 1) == c ? ((r & 1) ? 0x3f800000u : 0x3f80u) : 0u;
        }
        if (idx == 30 && tid < 128) *(u32x4*)(ws + WS_G2 + (xcd * 128 + tid) * 16) = (u32x4){0u, 0u, 0u, 0u};
        if (idx == 29 && tid < 32) *(unsigned*)(ws + WS_FL + (xcd * 32 + tid) * 128) = 0u;
        asm volatile("s_waitcnt vmcnt(0)" ::: "memory");
        STAMP(0, 7);
        return;
    }
    const unsigned lds0 = (unsigned)(size_t)(LDS_AS char*)smem;
    const int m = lane & 15, hg = lane >> 4;
    char* mine = smem + (w & 3) * KA_LO;
    char* minh = smem + KA_HI0 + (w & 3) * KA_HI;
    const unsigned m0v[2] = {lds0 + (w >> 1) * KA_LO + (w & 1) * 256, lds0 + KA_MB + (w >> 1) * KA_HI + (w & 1) * 256};
    ka_chain(m0v, pf[0], pf[1], pf[2], pf[3], pf[4], [&](int i) {
        if (i % XCS == 0 && i / XCS < 4)
            asm volatile("s_cmp_eq_u32 %1, 0\n\ts_cbranch_scc1 .Lka_c%=\n\tbuffer_load_dwordx2 %0, %2, %3, %4 offen offset:%5\n.Lka_c%=:"
                         : "=v"(xc[4 + i / XCS])
                         : "s"(owner), "v"(t * (8 * D_ * 2)), "s"(srdX), "s"(d0 * 2 + 4 * D_ * 2), "n"((i / XCS) * (D_ * 2)) : "scc", "memory");
    });
    TS(1);
    __builtin_amdgcn_s_barrier();
    if (w < 4) {
        TS(2);
        u16* kd = (u16*)(minh + 8192);
        {
            const char* fb = mine + m * KA_ROWB + hg * 16;
            const char* fh = minh + m * KA_ROWB + hg * 16;
            u32x4 fa[8][3], fl[8];
#pragma unroll
            for (int ks = 0; ks < 8; ++ks) {
                fl[ks] = *(const u32x4*)(fh + 9 * KA_ROWB + ks * 64);
                fa[ks][0] = *(const u32x4*)(fb + ks * 64);
                fa[ks][1] = *(const u32x4*)(fb + 16 * KA_ROWB + ks * 64);
                fa[ks][2] = *(const u32x4*)(fh + ks * 64);
            }
            __builtin_amdgcn_sched_barrier(0);
            f32x4 acc0 = {0.f, 0.f, 0.f, 0.f}, acc1 = acc0, acc2 = acc0;
#pragma unroll
            for (int ks = 0; ks < 8; ++ks) {
                acc0 = mfma16(fa[ks][0], fl[ks], acc0);
                acc1 = mfma16(fa[ks][1], fl[ks], acc1);
                acc2 = mfma16(fa[ks][2], fl[ks], acc2);
            }
            const int c = m;
            *(u32x2*)(kd + 128 * (hg >> 1) + 4 * (hg & 1) + 8 * c) = (u32x2){pk2(acc0[0], acc0[1]), pk2(acc0[2], acc0[3])};
            const unsigned p01 = pk2(acc1[0], acc1[1]), p23 = pk2(acc1[2], acc1[3]);
            const int i0 = hg < 2 ? 256 + 64 * hg + 4 * c : 256 + 64 * hg + 2 * c;
            *(unsigned*)(kd + i0) = p01;
            *(unsigned*)(kd + i0 + (hg < 2 ? 2 : 32)) = p23;
            const unsigned q01 = pk2(acc2[0], acc2[1]), q23 = pk2(acc2[2], acc2[3]);
            kd[512 + 64 * hg + c] = (u16)(q01 & 0xffff);
            kd[528 + 64 * hg + c] = (u16)(q01 >> 16);
            kd[544 + 64 * hg + c] = (u16)(q23 & 0xffff);
            kd[560 + 64 * hg + c] = (u16)(q23 >> 16);
            asm volatile("" ::: "memory");
        }
        const unsigned* kw = (const unsigned*)kd;
        const unsigned dw0 = kw[319 - lane], dw1 = kw[320 - lane];
        const unsigned cw0 = kw[255 - lane], cw1 = kw[256 - lane];
        const bool isA = lane >= 32;
        const char* bp = (const char*)kd + (isA ? 16 * (63 - lane) : 256 + 8 * (63 - lane));
        const u32x2 b01 = *(const u32x2*)bp, b23 = *(const u32x2*)(bp + 8);
        const unsigned P = __builtin_amdgcn_perm(0, dw1, 0x01000100), Q = __builtin_amdgcn_perm(0, dw0, 0x03020302),
                       M = __builtin_amdgcn_perm(0, dw0, 0x01000302), Z = __builtin_amdgcn_perm(0, dw0, 0x01000100);
        const unsigned c0 = __builtin_amdgcn_perm(0, cw1, 0x01000100), c1 = __builtin_amdgcn_perm(0, cw0, 0x03020302),
                       c2 = __builtin_amdgcn_perm(0, cw0, 0x01000302), c3 = __builtin_amdgcn_perm(0, cw0, 0x01000100);
        const unsigned o0 = __builtin_amdgcn_perm(b23.x, isA ? b23.y : b01.y, isA ? 0x01000302u : 0x03020504u),
                       o1 = __builtin_amdgcn_perm(b23.x, b01.y, isA ? 0x05040706u : 0x01000100u),
                       o2 = __builtin_amdgcn_perm(b01.y, b01.x, isA ? 0x05040706u : 0x03020504u),
                       o3 = __builtin_amdgcn_perm(0, b01.x, isA ? 0x01000302u : 0x01000100u);
        put_copies(mine + 3072 + lane * 16, o0, o1, o2, o3, next_lane(o0, 0), next_lane(o1, 0));
        put_copies(mine + 2048 + lane * 16, c0, c1, c2, c3, next_lane(c0, __builtin_amdgcn_readlane(o0, 0)),
                   next_lane(c1, __builtin_amdgcn_readlane(o1, 0)));
        put_copies(mine + lane * 32, P, P, Q, Q, Q, M);
        put_copies(mine + lane * 32 + 16, Q, M, Z, Z, next_lane(P, __builtin_amdgcn_readlane(c0, 0)),
                   next_lane(P, __builtin_amdgcn_readlane(c1, 0)));
        if (lane < 8) *(u32x4*)(mine + (lane >> 1) * CV_COPY + 4096 + (lane & 1) * 16) = (u32x4){0u, 0u, 0u, 0u};
        asm volatile("s_waitcnt vmcnt(0)" ::: "memory");
        TS(5);
        __syncthreads();
        TS(6);
        const unsigned aa = (unsigned)(size_t)(LDS_AS char*)(mine + (3 - (m & 3)) * CV_COPY + 2 * (2028 - 32 * 63 - 4 * (m >> 2) + 8 * hg));
        const unsigned ba = (unsigned)(size_t)(LDS_AS char*)(smem + KA_U + w * CV_UB + (2 * m + hg) * 16);
        f32x4 acc[8];
#pragma unroll
        for (int q = 0; q < 8; ++q) acc[q] = (f32x4){0.f, 0.f, 0.f, 0.f};
        CvFrag f0, f1;
        cv_first<0>(f0, aa, ba);
        cv_round<0>(f0, f1, acc, aa, ba);
        TS(7);
        const char* rp = minh + (16 * m + 4 * hg) * 4;
        char* hp = mine + (16 * m + 4 * hg) * 2;
#pragma unroll
        for (int q = 0; q < 8; ++q) {
            const f32x4 h = acc[q] + *(const f32x4*)(rp + q * 1024);
            *(u32x2*)(hp + q * 512) = (u32x2){pk2(h[0], h[1]), pk2(h[2], h[3])};
        }
    } else {
        const unsigned tag = etag * 0x85ebca6bu;
        u32x4 g[4];
        asm volatile("buffer_load_dwordx4 %0, %4, %5, 0 offen" GFIRST "\n\tbuffer_load_dwordx4 %1, %4, %5, 0 offen offset:16" GFIRST "\n\t"
                     "buffer_load_dwordx4 %2, %4, %5, 0 offen offset:32" GFIRST "\n\tbuffer_load_dwordx4 %3, %4, %5, 0 offen offset:48" GFIRST "\n\t"
                     "s_waitcnt vmcnt(4)"
                     : "=&v"(g[0]), "=&v"(g[1]), "=&v"(g[2]), "=&v"(g[3]) : "v"(t * 64), "s"(srdG) : "memory");
        float xf[8][4], dk[4];
#pragma unroll
        for (int k = 0; k < 8; ++k) unpack4(xf[k], xc[k]);
        unpack4(dk, xd);
        if (lane < 30) *(u32x4*)(smem + KA_U + (w - 4) * CV_UB + lane * 16) = (u32x4){0u, 0u, 0u, 0u};
        TS(3);
        asm volatile("s_waitcnt vmcnt(0)" : "+v"(g[0]), "+v"(g[1]), "+v"(g[2]), "+v"(g[3]) :: "memory");
        unsigned spins = 0;
#pragma unroll 1
        for (; spins < (1u << 16); ++spins) {
            const bool bad = ((g[0].y ^ tag) | (g[0].w ^ tag) | (g[1].y ^ tag) | (g[1].w ^ tag) |
                              (g[2].y ^ tag) | (g[2].w ^ tag) | (g[3].y ^ tag) | (g[3].w ^ tag)) != 0;
            if (!__any(bad)) break;
            if (bad)
                asm volatile("buffer_load_dwordx4 %0, %4, %5, 0 offen sc1\n\tbuffer_load_dwordx4 %1, %4, %5, 0 offen offset:16 sc1\n\t"
                             "buffer_load_dwordx4 %2, %4, %5, 0 offen offset:32 sc1\n\tbuffer_load_dwordx4 %3, %4, %5, 0 offen offset:48 sc1\n\t"
                             "s_waitcnt vmcnt(0)"
                             : "+v"(g[0]), "+v"(g[1]), "+v"(g[2]), "+v"(g[3]) : "v"(t * 64), "s"(srdG) : "memory");
        }
        TS(4);
        unsigned up[4][4];
#pragma unroll
        for (int k = 0; k < 4; ++k) {
            const float i0 = __builtin_amdgcn_rcpf(bfhi(g[k].x)), i1 = __builtin_amdgcn_rcpf(bfhi(g[k].z));
            const float m0 = bflo(g[k].x), m1 = bflo(g[k].z);
#pragma unroll
            for (int c = 0; c < 4; ++c) up[c][k] = pk2((xf[2 * k][c] - m0) * i0, (xf[2 * k + 1][c] - m1) * i1);
        }
#pragma unroll
        for (int c = 0; c < 4; ++c)
            *(u32x4*)(smem + KA_U + c * CV_UB + 480 + t * 16) = (u32x4){up[c][0], up[c][1], up[c][2], up[c][3]};
        TS(5);
        __syncthreads();
        if (blockIdx.x == 0 && tid == 256)
            __hip_atomic_store((gu32*)(ws + WS_EP), (etag ^ 0x9e3779b9u) + 1u, __ATOMIC_RELAXED, __HIP_MEMORY_SCOPE_AGENT);
#pragma unroll
        for (int c = 0; c < 4; ++c) {
            f32x4 r0, r1;
#pragma unroll
            for (int k = 0; k < 2; ++k) {
                r0[2 * k] = __builtin_fmaf(bflo(up[c][k]), dk[c], xf[2 * k][c]);
                r0[2 * k + 1] = __builtin_fmaf(bfhi(up[c][k]), dk[c], xf[2 * k + 1][c]);
                r1[2 * k] = __builtin_fmaf(bflo(up[c][2 + k]), dk[c], xf[4 + 2 * k][c]);
                r1[2 * k + 1] = __builtin_fmaf(bfhi(up[c][2 + k]), dk[c], xf[5 + 2 * k][c]);
            }
            *(f32x4*)(smem + KA_HI0 + c * KA_HI + t * 32) = r0;
            *(f32x4*)(smem + KA_HI0 + c * KA_HI + t * 32 + 16) = r1;
        }
        fetch_w2(w2, (w - 4) * 160 + blockIdx.x, W2C, 640, lds0 + KA_U + 4 * CV_UB, lane);
        asm volatile("s_waitcnt lgkmcnt(0)" ::: "memory");
        __builtin_amdgcn_s_barrier();
        asm volatile("s_waitcnt vmcnt(0)" ::: "memory");
    }
    __syncthreads();
    {
        const int ch = tid & 3;
        const int dt = grp / 20, rem = grp % 20, mq = rem / 5, j = rem % 5;
#pragma unroll
        for (int i = 0; i < 2; ++i) {
            const int piece = (tid >> 2) + 128 * i;
            const int unit = ((piece >> 2) * 40 + dt * 5 + j) * 64 + (piece & 3) * 16 + 4 * mq + ch;
            *(u32x4*)(ws + WS_HB + (size_t)unit * 16) = *(const u32x4*)(smem + ch * KA_LO + piece * 16);
        }
    }
    STAMP(0, 6);
    TSPUT(1, 0);
    TSPUT(5, 448);
    asm volatile(".Lka_conv_last:");
}

constexpr int B_REG = 40960;
constexpr int B_RED = 24576;
constexpr int B_TAB = 32768;
#define PPOS 5
#define NP1 6
#define NPRO 8
#define WL 6
#define PK23 0
#define TQ1 4
#define TQ2 9
#define RSTEPS 10
#define PLATE 2
#define EPW 2
#define G2D 4
#define PRE 2

__device__ __forceinline__ float gelu_tanh(float x) {
    const float p = __builtin_fmaf(x * x, -2.302208198f * 0.044715f, -2.302208198f);
    const float e = __builtin_amdgcn_exp2f(p * x);
    return x * __builtin_amdgcn_rcpf(1.0f + e);
}
template <int I>
__device__ __forceinline__ void w_piece(__amdgpu_buffer_rsrc_t srd, unsigned v, unsigned so, unsigned m0v) {
    if (I == 0) asm volatile("s_mov_b32 m0, %0\n\ts_nop 0\n\tbuffer_load_dwordx4 %1, %2, %3 offen lds"
                             :: "s"(m0v), "v"(v), "s"(srd), "s"(so) : "m0", "memory");
    else asm volatile("buffer_load_dwordx4 %0, %1, %2 offen offset:%3 lds"
                      :: "v"(v), "s"(srd), "s"(so), "n"(I * 1024) : "memory");
}
__device__ __forceinline__ void w_issue(__amdgpu_buffer_rsrc_t srd, const unsigned (&v)[4], unsigned so, unsigned m0v) {
    m0v = __builtin_amdgcn_readfirstlane(m0v);
    asm volatile("s_mov_b32 m0, %0\n\ts_nop 0\n\t"
                 "buffer_load_dwordx4 %1, %5, %6 offen lds\n\tbuffer_load_dwordx4 %2, %5, %6 offen offset:1024 lds\n\t"
                 "buffer_load_dwordx4 %3, %5, %6 offen offset:2048 lds\n\tbuffer_load_dwordx4 %4, %5, %6 offen offset:3072 lds"
                 :: "s"(m0v), "v"(v[0]), "v"(v[1]), "v"(v[2]), "v"(v[3]), "s"(srd), "s"(so) : "m0", "memory");
}
template <int SC, int SL, int J>
__device__ __forceinline__ void a_piece(__amdgpu_buffer_rsrc_t srd, unsigned av0, unsigned av1, unsigned so) {
    if (SC) asm volatile("buffer_load_dwordx4 a[%c0:%c1], %2, %3, %4 offen offset:%5 sc1"
                         :: "i"(SL * 20 + J * 4), "i"(SL * 20 + J * 4 + 3), "v"(J < 4 ? av0 : av1), "s"(srd), "s"(so), "n"((J & 3) * 1024) : "memory");
    else asm volatile("buffer_load_dwordx4 a[%c0:%c1], %2, %3, %4 offen offset:%5"
                      :: "i"(SL * 20 + J * 4), "i"(SL * 20 + J * 4 + 3), "v"(J < 4 ? av0 : av1), "s"(srd), "s"(so), "n"((J & 3) * 1024) : "memory");
}
template <int SC, int SL>
__device__ __forceinline__ void a_issue(__amdgpu_buffer_rsrc_t srd, unsigned av0, unsigned av1, unsigned so) {
    a_piece<SC, SL, 0>(srd, av0, av1, so); a_piece<SC, SL, 1>(srd, av0, av1, so); a_piece<SC, SL, 2>(srd, av0, av1, so);
    a_piece<SC, SL, 3>(srd, av0, av1, so); a_piece<SC, SL, 4>(srd, av0, av1, so);
}
template <int N> __device__ __forceinline__ void vm_wait() { asm volatile("s_waitcnt vmcnt(%0)" :: "n"(N) : "memory"); }
template <int R>
__device__ __forceinline__ void affine4(f32x4& d, u32x2 al, f32x4 be) {
    asm volatile("v_mfma_f32_4x4x4_16b_bf16 %0, %1, a[%c2:%c3], %4" : "=&v"(d) : "v"(al), "i"(R), "i"(R + 1), "v"(be));
}
template <int INIT>
__device__ __forceinline__ void mma_vv(f32x4& c, u32x4 a, u32x4 b, f32x4 c0) {
    if (INIT == 0) asm volatile("v_mfma_f32_16x16x32_bf16 %0, %1, %2, %0" : "+v"(c) : "v"(a), "v"(b));
    if (INIT == 1) asm volatile("v_mfma_f32_16x16x32_bf16 %0, %1, %2, 0" : "=&v"(c) : "v"(a), "v"(b));
    if (INIT == 2) asm volatile("v_mfma_f32_16x16x32_bf16 %0, %1, %2, %3" : "=&v"(c) : "v"(a), "v"(b), "v"(c0));
}
template <int INIT, int R>
__device__ __forceinline__ void mma_av(f32x4& c, u32x4 b, f32x4 c0) {
    if (INIT == 0) asm volatile("v_mfma_f32_16x16x32_bf16 %0, a[%c1:%c2], %3, %0" : "+v"(c) : "i"(R), "i"(R + 3), "v"(b));
    if (INIT == 1) asm volatile("v_mfma_f32_16x16x32_bf16 %0, a[%c1:%c2], %3, 0" : "=&v"(c) : "i"(R), "i"(R + 3), "v"(b));
    if (INIT == 2) asm volatile("v_mfma_f32_16x16x32_bf16 %0, a[%c1:%c2], %3, %4" : "=&v"(c) : "i"(R), "i"(R + 3), "v"(b), "v"(c0));
}
template <int INIT, int R, int Q>
__device__ __forceinline__ void mma_aa(f32x4& c, f32x4 c0) {
    if (INIT == 0) asm volatile("v_mfma_f32_16x16x32_bf16 %0, a[%c1:%c2], a[%c3:%c4], %0" : "+v"(c) : "i"(R), "i"(R + 3), "i"(Q), "i"(Q + 3));
    if (INIT == 1) asm volatile("v_mfma_f32_16x16x32_bf16 %0, a[%c1:%c2], a[%c3:%c4], 0" : "=&v"(c) : "i"(R), "i"(R + 3), "i"(Q), "i"(Q + 3));
    if (INIT == 2) asm volatile("v_mfma_f32_16x16x32_bf16 %0, a[%c1:%c2], a[%c3:%c4], %5" : "=&v"(c) : "i"(R), "i"(R + 3), "i"(Q), "i"(Q + 3), "v"(c0));
}
#define SB() __builtin_amdgcn_sched_barrier(0)
struct Nrm { u32x2 a0, a1; f32x4 b0, b1; };
struct KbCtx {
    __amdgpu_buffer_rsrc_t srdW1, srdW2, srdL, srdA, srdH;
    unsigned wv1[4], wv2[4], av0, av1, ring, w1o, a1o, w2o, a2o, wlo[2], alo[2];
    const char* tp[4];
    unsigned tpa[4];
    const char* ta;
    const char* tb;
    char* red;
    f32x4 bias;
};
__device__ __forceinline__ unsigned w1_off(const KbCtx& c, int g) { return c.w1o + g * (32 * F_ * 2); }
__device__ __forceinline__ unsigned a1_off(const KbCtx& c, int g) { return c.a1o + g * 40960; }
__device__ __forceinline__ unsigned w2_off(const KbCtx& c, int g) { return g >= 12 ? c.wlo[g & 1] : c.w2o + g * (32 * L_ * 2); }
__device__ __forceinline__ unsigned a2_off(const KbCtx& c, int g) { return g >= 12 ? c.alo[g & 1] : c.a2o + g * 40960; }
template <int R>
__device__ __forceinline__ void w_read(u32x4 (&wf)[4], const KbCtx& c, int SL) {
    const u32x2 v = tr_read(c.tp[R >> 1] + SL * 4096 + (R & 1) * 512);
    if (R & 1) { wf[R >> 1].z = v.x; wf[R >> 1].w = v.y; } else { wf[R >> 1].x = v.x; wf[R >> 1].y = v.y; }
}
template <int I>
__device__ __forceinline__ void nrm_read(Nrm& t, const KbCtx& c, int S) {
    if (I == 0) { t.a0 = *(const u32x2*)(c.ta + S * 256); t.a1 = *(const u32x2*)(c.ta + S * 256 + 32); }
    if (I == 1) t.b0 = *(const f32x4*)(c.tb + S * 128);
    if (I == 2) t.b1 = *(const f32x4*)(c.tb + S * 128 + 16);
}
template <int T>
__device__ __forceinline__ void red_put(const f32x4 (&acc)[4][5], const KbCtx& c) {
    if constexpr (T >= 8) *(f32x4*)(c.red + (T - 8) * 1024) = acc[(T - 8) / 5 + 1][(T - 8) % 5];
    if constexpr (T == 19) {
        SB();
#pragma unroll
        for (int j = 2; j < 5; ++j) *(f32x4*)(c.red + (10 + j) * 1024) = acc[3][j];
    }
}
constexpr int G1Q = (16 - NPRO) * 9;
constexpr int g1_cum(int s) {
    return s <= RSTEPS ? (G1Q * s + RSTEPS - 1) / RSTEPS : G1Q + (s >= 15 ? 4 * WL : 4 * WL * (s - RSTEPS) / (15 - RSTEPS));
}
template <int S, int P>
__device__ __forceinline__ void g1_req(const KbCtx& c) {
    constexpr int q = g1_cum(S) + P;
    static_assert(g1_cum(S + 1) - g1_cum(S) <= 9, "");
    if constexpr (q < g1_cum(S + 1)) {
        if constexpr (q < G1Q) {
            constexpr int g = NPRO + q / 9, r = q % 9;
            static_assert(g - 8 <= S && g >= S + 2, "");
            if constexpr (r < 4) w_piece<r>(c.srdW1, c.wv1[r], w1_off(c, g), c.ring + (g & 7) * 4096);
            else a_piece<0, g & 7, r - 4>(c.srdH, c.av0, c.av1, a1_off(c, g));
        } else {
            constexpr int g = (q - G1Q) / 4, r = (q - G1Q) % 4;
            static_assert(g + 8 <= S, "");
            w_piece<r>(c.srdW2, c.wv2[r], w2_off(c, g), c.ring + g * 4096);
        }
    }
}
template <int SL, int R>
__device__ __forceinline__ void w_park(const KbCtx& c) {
    asm volatile("ds_read_b64_tr_b16 a[%c0:%c1], %2 offset:%3"
                 :: "i"(160 + SL * 16 + R * 2), "i"(161 + SL * 16 + R * 2), "v"(c.tpa[R >> 1]), "n"(SL * 4096 + (R & 1) * 512) : "memory");
}
template <int SL> __device__ __forceinline__ void w_park8(const KbCtx& c) {
    w_park<SL, 0>(c); w_park<SL, 1>(c); w_park<SL, 2>(c); w_park<SL, 3>(c); w_park<SL, 4>(c); w_park<SL, 5>(c); w_park<SL, 6>(c); w_park<SL, 7>(c);
}
template <int S, int T>
__device__ __forceinline__ void g1_mma(f32x4 (&acc)[4][5], u32x4 (&n)[2][5], u32x4 (&wf)[2][4], const KbCtx& c) {
    mma_vv<S ? 0 : (T & 3) ? 1 : 2>(acc[T & 3][T >> 2], wf[S & 1][T & 3], n[S & 1][T >> 2], c.bias);
    SB();
}
constexpr int g1_aux(int t) {
    constexpr int a[20] = {0, 1, 2, 3, 4, -1, 5, -2, 6, -3, 7, -4, 8, 9, 10, -5, -6, -7, -8, -9};
    return a[t];
}
template <int S, int T>
__device__ __forceinline__ void g1_body(f32x4 (&acc)[4][5], u32x4 (&n)[2][5], u32x4 (&wf)[2][4], f32x4 (&d)[10], Nrm& t, const KbCtx& c) {
    g1_mma<S, T>(acc, n, wf, c);
    n[(S + 1) & 1][T >> 2][T & 3] = pk2(d[T >> 1][(T & 1) * 2], d[T >> 1][(T & 1) * 2 + 1]);
    constexpr int a = g1_aux(T);
    if constexpr (a < 0) g1_req<S, -1 - a>(c);
    if constexpr (a >= 3) w_read<a - 3>(wf[(S + 1) & 1], c, (S + 1) & 7);
    if constexpr (a >= 0 && a < 3 && S < 14) nrm_read<a>(t, c, S + 2);
    SB();
    if constexpr (T < 19) g1_body<S, T + 1>(acc, n, wf, d, t, c);
}
template <int S, int T>
__device__ __forceinline__ void g1_mid(f32x4 (&d)[10], const Nrm& t) {
    affine4<(S & 7) * 20 + T * 2>(d[T], (T & 1) ? t.a1 : t.a0, (T & 1) ? t.b1 : t.b0);
    SB();
    if constexpr (T < 9) g1_mid<S, T + 1>(d, t);
}
template <int T>
__device__ __forceinline__ void g1_last(f32x4 (&acc)[4][5], u32x4 (&n)[5], u32x4 (&wf)[4], const KbCtx& c) {
    constexpr int i = (T / 5 + 1) & 3, j = T % 5;
    mma_vv<0>(acc[i][j], wf[i], n[j], c.bias);
    SB();
    if constexpr (T < 8) { w_park<T / 4, (T & 3) * 2>(c); w_park<T / 4, (T & 3) * 2 + 1>(c); }
    red_put<T>(acc, c);
    constexpr int q0 = (24 - 4 * WL) * T / 20, q1 = (24 - 4 * WL) * (T + 1) / 20;
    if constexpr (q1 > q0) w_piece<q0 & 3>(c.srdW2, c.wv2[q0 & 3], w2_off(c, WL + q0 / 4), c.ring + (WL + q0 / 4) * 4096);
    SB();
    if constexpr (T < 19) g1_last<T + 1>(acc, n, wf, c);
}
template <int N> __device__ __forceinline__ void all_wait() {
    asm volatile("" ::: "memory");
    __builtin_amdgcn_s_waitcnt((N & 15) | 0x70 | ((N >> 4) << 14));
    asm volatile("" ::: "memory");
    SB();
}
template <int S>
__device__ __forceinline__ void g1_step(f32x4 (&acc)[4][5], u32x4 (&n)[2][5], u32x4 (&wf)[2][4], Nrm& t, const KbCtx& c) {
    if constexpr (S == 15) {
        all_wait<4 * WL - 8>();
        g1_last<0>(acc, n[1], wf[1], c);
    } else {
        constexpr int young = NPRO * 9 + g1_cum(S) - (S + 2) * 9;
        static_assert(young >= 0 && young + g1_cum(S + 1) - g1_cum(S) <= 63, "");
        all_wait<young>();
        f32x4 d[10];
        g1_mid<S + 1, 0>(d, t);
        g1_body<S, 0>(acc, n, wf, d, t, c);
        g1_step<S + 1>(acc, n, wf, t, c);
    }
}
constexpr int g2_slot(int s) { return s < 6 ? s : s < 10 ? s - 6 : s - 4; }
template <int S, int P>
__device__ __forceinline__ void g2_req(const KbCtx& c) {
    if constexpr (S < 14) a_piece<1, S & 7, P>(c.srdA, c.av0, c.av1, a2_off(c, S));
}
template <int S, int T>
__device__ __forceinline__ void g2_body(f32x4 (&acc)[4][5], u32x4 (&wf)[2][4], const KbCtx& c) {
    constexpr int i = (T / 5 + 1) & 3, j = T % 5;
    if constexpr (S < 4) mma_aa<S ? 0 : i ? 1 : 2, (S & 7) * 20 + j * 4, 160 + S * 16 + i * 4>(acc[i][j], c.bias);
    else mma_av<0, (S & 7) * 20 + j * 4>(acc[i][j], wf[S & 1][i], c.bias);
    SB();
    if constexpr (S == 13) red_put<T>(acc, c);
    else {
        if constexpr (T % 4 == 0) g2_req<S + G2D, T / 4>(c);
        else if constexpr (S >= 3 && T > 5 && T < 17) w_read<(T - T / 4 - 3) & 7>(wf[(S + 1) & 1], c, g2_slot(S + 1));
    }
    SB();
    if constexpr (T < 19) g2_body<S, T + 1>(acc, wf, c);
}
template <int S>
__device__ __forceinline__ void g2_step(f32x4 (&acc)[4][5], u32x4 (&wf)[2][4], const KbCtx& c) {
    vm_wait<5 * (S + G2D > 14 ? 13 - S : G2D - 1)>();
    if (S == 0) STAMPX(4, 1);
    SB();
    g2_body<S, 0>(acc, wf, c);
    if constexpr (S < 13) g2_step<S + 1>(acc, wf, c);
}
__device__ __forceinline__ void kb_reduce(f32x4 (&r)[5], const f32x4 (&acc)[4][5], char* smem, int w, int lane) {
    asm volatile("s_waitcnt lgkmcnt(0)\n\ts_barrier" ::: "memory");
    f32x4 p[5][3];
#pragma unroll
    for (int j = 0; j < 5; ++j)
#pragma unroll
        for (int i = 1; i < 4; ++i)
            p[j][i - 1] = *(const f32x4*)(smem + ((w - i) & 3) * B_REG + B_RED + lane * 16 + (i - 1) * 5120 + j * 1024);
    SB();
#pragma unroll
    for (int j = 0; j < 5; ++j) r[j] = (acc[0][j] + p[j][0]) + (p[j][1] + p[j][2]);
}
template <int S0, int S1>
__device__ __forceinline__ void g1_groups(const KbCtx& c) {
    if constexpr (S0 < S1) {
        if (S0 >= 3) vm_wait<54>();
        w_issue(c.srdW1, c.wv1, w1_off(c, S0), c.ring + S0 * 4096);
        a_issue<0, S0>(c.srdH, c.av0, c.av1, a1_off(c, S0));
        g1_groups<S0 + 1, S1>(c);
    }
}
template <int Q0, int Q1>
__device__ __forceinline__ void g1_tail(const KbCtx& c) {
    if constexpr (Q0 < Q1 && Q0 < (NPRO - NP1) * 9) {
        constexpr int g = NP1 + Q0 / 9, r = Q0 % 9;
        if constexpr (r < 4) w_piece<r>(c.srdW1, c.wv1[r], w1_off(c, g), c.ring + g * 4096);
        else a_piece<0, g, r - 4>(c.srdH, c.av0, c.av1, a1_off(c, g));
        g1_tail<Q0 + 1, Q1>(c);
    }
    SB();
}
template <int T>
__device__ __forceinline__ void g1_mid0(f32x4 (&d)[10], const Nrm& t, const KbCtx& c) {
    affine4<T * 2>(d[T], (T & 1) ? t.a1 : t.a0, (T & 1) ? t.b1 : t.b0);
    SB();
    if constexpr (T & 1) g1_tail<TQ2 + T / 2, TQ2 + T / 2 + 1>(c);
    if constexpr (T < 9) g1_mid0<T + 1>(d, t, c);
}
template <int S>
__device__ __forceinline__ void g2_first(const KbCtx& c) {
    a_issue<1, S>(c.srdA, c.av0, c.av1, a2_off(c, S));
    if constexpr (S < G2D - 1) g2_first<S + 1>(c);
}
template <int CTRL> __device__ __forceinline__ f32x2 dpp_add2(f32x2 v) { return (f32x2){dpp_add<CTRL>(v.x), dpp_add<CTRL>(v.y)}; }

extern "C" __global__ __launch_bounds__(256, 1) void kb_mlp(const u16* __restrict__ w1, const u16* __restrict__ b1, const u16* __restrict__ w2,
                                                 const u16* __restrict__ b2, char* ws, u16* __restrict__ out) {
    __shared__ __attribute__((aligned(16))) char smem[4 * B_REG];
    const int tid = threadIdx.x, lane = tid & 63;
    const int w = __builtin_amdgcn_readfirstlane(tid >> 6);
    STAMP(2, 0);
    const int n = lane & 15, hg = lane >> 4;
    KbCtx c;
    c.srdA = __builtin_amdgcn_make_buffer_rsrc((void*)ws, (short)0, WS_END, 0x00020000);
    u32x4 hs[3];
    {
        const unsigned v = hg * 1024 + n * 16;
        asm volatile("buffer_load_dwordx4 %0, %3, %5, %6 offen\n\tbuffer_load_dwordx4 %1, %3, %5, %7 offen\n\tbuffer_load_dwordx4 %2, %4, %5, %7 offen"
                     : "=&v"(hs[0]), "=&v"(hs[1]), "=&v"(hs[2])
                     : "v"(v), "v"(v + 4096 + (lane >> 5) * WS_END), "s"(c.srdA),
                       "s"(WS_HB + ((blockIdx.x >> 2) * 40 + 10 * w) * 1024 + (blockIdx.x & 3) * 256),
                       "s"(WS_HB + ((blockIdx.x >> 2) * 40 + 10 * w + 4) * 1024 + (blockIdx.x & 3) * 256) : "memory");
    }
    const int xcd = blockIdx.x & 7, idx = blockIdx.x >> 3;
    const bool g1 = idx < 27;
    const int t1 = (xcd >> 1) * 27 + (g1 ? idx : 0), t2 = (xcd >> 1) * 32 + idx;
    const int mt1 = t1 >> 2, dt1 = (xcd & 1) * 4 + (t1 & 3), mt2 = mt2_of(xcd >> 1, idx >> 2), dt2 = (xcd & 1) * 4 + (t2 & 3);
    char* ring = smem + w * B_REG;
    c.ring = (unsigned)(size_t)(LDS_AS char*)ring;
    c.srdW1 = __builtin_amdgcn_make_buffer_rsrc((void*)w1, (short)0, g1 ? L_ * F_ * 2 : 0, 0x00020000);
    c.srdH = __builtin_amdgcn_make_buffer_rsrc((void*)ws, (short)0, g1 ? WS_END : 0, 0x00020000);
    c.srdW2 = __builtin_amdgcn_make_buffer_rsrc((void*)w2, (short)0, L_ * F_ * 2, 0x00020000);
    const bool idw = w == 3;
    c.srdL = __builtin_amdgcn_make_buffer_rsrc((void*)(idw ? ws + WS_ID : (const char*)w2), (short)0, L_ * F_ * 2, 0x00020000);
    {
        const int wr = lane >> 3, wslot = lane & 7;
        const unsigned s0 = ((((wslot >> 1) ^ ((wr >> 1) & 1)) * 2 + (wslot & 1)) * 8) * 2;
        const unsigned s1 = ((((wslot >> 1) ^ (((wr >> 1) & 1) | 2)) * 2 + (wslot & 1)) * 8) * 2;
        c.wv1[0] = wr * F_ * 2 + s0; c.wv1[1] = (wr + 8) * F_ * 2 + s1 - 1024;
        c.wv1[2] = c.wv1[0] + 16 * F_ * 2 - 2048; c.wv1[3] = c.wv1[1] + 16 * F_ * 2 - 2048;
        c.wv2[0] = wr * L_ * 2 + s0; c.wv2[1] = (wr + 8) * L_ * 2 + s1 - 1024;
        c.wv2[2] = c.wv2[0] + 16 * L_ * 2 - 2048; c.wv2[3] = c.wv2[1] + 16 * L_ * 2 - 2048;
    }
    c.av0 = lane * 16; c.av1 = lane * 16 + 4096;
    c.w1o = (w * 16 * 32 * F_ + mt1 * 64) * 2;
    c.a1o = WS_HB + (w * 16 * 40 + dt1 * 5) * 1024;
    c.w2o = (w * 14 * 32 * L_ + mt2 * 64) * 2;
    c.a2o = WS_GB + (w * 14 * 40 + dt2 * 5) * 1024;
#pragma unroll
    for (int i = 0; i < 2; ++i) {
        c.wlo[i] = idw ? i * (32 * L_ * 2) : c.w2o + (12 + i) * (32 * L_ * 2);
        c.alo[i] = idw ? WS_HB + ((mt2 * 2 + i) * 40 + dt2 * 5) * 1024 : c.a2o + (12 + i) * 40960;
    }
    u32x2 bb;
    asm volatile("global_load_dwordx2 %0, %1, %2" : "=v"(bb) : "v"(hg * 8), "s"(b1 + mt1 * 64 + 16 * w) : "memory");
    asm volatile("" ::: "a0", "a223");
    g1_groups<0, PRE>(c);
    {
        const int q4 = n >> 2, p4 = n & 3;
        const int fsw = ((q4 >> 1) & 1) | ((hg & 1) << 1);
        const char* tb = ring + (8 * hg + q4) * 128 + p4 * 8;
#pragma unroll
        for (int i = 0; i < 4; ++i) {
            c.tp[i] = tb + ((((i + w) & 3) ^ fsw) * 32);
            c.tpa[i] = (unsigned)(size_t)(LDS_AS const char*)c.tp[i];
        }
        c.ta = ring + B_TAB + (8 * hg + (lane & 3)) * 8;
        c.tb = ring + B_TAB + 4096 + hg * 32;
        c.red = ring + B_RED + lane * 16;
    }
    const __amdgpu_buffer_rsrc_t srdG = __builtin_amdgcn_make_buffer_rsrc((void*)(ws + WS_G2), (short)0, L_ * 8, 0x00020000);
    {
        const unsigned one = (n & 1) ? 0x3f800000u : 0x3f80u;
        const int dw = (hg & 1) == (n >> 3) ? (n >> 1) & 3 : 4;
        const u32x4 sel = {dw == 0 ? one : 0u, dw == 1 ? one : 0u, dw == 2 ? one : 0u, dw == 3 ? one : 0u};
        const u32x4 z4 = {0u, 0u, 0u, 0u};
        const u32x4 selA = hg < 2 ? sel : z4, selB = hg < 2 ? z4 : sel;
        f32x4 sum, x0, x1, x2, x3, x4;
        asm volatile("s_waitcnt vmcnt(%12)\n\t"
                     "v_mfma_f32_16x16x32_bf16 %1, %6, %9, 0\n\tv_mfma_f32_16x16x32_bf16 %2, %6, %10, 0\n\t"
                     "v_mfma_f32_16x16x32_bf16 %3, %7, %9, 0\n\tv_mfma_f32_16x16x32_bf16 %4, %7, %10, 0\n\t"
                     "v_mfma_f32_16x16x32_bf16 %5, %8, %9, 0\n\tv_mfma_f32_16x16x32_bf16 %0, %6, %11, 0\n\t"
                     "v_mfma_f32_16x16x32_bf16 %0, %7, %11, %0\n\tv_mfma_f32_16x16x32_bf16 %0, %8, %11, %0\n\ts_nop 4"
                     : "=&v"(sum), "=&v"(x0), "=&v"(x1), "=&v"(x2), "=&v"(x3), "=&v"(x4)
                     : "v"(hs[0]), "v"(hs[1]), "v"(hs[2]), "v"(selA), "v"(selB), "v"(sel), "n"(9 * PRE + 1) : "memory");
        STAMPX(2, 1);
        f32x4 q = (x0 * x0 + x1 * x1) + (x2 * x2 + x3 * x3) + x4 * x4;
        asm volatile("s_nop 2" : "+v"(sum), "+v"(q));
        char* sc = smem + B_TAB + 6144;
        *(f32x2*)(sc + (n & 7) * 256 + (w * 8 + hg * 2 + (n >> 3)) * 8) = (f32x2){(sum[0] + sum[1]) + (sum[2] + sum[3]), (q[0] + q[1]) + (q[2] + q[3])};
        asm volatile("s_waitcnt lgkmcnt(0)\n\ts_barrier" ::: "memory");
        if (w == 0) {
            const f32x4 p0 = *(const f32x4*)(sc + lane * 32), p1 = *(const f32x4*)(sc + lane * 32 + 16);
            f32x2 v = {(p0[0] + p0[2]) + (p1[0] + p1[2]), (p0[1] + p0[3]) + (p1[1] + p1[3])};
            v = dpp_add2<0xb1>(v);
            v = dpp_add2<0x4e>(v);
            v = dpp_add2<0x141>(v);
            const float mean = v.x * (1.0f / D_);
            const float var = fmaxf(__builtin_fmaf(-mean, mean, v.y * (1.0f / D_)), 0.f);
            const float m1 = rb(mean), d1 = rb(__builtin_amdgcn_sqrtf(rb(rb(var) + 1e-5f)));
            const float i1 = __builtin_amdgcn_rcpf(d1);
            const float m2 = rb((mean - m1) * i1), d2 = rb(__builtin_amdgcn_sqrtf(rb(rb(var * i1 * i1) + 1e-5f)));
            const float al = rb(i1 * __builtin_amdgcn_rcpf(d2));
            const float be = -__builtin_fmaf(m2, d1, m1) * al;
            asm volatile("buffer_store_dwordx2 %0, %1, %2, %3 offen sc1"
                         :: "v"((u32x2){__float_as_uint(be), __float_as_uint(al) | 1u}), "v"((lane & 7) * (L_ * 8) + lane), "s"(srdG), "s"(blockIdx.x * 64) : "memory");
            STAMP(2, 2);
        }
    }
    f32x4 acc[4][5];
    u32x4 wf[2][4];
    unsigned f, bq;
    const unsigned fo = (lane < (w == 3 ? 6 : 7) ? lane : 0) * 128;
    const char* fp = ws + WS_FL + (dt2 * 32 + 7 * w) * 128;
    if (g1) {
        g1_groups<PRE, PPOS>(c);
        u32x4 g[4];
        const unsigned gvo = lane * 64;
        asm volatile("s_waitcnt vmcnt(59)\n\t"
                     "buffer_load_dwordx4 %0, %4, %5, %6 offen sc1\n\tbuffer_load_dwordx4 %1, %4, %5, %6 offen offset:16 sc1\n\t"
                     "buffer_load_dwordx4 %2, %4, %5, %6 offen offset:32 sc1\n\tbuffer_load_dwordx4 %3, %4, %5, %6 offen offset:48 sc1"
                     : "=&v"(g[0]), "=&v"(g[1]), "=&v"(g[2]), "=&v"(g[3]) : "v"(gvo), "s"(srdG), "s"(w * 4096) : "memory");
        g1_groups<PPOS, NP1>(c);
        STAMPX(2, 3);
        asm volatile("s_waitcnt vmcnt(%5)" : "+v"(g[0]), "+v"(g[1]), "+v"(g[2]), "+v"(g[3]), "+v"(bb) : "n"(9 * (NP1 - PPOS)) : "memory");
        STAMPX(2, 4);
        unsigned spins = 0;
#pragma unroll 1
        for (; spins < (1u << 16); ++spins) {
            if (__all(g[0].y & g[0].w & g[1].y & g[1].w & g[2].y & g[2].w & g[3].y & g[3].w & 1u)) break;
            asm volatile("buffer_load_dwordx4 %0, %4, %5, %6 offen sc1\n\tbuffer_load_dwordx4 %1, %4, %5, %6 offen offset:16 sc1\n\t"
                         "buffer_load_dwordx4 %2, %4, %5, %6 offen offset:32 sc1\n\tbuffer_load_dwordx4 %3, %4, %5, %6 offen offset:48 sc1\n\t"
                         "s_waitcnt vmcnt(0)"
                         : "=&v"(g[0]), "=&v"(g[1]), "=&v"(g[2]), "=&v"(g[3]) : "v"(gvo), "s"(srdG), "s"(w * 4096) : "memory");
        }
        STAMPX(2, 5);
        {
            char* tw = ring + B_TAB + lane * 64;
#pragma unroll
            for (int k = 0; k < 4; ++k) {
                const unsigned lo = g[k].y >> 16, hi = g[k].w & 0xffff0000u;
                *(u32x4*)(tw + k * 16) = (k & 1) ? (u32x4){0u, lo, 0u, hi} : (u32x4){lo, 0u, hi, 0u};
            }
            *(u32x4*)(ring + B_TAB + 4096 + lane * 32) = (u32x4){g[0].x, g[0].z, g[1].x, g[1].z};
            *(u32x4*)(ring + B_TAB + 4096 + lane * 32 + 16) = (u32x4){g[2].x, g[2].z, g[3].x, g[3].z};
            asm volatile("" ::: "memory");
        }
        Nrm t;
        u32x4 nf[2][5];
        nrm_read<0>(t, c, 0); nrm_read<1>(t, c, 0); nrm_read<2>(t, c, 0);
        SB();
        g1_tail<0, TQ1>(c);
        w_read<0>(wf[0], c, 0); w_read<1>(wf[0], c, 0); w_read<2>(wf[0], c, 0); w_read<3>(wf[0], c, 0);
        w_read<4>(wf[0], c, 0); w_read<5>(wf[0], c, 0); w_read<6>(wf[0], c, 0); w_read<7>(wf[0], c, 0);
        SB();
        g1_tail<TQ1, TQ2>(c);
        {
            c.bias = (f32x4){bflo(bb.x), bfhi(bb.x), bflo(bb.y), bfhi(bb.y)};
            asm volatile("" : "+v"(c.bias));
            f32x4 d[10];
            SB();
            g1_mid0<0>(d, t, c);
            nrm_read<0>(t, c, 1); nrm_read<1>(t, c, 1); nrm_read<2>(t, c, 1);
            asm volatile("s_nop 4");
            SB();
#pragma unroll
            for (int k = 0; k < 20; ++k) nf[0][k >> 2][k & 3] = pk2(d[k >> 1][(k & 1) * 2], d[k >> 1][(k & 1) * 2 + 1]);
            SB();
            g1_tail<TQ2 + 5, 27>(c);
        }
        STAMP(3, 1);
        g1_step<0>(acc, nf, wf, t, c);
        STAMP(3, 2);
        f32x4 r[5];
        kb_reduce(r, acc, smem, w, lane);
        STAMPX(3, 3);
        const unsigned go = WS_GB + ((mt1 * 2 + (w >> 1)) * 40 + dt1 * 5) * 1024;
        const unsigned gv = (((w & 1) * 2 + (hg >> 1)) * 16 + n) * 16 + (hg & 1) * 8;
        if (!PK23) {
            vm_wait<8>();
            w_park8<2>(c);
            w_park8<3>(c);
        }
#pragma unroll
        for (int j = 0; j < 5; ++j) {
            if (j == 2) asm volatile("s_waitcnt lgkmcnt(0)" ::: "memory");
            if (j < EPW) w_issue(c.srdW2, c.wv2, w2_off(c, 6 + j), c.ring + j * 4096);
            SB();
            u32x2 o;
            o.x = pk2(gelu_tanh(r[j][0]), gelu_tanh(r[j][1]));
            o.y = pk2(gelu_tanh(r[j][2]), gelu_tanh(r[j][3]));
            __builtin_amdgcn_raw_buffer_store_b64(o, c.srdA, gv + j * 1024, go, 16);
            SB();
        }
        STAMPX(3, 4);
        asm volatile("s_waitcnt vmcnt(0)" ::: "memory");
        if (PK23) {
            w_park8<2>(c);
            w_park8<3>(c);
        }
        asm volatile("s_barrier" ::: "memory");
        if (tid == 0) __hip_atomic_store((gu32*)(ws + WS_FL + (dt1 * 32 + mt1) * 128), 1u, __ATOMIC_RELAXED, __HIP_MEMORY_SCOPE_AGENT);
        STAMP(3, 5);
        asm volatile("s_waitcnt lgkmcnt(0)" ::: "memory");
#pragma unroll
        for (int s = 6 + EPW; s < 14; ++s) {
            if (s == 14 - PLATE)
                asm volatile("global_load_ushort %1, %4, %5\n\tglobal_load_dword %0, %2, %3 sc1"
                             : "=&v"(f), "=&v"(bq) : "v"(fo), "s"(fp), "v"(n * 2), "s"(b2 + mt2 * 64 + 16 * w) : "memory");
            w_issue(s < 12 ? c.srdW2 : c.srdL, c.wv2, w2_off(c, s), c.ring + g2_slot(s) * 4096);
        }
        asm volatile("s_waitcnt vmcnt(%2)" : "+v"(f), "+v"(bq) : "n"(4 * PLATE) : "memory");
    } else {
        const unsigned t0 = (unsigned)__builtin_amdgcn_s_memrealtime();
#pragma unroll
        for (int s = 0; s < 6; ++s) w_issue(c.srdW2, c.wv2, w2_off(c, s), c.ring + s * 4096);
        vm_wait<0>();
        w_park8<0>(c); w_park8<1>(c); w_park8<2>(c); w_park8<3>(c);
        asm volatile("s_waitcnt lgkmcnt(0)" ::: "memory");
#pragma unroll
        for (int s = 6; s < 13; ++s) w_issue(s < 12 ? c.srdW2 : c.srdL, c.wv2, w2_off(c, s), c.ring + g2_slot(s) * 4096);
        fetch_w2(w2, W2N + ((idx - 27) * 4 + w) * 8 + xcd, 864, 160, c.ring + 9 * 4096, lane);
        pause_until(t0, KT);
        warm_w<1>(w2, b2, xcd, (idx - 27) * 4 + w, 20, c.ring + 9 * 4096, lane);
        vm_wait<0>();
        w_issue(c.srdL, c.wv2, w2_off(c, 13), c.ring + g2_slot(13) * 4096);
        asm volatile("global_load_ushort %1, %4, %5\n\tglobal_load_dword %0, %2, %3 sc1\n\ts_waitcnt vmcnt(0)"
                     : "=&v"(f), "=&v"(bq) : "v"(fo), "s"(fp), "v"(n * 2), "s"(b2 + mt2 * 64 + 16 * w) : "memory");
        STAMP(3, 6);
    }
    {
        unsigned spins = 0;
#pragma unroll 1
        for (; spins < (1u << 16); ++spins) {
            if (__all(f != 0)) break;
            asm volatile("global_load_dword %0, %1, %2 sc1\n\ts_waitcnt vmcnt(0)" : "=&v"(f) : "v"(fo), "s"(fp) : "memory");
        }
    }
    STAMP(4, 0);
    g2_first<0>(c);
    {
        const float b = bflo(bq);
        c.bias = (f32x4){b, b, b, b};
        asm volatile("" : "+v"(c.bias));
    }
    SB();
    g2_step<0>(acc, wf, c);
    STAMP(4, 2);
    f32x4 r[5];
    kb_reduce(r, acc, smem, w, lane);
    STAMPX(4, 3);
    {
        u16* op = out + (size_t)(mt2 * 64 + 16 * w + n) * D_ + dt2 * 80 + 20 * hg;
        typedef unsigned u32x4u __attribute__((ext_vector_type(4), aligned(8)));
        u32x4u o0 = {pk2(r[0][0], r[0][1]), pk2(r[0][2], r[0][3]), pk2(r[1][0], r[1][1]), pk2(r[1][2], r[1][3])};
        u32x4u o1 = {pk2(r[2][0], r[2][1]), pk2(r[2][2], r[2][3]), pk2(r[3][0], r[3][1]), pk2(r[3][2], r[3][3])};
        u32x2 o2 = {pk2(r[4][0], r[4][1]), pk2(r[4][2], r[4][3])};
        *(u32x4u*)op = o0;
        *(u32x4u*)(op + 8) = o1;
        *(u32x2*)(op + 16) = o2;
    }
    STAMP(4, 4);
    asm volatile(".Lkb_mlp_last:");
}

extern "C" void kernel_launch(void* const* d_in, const int* in_sizes, int n_in, void* d_out, int out_size,
                              void* d_ws, size_t ws_size, hipStream_t stream) {
    const u16* x = (const u16*)d_in[0];
    const u16* logA = (const u16*)d_in[1];
    const u16* Aim = (const u16*)d_in[2];
    const u16* Cre = (const u16*)d_in[3];
    const u16* Cim = (const u16*)d_in[4];
    const u16* logdt = (const u16*)d_in[5];
    const u16* dskip = (const u16*)d_in[6];
    const u16* w1 = (const u16*)d_in[8];
    const u16* b1 = (const u16*)d_in[9];
    const u16* w2 = (const u16*)d_in[10];
    const u16* b2 = (const u16*)d_in[11];
    char* ws = (char*)d_ws;
    ka_conv<<<256, 512, 0, stream>>>(x, logA, Aim, Cre, Cim, logdt, dskip, ws, w1, b1, w2, b2);
    kb_mlp<<<256, 256, 0, stream>>>(w1, b1, w2, b2, ws, (u16*)d_out);
}
```

```cpp
#include <hip/hip_runtime.h>
#include <hip/hip_bf16.h>
#include <stdint.h>
#pragma clang diagnostic ignored "-Winline-asm"

typedef unsigned short u16;
typedef __bf16 bf16x8 __attribute__((ext_vector_type(8)));
typedef __bf16 bf16x2 __attribute__((ext_vector_type(2)));
typedef short s16x4 __attribute__((ext_vector_type(4)));
typedef float f32x4 __attribute__((ext_vector_type(4)));
typedef float f32x2 __attribute__((ext_vector_type(2)));
typedef unsigned u32x4 __attribute__((ext_vector_type(4)));
typedef unsigned u32x2 __attribute__((ext_vector_type(2)));
#define LDS_AS __attribute__((address_space(3)))

#define STAMP(k, i) do { } while (0)
#define STAMPT(k, i, T) do { } while (0)

#define TS(i) do { } while (0)
#define TSPUT(k, T) do { } while (0)
#define STAMPX(k, i) do { } while (0)
#define FSTAMP STAMP
constexpr int L_ = 2048;
constexpr int D_ = 640;
constexpr int N_ = 128;
constexpr int F_ = 1728;
constexpr unsigned SZ_ = L_ * D_ * 2;
constexpr unsigned WS_HB = 0, WS_GB = SZ_, WS_END = 2 * SZ_;
constexpr unsigned WS_ID = 2 * SZ_, WS_GR = WS_ID + 64 * L_ * 2, WS_EP = WS_GR + 32768;
constexpr unsigned WS_G2 = WS_EP + 4096, WS_FL = WS_G2 + L_ * 8;

__device__ __forceinline__ float bf2f(unsigned v) { return __uint_as_float(v << 16); }
__device__ __forceinline__ float bflo(unsigned v) { return __uint_as_float(v << 16); }
__device__ __forceinline__ float bfhi(unsigned v) { return __uint_as_float(v & 0xffff0000u); }
__device__ __forceinline__ unsigned pk2(float lo, float hi) {
    f32x2 f = {lo, hi};
    bf16x2 b = __builtin_convertvector(f, bf16x2);
    return __builtin_bit_cast(unsigned, b);
}
__device__ __forceinline__ float rb(float x) { return bflo(pk2(x, x)); }

__device__ __forceinline__ f32x4 mfma16(u32x4 a, u32x4 b, f32x4 c) {
    return __builtin_amdgcn_mfma_f32_16x16x32_bf16(__builtin_bit_cast(bf16x8, a), __builtin_bit_cast(bf16x8, b), c, 0, 0, 0);
}
__device__ __forceinline__ u32x2 tr_read(const char* p) {
    s16x4 v = __builtin_amdgcn_ds_read_tr16_b64_v4i16((LDS_AS s16x4*)p);
    return __builtin_bit_cast(u32x2, v);
}
template <int CTRL>
__device__ __forceinline__ float dpp_add(float v) {
    int t = __builtin_amdgcn_update_dpp(0, __float_as_int(v), CTRL, 0xf, 0xf, true);
    return v + __int_as_float(t);
}
__device__ __forceinline__ float row16_sum(float v) {
    v = dpp_add<0x128>(v);
    v = dpp_add<0x124>(v);
    v = dpp_add<0x122>(v);
    v = dpp_add<0x121>(v);
    return v;
}

template <int NT = 0>
__device__ __forceinline__ void touch4(__amdgpu_buffer_rsrc_t srd, unsigned voff, unsigned soff, unsigned m0v) {
    if (NT) asm volatile("s_mov_b32 m0, %0\n\ts_nop 0\n\tbuffer_load_dword %1, %2, %3 offen nt lds"
                 :: "s"(m0v), "v"(voff), "s"(srd), "s"(soff) : "m0", "memory");
    else asm volatile("s_mov_b32 m0, %0\n\ts_nop 0\n\tbuffer_load_dword %1, %2, %3 offen lds"
                 :: "s"(m0v), "v"(voff), "s"(srd), "s"(soff) : "m0", "memory");
}
#define MT2S 1
__device__ __forceinline__ int mt2_of(int p, int i) { return MT2S == 1 ? 8 * p + i : MT2S == 2 ? 16 * (p >> 1) + 2 * i + (p & 1) : 4 * i + p; }
#define W2M 1
#define W2N 864
#define XT 1
#define W1SL 3
#define KT 300
#define W1X 8
#define BAL 1
#define W2C 320
#define CW 11
#define CODE_SRD(srd, sym) do { unsigned lo_, hi_, n_; \
    asm("s_getpc_b64 vcc\n\ts_add_u32 %0, vcc_lo, " sym "@rel32@lo+4\n\ts_addc_u32 %1, vcc_hi, " sym "@rel32@hi+12\n\t" \
        "s_mov_b32 %2, .L" sym "_last-" sym : "=s"(lo_), "=s"(hi_), "=s"(n_) :: "vcc", "scc"); \
    srd = __builtin_amdgcn_make_buffer_rsrc((void*)(((unsigned long long)hi_ << 32) | lo_), (short)0, n_, 0x00020000); } while (0)
__device__ __forceinline__ void pause_until(unsigned t0, unsigned ticks) {
#pragma unroll 1
    for (int i = 0; i < 64 && (unsigned)__builtin_amdgcn_s_memrealtime() - t0 < ticks; ++i) __builtin_amdgcn_s_sleep(4);
}
__device__ __forceinline__ void fetch_w2(const u16* W, int j, int j1, int nw, unsigned lds, int lane) {
    const __amdgpu_buffer_rsrc_t srdW = __builtin_amdgcn_make_buffer_rsrc((void*)W, (short)0, L_ * F_ * 2, 0x00020000);
    for (; j < j1; j += nw) touch4(srdW, lane * 128, j * 8192, lds);
}
template <int MODE>
__device__ __forceinline__ void warm_w(const u16* W, const u16* bias, int xcd, int gw, int nw, unsigned lds, int lane) {
    constexpr int LDW = MODE == 0 ? F_ : L_;
    const __amdgpu_buffer_rsrc_t srdW = __builtin_amdgcn_make_buffer_rsrc((void*)W, (short)0, L_ * F_ * 2, 0x00020000);
    if (gw == 0) touch4(__builtin_amdgcn_make_buffer_rsrc((void*)bias, (short)0, LDW * 2, 0x00020000), lane * 128, 0, lds);
    const int mt0 = MODE == 0 ? ((xcd >> 1) * 27) >> 2 : mt2_of(xcd >> 1, 0);
    const unsigned voff = ((lane >> 3) * LDW + (lane & 7) * (MODE == 0 ? 64 : 64 * MT2S)) * 2;
    int r = 0;
    for (int j = gw; j < (MODE == 0 ? 256 : 224); j += nw) {
        if (MODE == 0 && r++ < W1SL) asm volatile("s_waitcnt vmcnt(0)" ::: "memory");
        const int s = MODE == 0 ? (j >> 4) ^ ((xcd & 1) * W1X) : j >> 4, w = (j >> 2) & 3;
        const int ks = MODE == 0 ? w * 16 + s : w * 14 + s;
        if (MODE != 0 && ks >= 54) continue;
        touch4(srdW, voff, ((ks * 32 + (j & 3) * 8) * LDW + mt0 * 64) * 2, lds);
    }
}

constexpr int KA_ROWB = 528;
constexpr int KA_LO = 32 * KA_ROWB;
constexpr int KA_HI0 = 4 * KA_LO;
constexpr int KA_HI = 13312;
constexpr int KA_MB = 20000;
constexpr int CV_COPY = 4160;
constexpr int CV_UB = 4608;
constexpr int KA_U = KA_HI0 + 4 * KA_HI;
#define XC1 4
#define XCS 3
#define GFIRST " sc1"
typedef __attribute__((address_space(1))) unsigned gu32;

__device__ __forceinline__ void cm4(f32x2& r0, f32x2 a0, f32x2 b0, f32x2& r1, f32x2 a1, f32x2 b1,
                                    f32x2& r2, f32x2 a2, f32x2 b2, f32x2& r3, f32x2 a3, f32x2 b3) {
    asm("v_pk_mul_f32 %0, %4, %5 op_sel:[0,0] op_sel_hi:[1,0]\n\t"
        "v_pk_mul_f32 %1, %6, %7 op_sel:[0,0] op_sel_hi:[1,0]\n\t"
        "v_pk_mul_f32 %2, %8, %9 op_sel:[0,0] op_sel_hi:[1,0]\n\t"
        "v_pk_mul_f32 %3, %10, %11 op_sel:[0,0] op_sel_hi:[1,0]\n\t"
        "v_pk_fma_f32 %0, %4, %5, %0 op_sel:[1,1,0] op_sel_hi:[0,1,1] neg_lo:[1,0,0]\n\t"
        "v_pk_fma_f32 %1, %6, %7, %1 op_sel:[1,1,0] op_sel_hi:[0,1,1] neg_lo:[1,0,0]\n\t"
        "v_pk_fma_f32 %2, %8, %9, %2 op_sel:[1,1,0] op_sel_hi:[0,1,1] neg_lo:[1,0,0]\n\t"
        "v_pk_fma_f32 %3, %10, %11, %3 op_sel:[1,1,0] op_sel_hi:[0,1,1] neg_lo:[1,0,0]"
        : "=&v"(r0), "=&v"(r1), "=&v"(r2), "=&v"(r3)
        : "v"(a0), "v"(b0), "v"(a1), "v"(b1), "v"(a2), "v"(b2), "v"(a3), "v"(b3));
}
constexpr int ka_off(int r) { return r < 32 ? r * KA_ROWB : KA_HI0 - KA_MB + (r - 32) * KA_ROWB; }
template <int R0, int R1, int R2, int R3>
__device__ __forceinline__ void put4(const unsigned (&m0v)[2], f32x2 v0, f32x2 v1, f32x2 v2, f32x2 v3) {
    static_assert((R0 < 32) == (R1 < 32) && (R0 < 32) == (R2 < 32) && (R0 < 32) == (R3 < 32), "");
    unsigned t0, t1, t2, t3;
    asm volatile("s_mov_b32 m0, %4\n\t"
                 "v_cvt_pk_bf16_f32 %0, %5, %6\n\tv_cvt_pk_bf16_f32 %1, %7, %8\n\t"
                 "v_cvt_pk_bf16_f32 %2, %9, %10\n\tv_cvt_pk_bf16_f32 %3, %11, %12\n\t"
                 "ds_write_addtid_b32 %0 offset:%13\n\tds_write_addtid_b32 %1 offset:%14\n\t"
                 "ds_write_addtid_b32 %2 offset:%15\n\tds_write_addtid_b32 %3 offset:%16"
                 : "=&v"(t0), "=&v"(t1), "=&v"(t2), "=&v"(t3)
                 : "s"(m0v[R0 >= 32]), "v"(v0.x), "v"(v0.y), "v"(v1.x), "v"(v1.y), "v"(v2.x), "v"(v2.y), "v"(v3.x), "v"(v3.y),
                   "n"(ka_off(R0)), "n"(ka_off(R1)), "n"(ka_off(R2)), "n"(ka_off(R3))
                 : "m0", "memory");
}
__device__ __forceinline__ f32x2 cexp_p(float zr, float zi, float p) {
    const float mag = __builtin_amdgcn_exp2f(zr * p);
    const float rev = __builtin_amdgcn_fractf(zi * p);
    f32x2 r;
    r.x = mag * __builtin_amdgcn_cosf(rev);
    r.y = mag * __builtin_amdgcn_sinf(rev);
    return r;
}
__device__ __forceinline__ float wave_sum(float v) {
    v = row16_sum(v);
    const int i = __float_as_int(v);
    return (__int_as_float(__builtin_amdgcn_readlane(i, 0)) + __int_as_float(__builtin_amdgcn_readlane(i, 16))) +
           (__int_as_float(__builtin_amdgcn_readlane(i, 32)) + __int_as_float(__builtin_amdgcn_readlane(i, 48)));
}
__device__ __forceinline__ unsigned next_lane(unsigned v, unsigned last) {
    return (unsigned)__builtin_amdgcn_update_dpp((int)last, (int)v, 0x130, 0xf, 0xf, false);
}
__device__ __forceinline__ void put_copies(char* p, unsigned r0, unsigned r1, unsigned r2, unsigned r3, unsigned r4, unsigned r5) {
    const unsigned s0 = __builtin_amdgcn_alignbit(r1, r0, 16), s1 = __builtin_amdgcn_alignbit(r2, r1, 16),
                   s2 = __builtin_amdgcn_alignbit(r3, r2, 16), s3 = __builtin_amdgcn_alignbit(r4, r3, 16),
                   s4 = __builtin_amdgcn_alignbit(r5, r4, 16);
    *(u32x4*)(p + 0 * CV_COPY) = (u32x4){r0, r1, r2, r3};
    *(u32x4*)(p + 1 * CV_COPY) = (u32x4){s0, s1, s2, s3};
    *(u32x4*)(p + 2 * CV_COPY) = (u32x4){r1, r2, r3, r4};
    *(u32x4*)(p + 3 * CV_COPY) = (u32x4){s1, s2, s3, s4};
}
template <int O> __device__ __forceinline__ void rd64(u32x2& v, unsigned a) {
    asm volatile("ds_read_b64 %0, %1 offset:%2" : "=v"(v) : "v"(a), "n"(O) : "memory");
}
template <int O> __device__ __forceinline__ void rd128(u32x4& v, unsigned a) {
    asm volatile("ds_read_b128 %0, %1 offset:%2" : "=v"(v) : "v"(a), "n"(O) : "memory");
}
struct CvFrag { u32x2 lo[8], hi[8]; u32x4 b[8]; };
__device__ __forceinline__ void cv_wait(CvFrag& f) {
    asm volatile("s_waitcnt lgkmcnt(0)"
                 : "+v"(f.lo[0]), "+v"(f.lo[1]), "+v"(f.lo[2]), "+v"(f.lo[3]), "+v"(f.lo[4]), "+v"(f.lo[5]), "+v"(f.lo[6]), "+v"(f.lo[7]),
                   "+v"(f.hi[0]), "+v"(f.hi[1]), "+v"(f.hi[2]), "+v"(f.hi[3]), "+v"(f.hi[4]), "+v"(f.hi[5]), "+v"(f.hi[6]), "+v"(f.hi[7]),
                   "+v"(f.b[0]), "+v"(f.b[1]), "+v"(f.b[2]), "+v"(f.b[3]), "+v"(f.b[4]), "+v"(f.b[5]), "+v"(f.b[6]), "+v"(f.b[7])
                 :: "memory");
}
template <int T>
__device__ __forceinline__ void cv_mma(const CvFrag& f, f32x4 (&acc)[8]) {
    constexpr int q = T < 1 ? 0 : T < 3 ? 1 : T < 6 ? 2 : T < 10 ? 3 : T < 15 ? 4 : T < 21 ? 5 : T < 28 ? 6 : 7;
    constexpr int g1 = T - q * (q + 1) / 2;
    const u32x4 a = {f.lo[g1].x, f.lo[g1].y, f.hi[g1].x, f.hi[g1].y};
    acc[q] = mfma16(a, f.b[q - g1], acc[q]);
}
template <int G0, int R>
__device__ __forceinline__ void cv_read(CvFrag& f, unsigned aa, unsigned ba) {
    constexpr int G1 = R / 3;
    if constexpr (R % 3 == 0) rd64<(63 - 8 * G1 - G0) * 64>(f.lo[G1], aa);
    else if constexpr (R % 3 == 1) rd64<(63 - 8 * G1 - G0) * 64 + 8>(f.hi[G1], aa);
    else rd128<(8 * G1 - G0 + 7) * 64>(f.b[G1], ba);
}
template <int G0, int T>
__device__ __forceinline__ void cv_group(const CvFrag& cur, CvFrag& nxt, f32x4 (&acc)[8], unsigned aa, unsigned ba) {
    cv_mma<T>(cur, acc);
    __builtin_amdgcn_sched_barrier(0);
    if constexpr (G0 < 7 && T < 30 && T % 5 != 4) {
        cv_read<G0 + 1, T - T / 5>(nxt, aa, ba);
        __builtin_amdgcn_sched_barrier(0);
    }
    if constexpr (T < 35) cv_group<G0, T + 1>(cur, nxt, acc, aa, ba);
}
template <int G0>
__device__ __forceinline__ void cv_round(CvFrag& cur, CvFrag& nxt, f32x4 (&acc)[8], unsigned aa, unsigned ba) {
    cv_wait(cur);
    if constexpr (G0 == 5) __builtin_amdgcn_s_barrier();
    __builtin_amdgcn_sched_barrier(0);
    cv_group<G0, 0>(cur, nxt, acc, aa, ba);
    if constexpr (G0 < 7) cv_round<G0 + 1>(nxt, cur, acc, aa, ba);
}
template <int I>
__device__ __forceinline__ void cv_first(CvFrag& f, unsigned aa, unsigned ba) {
    cv_read<0, I>(f, aa, ba);
    if constexpr (I < 23) cv_first<I + 1>(f, aa, ba);
}

template <class Hook>
__device__ __forceinline__ void ka_chain(const unsigned (&m0v)[2], float p_la, float p_ai, float p_cr, float p_ci, float p_dt, Hook hook) {
    const float dt = rb(__builtin_amdgcn_exp2f(p_dt * 1.44269504088896341f));
    const float ar = -rb(__builtin_amdgcn_exp2f(p_la * 1.44269504088896341f));
    const float zr = ar * dt * 1.44269504088896341f, zi = p_ai * dt * 0.15915494309189535f;
    f32x2 a[17], b[16], e[8], F1, F2, F4, F8, r0, r1, r2, r3;
    a[0] = (f32x2){2.0f * (p_cr * dt), -2.0f * (p_ci * dt)};
    e[1] = cexp_p(zr, -zi, 1.f);
    b[1] = cexp_p(zr, zi, 8.f);
    F1 = cexp_p(zr, -zi, 128.f);
    cm4(e[2], e[1], e[1], b[2], b[1], b[1], F2, F1, F1, a[1], a[0], F1);
    cm4(e[4], e[2], e[2], b[4], b[2], b[2], F4, F2, F2, e[3], e[2], e[1]);
    put4<41, 42, 43, 45>(m0v, (f32x2){1.f, 0.f}, b[1], b[2], b[4]);
    hook(0);
    cm4(a[2], a[0], F2, a[3], a[1], F2, b[3], b[2], b[1], r0, a[0], e[1]);
    cm4(F8, F4, F4, b[8], b[4], b[4], r1, a[1], e[1], r2, a[0], e[2]);
    put4<16, 20, 1, 9>(m0v, a[2], a[3], r0, r1);
    hook(1);
    cm4(a[4], a[0], F4, a[5], a[1], F4, a[6], a[2], F4, a[7], a[3], F4);
    put4<24, 26, 28, 30>(m0v, a[4], a[5], a[6], a[7]);
    hook(2);
    cm4(e[5], e[4], e[1], e[6], e[4], e[2], e[7], e[4], e[3], r3, a[1], e[2]);
    put4<0, 8, 2, 10>(m0v, a[0], a[1], r2, r3);
    hook(3);
    cm4(b[5], b[4], b[1], b[6], b[4], b[2], b[7], b[4], b[3], r0, a[0], e[3]);
    put4<44, 49, 46, 47>(m0v, b[3], b[8], b[5], b[6]);
    hook(4);
    cm4(a[8], a[0], F8, a[9], a[1], F8, a[10], a[2], F8, a[11], a[3], F8);
    put4<32, 33, 34, 35>(m0v, a[8], a[9], a[10], a[11]);
    hook(5);
    cm4(a[12], a[4], F8, a[13], a[5], F8, a[14], a[6], F8, a[15], a[7], F8);
    put4<36, 37, 38, 39>(m0v, a[12], a[13], a[14], a[15]);
    hook(6);
    cm4(a[16], a[8], F8, r1, a[1], e[3], r2, a[0], e[4], r3, a[1], e[4]);
    put4<3, 11, 4, 12>(m0v, r0, r1, r2, r3);
    hook(7);
    cm4(b[9], b[8], b[1], b[10], b[8], b[2], b[11], b[8], b[3], b[12], b[8], b[4]);
    put4<50, 51, 52, 53>(m0v, b[9], b[10], b[11], b[12]);
    hook(8);
    cm4(b[13], b[8], b[5], b[14], b[8], b[6], b[15], b[8], b[7], r0, a[0], e[5]);
    put4<54, 55, 56, 48>(m0v, b[13], b[14], b[15], b[7]);
    asm volatile("ds_write_addtid_b32 %0 offset:%1" :: "v"(pk2(a[16].x, a[16].y)), "n"(ka_off(40)) : "memory");
    hook(9);
    cm4(r1, a[1], e[5], r2, a[0], e[6], r3, a[1], e[6], F1, a[0], e[7]);
    put4<5, 13, 6, 14>(m0v, r0, r1, r2, r3);
    hook(10);
    cm4(r0, a[1], e[7], r1, a[2], e[2], r2, a[3], e[2], F2, a[2], e[4]);
    put4<7, 15, 17, 21>(m0v, F1, r0, r1, r2);
    hook(11);
    cm4(r3, a[3], e[4], F1, a[2], e[6], r0, a[3], e[6], r1, a[4], e[4]);
    put4<18, 22, 19, 23>(m0v, F2, r3, F1, r0);
    hook(12);
    cm4(r2, a[5], e[4], r3, a[6], e[4], F1, a[7], e[4], F2, a[7], e[4]);
    put4<25, 27, 29, 31>(m0v, r1, r2, r3, F1);
    asm volatile("s_waitcnt lgkmcnt(0)" ::: "memory");
}
__device__ __forceinline__ void unpack4(float (&o)[4], u32x2 v) {
    asm volatile("v_lshlrev_b32 %0, 16, %4\n\tv_and_b32 %1, 0xffff0000, %4\n\tv_lshlrev_b32 %2, 16, %5\n\tv_and_b32 %3, 0xffff0000, %5"
                 : "=&v"(o[0]), "=&v"(o[1]), "=&v"(o[2]), "=&v"(o[3]) : "v"(v.x), "v"(v.y));
}

extern "C" __global__ __launch_bounds__(512, 2) void ka_conv(const u16* __restrict__ x, const u16* __restrict__ logA,
                                                  const u16* __restrict__ Aim, const u16* __restrict__ Cre,
                                                  const u16* __restrict__ Cim, const u16* __restrict__ logdt,
                                                  const u16* __restrict__ dskip, char* ws,
                                                  const u16* __restrict__ w1, const u16* __restrict__ b1,
                                                  const u16* __restrict__ w2, const u16* __restrict__ b2) {
    __shared__ __attribute__((aligned(16))) char smem[KA_U + 4 * CV_UB + 256];
    const int tid = threadIdx.x, lane = tid & 63;
    const int w = __builtin_amdgcn_readfirstlane(tid >> 6);
    STAMP(0, 0);
    if ((CW & 1) && w >= 6) {
        __amdgpu_buffer_rsrc_t srdC;
        CODE_SRD(srdC, "ka_conv");
        touch4(srdC, lane * 128, (w - 6) * 8192, (unsigned)(size_t)(LDS_AS char*)(smem + KA_U + 4 * CV_UB));
    }
    asm volatile("" :: "s"(x), "s"(logA), "s"(Aim), "s"(Cre), "s"(Cim), "s"(logdt), "s"(dskip), "s"(ws));
    const int xcd = blockIdx.x & 7, idx = blockIdx.x >> 3;
    const bool conv = idx < 20;
    const int grp = ((xcd & 1) * 4 + (xcd >> 1)) * 20 + (conv ? idx : 0);
    const int d0 = grp * 4;
    const int row = (xcd * 32 + idx) * 8 + w;
    unsigned sv[5], epoch, pp[5];
    float pf[5];
    asm volatile("global_load_dword %0, %11, %12\n\tglobal_load_dword %1, %11, %12 offset:256\n\t"
                 "global_load_dword %2, %11, %12 offset:512\n\tglobal_load_dword %3, %11, %12 offset:768\n\t"
                 "global_load_dword %4, %11, %12 offset:1024\n\tglobal_load_dword %5, %13, %14\n\t"
                 "global_load_ushort %6, %15, %16\n\tglobal_load_ushort %7, %15, %17\n\t"
                 "global_load_ushort %8, %15, %18\n\tglobal_load_ushort %9, %15, %19\n\t"
                 "global_load_ushort %10, %13, %20"
                 : "=&v"(sv[0]), "=&v"(sv[1]), "=&v"(sv[2]), "=&v"(sv[3]), "=&v"(sv[4]), "=&v"(epoch),
                   "=&v"(pp[0]), "=&v"(pp[1]), "=&v"(pp[2]), "=&v"(pp[3]), "=&v"(pp[4])
                 : "v"(lane * 4), "s"(x + (size_t)row * D_), "v"(0), "s"(ws + WS_EP),
                   "v"((d0 * N_ + tid) * 2), "s"(logA), "s"(Aim), "s"(Cre), "s"(Cim), "s"(logdt + d0 + (w >> 1))
                 : "memory");
    const int t = tid & 255;
    const __amdgpu_buffer_rsrc_t srdG = __builtin_amdgcn_make_buffer_rsrc((void*)(ws + WS_GR), (short)0, L_ * 8, 0x00020000);
    const __amdgpu_buffer_rsrc_t srdX = __builtin_amdgcn_make_buffer_rsrc((void*)x, (short)0, SZ_, 0x00020000);
    {
        const int tw = XT == 1 ? (BAL ? (conv ? 64 : (7 - w) * 12 + idx - 20) : (idx - 20) * 8 + w) : XT == 2 ? (w < 4 ? idx * 4 + w : 64) : XT == 3 ? (w < 2 ? idx * 2 + w : 64) : 64;
        touch4(srdX, (lane >> 1) * (D_ * 2) + (lane & 1) * 156, (unsigned)tw < 64u ? tw * (32 * D_ * 2) + (grp / 20) * 160 : SZ_,
               (unsigned)(size_t)(LDS_AS char*)(smem + KA_U + 4 * CV_UB));
    }
    const int owner = conv ? w >> 2 : 0;
    u32x2 xc[8], xd;
    asm volatile("s_barrier\n\ts_cmp_eq_u32 %5, 0\n\ts_cbranch_scc1 .Lka_a%=\n\t"
                 "global_load_dwordx2 %4, %6, %7\n\t"
                 "buffer_load_dwordx2 %0, %8, %9, %10 offen\n\tbuffer_load_dwordx2 %1, %8, %9, %10 offen offset:1280\n\t"
                 "buffer_load_dwordx2 %2, %8, %9, %10 offen offset:2560\n\tbuffer_load_dwordx2 %3, %8, %9, %10 offen offset:3840\n\t"
                 "s_waitcnt vmcnt(6)\n\ts_branch .Lka_b%=\n"
                 ".Lka_a%=:\n\ts_waitcnt vmcnt(1)\n"
                 ".Lka_b%=:"
                 : "=&v"(xc[0]), "=&v"(xc[1]), "=&v"(xc[2]), "=&v"(xc[3]), "=&v"(xd)
                 : "s"(owner), "v"(0), "s"(dskip + d0), "v"(t * (8 * D_ * 2)), "s"(srdX), "s"(d0 * 2) : "scc", "memory");
    unsigned etag;
    {
        float v[12];
        unpack4(*(float(*)[4])v, (u32x2){sv[0], sv[1]});
        unpack4(*(float(*)[4])(v + 4), (u32x2){sv[2], sv[3]});
        asm volatile("v_lshlrev_b32 %0, 16, %3\n\tv_and_b32 %1, 0xffff0000, %3\n\tv_xor_b32 %2, 0x9e3779b9, %4"
                     : "=&v"(v[8]), "=&v"(v[9]), "=&v"(etag) : "v"(sv[4]), "v"(epoch));
        const float s = ((v[0] + v[1]) + (v[2] + v[3])) + ((v[4] + v[5]) + (v[6] + v[7])) + (v[8] + v[9]);
        const float mean = wave_sum(s) * (1.0f / D_);
        float q0 = 0.f, q1 = 0.f;
#pragma unroll
        for (int j = 0; j < 5; ++j) {
            const float a = v[2 * j] - mean, b = v[2 * j + 1] - mean;
            q0 = __builtin_fmaf(a, a, q0);
            q1 = __builtin_fmaf(b, b, q1);
        }
        const float q = wave_sum(q0 + q1);
        const float den = __builtin_amdgcn_sqrtf(rb(rb(q * (1.0f / D_)) + 1e-5f));
        asm volatile("buffer_store_dwordx2 %10, %11, %12, %13 offen sc1\n\t"
                     "v_lshlrev_b32 %0, 16, %5\n\tv_lshlrev_b32 %1, 16, %6\n\tv_lshlrev_b32 %2, 16, %7\n\t"
                     "v_lshlrev_b32 %3, 16, %8\n\tv_lshlrev_b32 %4, 16, %9"
                     : "=&v"(pf[0]), "=&v"(pf[1]), "=&v"(pf[2]), "=&v"(pf[3]), "=&v"(pf[4])
                     : "v"(pp[0]), "v"(pp[1]), "v"(pp[2]), "v"(pp[3]), "v"(pp[4]),
                       "v"((u32x2){pk2(mean, den), etag * 0x85ebca6bu}), "v"(lane * (L_ * 8)), "s"(srdG), "s"(row * 8) : "memory");
    }
    TS(0);
    if (!conv) {
        const unsigned lds = (unsigned)(size_t)(LDS_AS char*)(smem + w * 1024);
        if (XT == 4 && idx < 28) touch4(srdX, (lane >> 1) * (D_ * 2) + (lane & 1) * 156, ((idx - 20) * 8 + w) * (32 * D_ * 2) + (grp / 20) * 160, lds);
        if ((CW & 2) && idx == 28 && w < 3) {
            __amdgpu_buffer_rsrc_t srdC;
            CODE_SRD(srdC, "kb_mlp");
            touch4(srdC, lane * 128, w * 8192, lds);
        }
        if ((CW & 4) && idx == 28 && w == 3) {
            unsigned lo, hi;
            asm("s_getpc_b64 vcc\n\ts_add_u32 %0, vcc_lo, kb_mlp.kd@rel32@lo+4\n\ts_addc_u32 %1, vcc_hi, kb_mlp.kd@rel32@hi+12"
                : "=s"(lo), "=s"(hi) :: "vcc", "scc");
            touch4(__builtin_amdgcn_make_buffer_rsrc((void*)(((unsigned long long)hi << 32) | lo), (short)0, 64, 0x00020000), lane * 4, 0, lds);
        }
        const int gw = BAL ? w * 12 + idx - 20 : (idx - 20) * 8 + w;
        warm_w<0>(w1, b1, xcd, gw, 96, lds, lane);
        if (W2M & 1) asm volatile("s_waitcnt vmcnt(0)" ::: "memory");
        fetch_w2(w2, W2C + (95 - gw) * 8 + xcd, W2N, 768, lds, lane);
        if ((CW & 8) && idx == 27 && w >= 5) {
            __amdgpu_buffer_rsrc_t srdC;
            CODE_SRD(srdC, "kb_mlp");
            touch4(srdC, lane * 128, (w - 5) * 8192, lds);
        }
        if (idx == 31 && tid < 256) {
            const int r = xcd * 8 + (tid >> 5), c = tid & 31;
            *(unsigned*)(ws + WS_ID + r * (L_ * 2) + 4 * c) = (r >> 1) == c ? ((r & 1) ? 0x3f800000u : 0x3f80u) : 0u;
        }
        if (idx == 30 && tid < 128) *(u32x4*)(ws + WS_G2 + (xcd * 128 + tid) * 16) = (u32x4){0u, 0u, 0u, 0u};
        if (idx == 29 && tid < 32) *(unsigned*)(ws + WS_FL + (xcd * 32 + tid) * 128) = 0u;
        asm volatile("s_waitcnt vmcnt(0)" ::: "memory");
        STAMP(0, 7);
        return;
    }
    const unsigned lds0 = (unsigned)(size_t)(LDS_AS char*)smem;
    const int m = lane & 15, hg = lane >> 4;
    char* mine = smem + (w & 3) * KA_LO;
    char* minh = smem + KA_HI0 + (w & 3) * KA_HI;
    const unsigned m0v[2] = {lds0 + (w >> 1) * KA_LO + (w & 1) * 256, lds0 + KA_MB + (w >> 1) * KA_HI + (w & 1) * 256};
    ka_chain(m0v, pf[0], pf[1], pf[2], pf[3], pf[4], [&](int i) {
        if (i % XCS == 0 && i / XCS < 4)
            asm volatile("s_cmp_eq_u32 %1, 0\n\ts_cbranch_scc1 .Lka_c%=\n\tbuffer_load_dwordx2 %0, %2, %3, %4 offen offset:%5\n.Lka_c%=:"
                         : "=v"(xc[4 + i / XCS])
                         : "s"(owner), "v"(t * (8 * D_ * 2)), "s"(srdX), "s"(d0 * 2 + 4 * D_ * 2), "n"((i / XCS) * (D_ * 2)) : "scc", "memory");
    });
    TS(1);
    __builtin_amdgcn_s_barrier();
    if (w < 4) {
        TS(2);
        u16* kd = (u16*)(minh + 8192);
        {
            const char* fb = mine + m * KA_ROWB + hg * 16;
            const char* fh = minh + m * KA_ROWB + hg * 16;
            u32x4 fa[8][3], fl[8];
#pragma unroll
            for (int ks = 0; ks < 8; ++ks) {
                fl[ks] = *(const u32x4*)(fh + 9 * KA_ROWB + ks * 64);
                fa[ks][0] = *(const u32x4*)(fb + ks * 64);
                fa[ks][1] = *(const u32x4*)(fb + 16 * KA_ROWB + ks * 64);
                fa[ks][2] = *(const u32x4*)(fh + ks * 64);
            }
            __builtin_amdgcn_sched_barrier(0);
            f32x4 acc0 = {0.f, 0.f, 0.f, 0.f}, acc1 = acc0, acc2 = acc0;
#pragma unroll
            for (int ks = 0; ks < 8; ++ks) {
                acc0 = mfma16(fa[ks][0], fl[ks], acc0);
                acc1 = mfma16(fa[ks][1], fl[ks], acc1);
                acc2 = mfma16(fa[ks][2], fl[ks], acc2);
            }
            const int c = m;
            *(u32x2*)(kd + 128 * (hg >> 1) + 4 * (hg & 1) + 8 * c) = (u32x2){pk2(acc0[0], acc0[1]), pk2(acc0[2], acc0[3])};
            const unsigned p01 = pk2(acc1[0], acc1[1]), p23 = pk2(acc1[2], acc1[3]);
            const int i0 = hg < 2 ? 256 + 64 * hg + 4 * c : 256 + 64 * hg + 2 * c;
            *(unsigned*)(kd + i0) = p01;
            *(unsigned*)(kd + i0 + (hg < 2 ? 2 : 32)) = p23;
            const unsigned q01 = pk2(acc2[0], acc2[1]), q23 = pk2(acc2[2], acc2[3]);
            kd[512 + 64 * hg + c] = (u16)(q01 & 0xffff);
            kd[528 + 64 * hg + c] = (u16)(q01 >> 16);
            kd[544 + 64 * hg + c] = (u16)(q23 & 0xffff);
            kd[560 + 64 * hg + c] = (u16)(q23 >> 16);
            asm volatile("" ::: "memory");
        }
        const unsigned* kw = (const unsigned*)kd;
        const unsigned dw0 = kw[319 - lane], dw1 = kw[320 - lane];
        const unsigned cw0 = kw[255 - lane], cw1 = kw[256 - lane];
        const bool isA = lane >= 32;
        const char* bp = (const char*)kd + (isA ? 16 * (63 - lane) : 256 + 8 * (63 - lane));
        const u32x2 b01 = *(const u32x2*)bp, b23 = *(const u32x2*)(bp + 8);
        const unsigned P = __builtin_amdgcn_perm(0, dw1, 0x01000100), Q = __builtin_amdgcn_perm(0, dw0, 0x03020302),
                       M = __builtin_amdgcn_perm(0, dw0, 0x01000302), Z = __builtin_amdgcn_perm(0, dw0, 0x01000100);
        const unsigned c0 = __builtin_amdgcn_perm(0, cw1, 0x01000100), c1 = __builtin_amdgcn_perm(0, cw0, 0x03020302),
                       c2 = __builtin_amdgcn_perm(0, cw0, 0x01000302), c3 = __builtin_amdgcn_perm(0, cw0, 0x01000100);
        const unsigned o0 = __builtin_amdgcn_perm(b23.x, isA ? b23.y : b01.y, isA ? 0x01000302u : 0x03020504u),
                       o1 = __builtin_amdgcn_perm(b23.x, b01.y, isA ? 0x05040706u : 0x01000100u),
                       o2 = __builtin_amdgcn_perm(b01.y, b01.x, isA ? 0x05040706u : 0x03020504u),
                       o3 = __builtin_amdgcn_perm(0, b01.x, isA ? 0x01000302u : 0x01000100u);
        put_copies(mine + 3072 + lane * 16, o0, o1, o2, o3, next_lane(o0, 0), next_lane(o1, 0));
        put_copies(mine + 2048 + lane * 16, c0, c1, c2, c3, next_lane(c0, __builtin_amdgcn_readlane(o0, 0)),
                   next_lane(c1, __builtin_amdgcn_readlane(o1, 0)));
        put_copies(mine + lane * 32, P, P, Q, Q, Q, M);
        put_copies(mine + lane * 32 + 16, Q, M, Z, Z, next_lane(P, __builtin_amdgcn_readlane(c0, 0)),
                   next_lane(P, __builtin_amdgcn_readlane(c1, 0)));
        if (lane < 8) *(u32x4*)(mine + (lane >> 1) * CV_COPY + 4096 + (lane & 1) * 16) = (u32x4){0u, 0u, 0u, 0u};
        asm volatile("s_waitcnt vmcnt(0)" ::: "memory");
        TS(5);
        __syncthreads();
        TS(6);
        const unsigned aa = (unsigned)(size_t)(LDS_AS char*)(mine + (3 - (m & 3)) * CV_COPY + 2 * (2028 - 32 * 63 - 4 * (m >> 2) + 8 * hg));
        const unsigned ba = (unsigned)(size_t)(LDS_AS char*)(smem + KA_U + w * CV_UB + (2 * m + hg) * 16);
        f32x4 acc[8];
#pragma unroll
        for (int q = 0; q < 8; ++q) acc[q] = (f32x4){0.f, 0.f, 0.f, 0.f};
        CvFrag f0, f1;
        cv_first<0>(f0, aa, ba);
        cv_round<0>(f0, f1, acc, aa, ba);
        TS(7);
        const char* rp = minh + (16 * m + 4 * hg) * 4;
        char* hp = mine + (16 * m + 4 * hg) * 2;
#pragma unroll
        for (int q = 0; q < 8; ++q) {
            const f32x4 h = acc[q] + *(const f32x4*)(rp + q * 1024);
            *(u32x2*)(hp + q * 512) = (u32x2){pk2(h[0], h[1]), pk2(h[2], h[3])};
        }
    } else {
        const unsigned tag = etag * 0x85ebca6bu;
        u32x4 g[4];
        asm volatile("buffer_load_dwordx4 %0, %4, %5, 0 offen" GFIRST "\n\tbuffer_load_dwordx4 %1, %4, %5, 0 offen offset:16" GFIRST "\n\t"
                     "buffer_load_dwordx4 %2, %4, %5, 0 offen offset:32" GFIRST "\n\tbuffer_load_dwordx4 %3, %4, %5, 0 offen offset:48" GFIRST "\n\t"
                     "s_waitcnt vmcnt(4)"
                     : "=&v"(g[0]), "=&v"(g[1]), "=&v"(g[2]), "=&v"(g[3]) : "v"(t * 64), "s"(srdG) : "memory");
        float xf[8][4], dk[4];
#pragma unroll
        for (int k = 0; k < 8; ++k) unpack4(xf[k], xc[k]);
        unpack4(dk, xd);
        if (lane < 30) *(u32x4*)(smem + KA_U + (w - 4) * CV_UB + lane * 16) = (u32x4){0u, 0u, 0u, 0u};
        TS(3);
        asm volatile("s_waitcnt vmcnt(0)" : "+v"(g[0]), "+v"(g[1]), "+v"(g[2]), "+v"(g[3]) :: "memory");
        unsigned spins = 0;
#pragma unroll 1
        for (; spins < (1u << 16); ++spins) {
            const bool bad = ((g[0].y ^ tag) | (g[0].w ^ tag) | (g[1].y ^ tag) | (g[1].w ^ tag) |
                              (g[2].y ^ tag) | (g[2].w ^ tag) | (g[3].y ^ tag) | (g[3].w ^ tag)) != 0;
            if (!__any(bad)) break;
            if (bad)
                asm volatile("buffer_load_dwordx4 %0, %4, %5, 0 offen sc1\n\tbuffer_load_dwordx4 %1, %4, %5, 0 offen offset:16 sc1\n\t"
                             "buffer_load_dwordx4 %2, %4, %5, 0 offen offset:32 sc1\n\tbuffer_load_dwordx4 %3, %4, %5, 0 offen offset:48 sc1\n\t"
                             "s_waitcnt vmcnt(0)"
                             : "+v"(g[0]), "+v"(g[1]), "+v"(g[2]), "+v"(g[3]) : "v"(t * 64), "s"(srdG) : "memory");
        }
        TS(4);
        unsigned up[4][4];
#pragma unroll
        for (int k = 0; k < 4; ++k) {
            const float i0 = __builtin_amdgcn_rcpf(bfhi(g[k].x)), i1 = __builtin_amdgcn_rcpf(bfhi(g[k].z));
            const float m0 = bflo(g[k].x), m1 = bflo(g[k].z);
#pragma unroll
            for (int c = 0; c < 4; ++c) up[c][k] = pk2((xf[2 * k][c] - m0) * i0, (xf[2 * k + 1][c] - m1) * i1);
        }
#pragma unroll
        for (int c = 0; c < 4; ++c)
            *(u32x4*)(smem + KA_U + c * CV_UB + 480 + t * 16) = (u32x4){up[c][0], up[c][1], up[c][2], up[c][3]};
        TS(5);
        __syncthreads();
        if (blockIdx.x == 0 && tid == 256)
            __hip_atomic_store((gu32*)(ws + WS_EP), (etag ^ 0x9e3779b9u) + 1u, __ATOMIC_RELAXED, __HIP_MEMORY_SCOPE_AGENT);
#pragma unroll
        for (int c = 0; c < 4; ++c) {
            f32x4 r0, r1;
#pragma unroll
            for (int k = 0; k < 2; ++k) {
                r0[2 * k] = __builtin_fmaf(bflo(up[c][k]), dk[c], xf[2 * k][c]);
                r0[2 * k + 1] = __builtin_fmaf(bfhi(up[c][k]), dk[c], xf[2 * k + 1][c]);
                r1[2 * k] = __builtin_fmaf(bflo(up[c][2 + k]), dk[c], xf[4 + 2 * k][c]);
                r1[2 * k + 1] = __builtin_fmaf(bfhi(up[c][2 + k]), dk[c], xf[5 + 2 * k][c]);
            }
            *(f32x4*)(smem + KA_HI0 + c * KA_HI + t * 32) = r0;
            *(f32x4*)(smem + KA_HI0 + c * KA_HI + t * 32 + 16) = r1;
        }
        fetch_w2(w2, (w - 4) * 160 + blockIdx.x, W2C, 640, lds0 + KA_U + 4 * CV_UB, lane);
        asm volatile("s_waitcnt lgkmcnt(0)" ::: "memory");
        __builtin_amdgcn_s_barrier();
        asm volatile("s_waitcnt vmcnt(0)" ::: "memory");
    }
    __syncthreads();
    {
        const int ch = tid & 3;
        const int dt = grp / 20, rem = grp % 20, mq = rem / 5, j = rem % 5;
#pragma unroll
        for (int i = 0; i < 2; ++i) {
            const int piece = (tid >> 2) + 128 * i;
            const int unit = ((piece >> 2) * 40 + dt * 5 + j) * 64 + (piece & 3) * 16 + 4 * mq + ch;
            *(u32x4*)(ws + WS_HB + (size_t)unit * 16) = *(const u32x4*)(smem + ch * KA_LO + piece * 16);
        }
    }
    STAMP(0, 6);
    TSPUT(1, 0);
    TSPUT(5, 448);
    asm volatile(".Lka_conv_last:");
}

constexpr int B_REG = 40960;
constexpr int B_RED = 24576;
constexpr int B_TAB = 32768;
#define PPOS 5
#define NP1 6
#define NPRO 8
#define WL 6
#define PK23 0
#define TQ1 4
#define TQ2 9
#define RSTEPS 10
#define PLATE 2
#define EPW 2
#define G2D 4
#define PRE 2

__device__ __forceinline__ float gelu_tanh(float x) {
    const float p = __builtin_fmaf(x * x, -2.302208198f * 0.044715f, -2.302208198f);
    const float e = __builtin_amdgcn_exp2f(p * x);
    return x * __builtin_amdgcn_rcpf(1.0f + e);
}
template <int I>
__device__ __forceinline__ void w_piece(__amdgpu_buffer_rsrc_t srd, unsigned v, unsigned so, unsigned m0v) {
    if (I == 0) asm volatile("s_mov_b32 m0, %0\n\ts_nop 0\n\tbuffer_load_dwordx4 %1, %2, %3 offen lds"
                             :: "s"(m0v), "v"(v), "s"(srd), "s"(so) : "m0", "memory");
    else asm volatile("buffer_load_dwordx4 %0, %1, %2 offen offset:%3 lds"
                      :: "v"(v), "s"(srd), "s"(so), "n"(I * 1024) : "memory");
}
__device__ __forceinline__ void w_issue(__amdgpu_buffer_rsrc_t srd, const unsigned (&v)[4], unsigned so, unsigned m0v) {
    m0v = __builtin_amdgcn_readfirstlane(m0v);
    asm volatile("s_mov_b32 m0, %0\n\ts_nop 0\n\t"
                 "buffer_load_dwordx4 %1, %5, %6 offen lds\n\tbuffer_load_dwordx4 %2, %5, %6 offen offset:1024 lds\n\t"
                 "buffer_load_dwordx4 %3, %5, %6 offen offset:2048 lds\n\tbuffer_load_dwordx4 %4, %5, %6 offen offset:3072 lds"
                 :: "s"(m0v), "v"(v[0]), "v"(v[1]), "v"(v[2]), "v"(v[3]), "s"(srd), "s"(so) : "m0", "memory");
}
template <int SC, int SL, int J>
__device__ __forceinline__ void a_piece(__amdgpu_buffer_rsrc_t srd, unsigned av0, unsigned av1, unsigned so) {
    if (SC) asm volatile("buffer_load_dwordx4 a[%c0:%c1], %2, %3, %4 offen offset:%5 sc1"
                         :: "i"(SL * 20 + J * 4), "i"(SL * 20 + J * 4 + 3), "v"(J < 4 ? av0 : av1), "s"(srd), "s"(so), "n"((J & 3) * 1024) : "memory");
    else asm volatile("buffer_load_dwordx4 a[%c0:%c1], %2, %3, %4 offen offset:%5"
                      :: "i"(SL * 20 + J * 4), "i"(SL * 20 + J * 4 + 3), "v"(J < 4 ? av0 : av1), "s"(srd), "s"(so), "n"((J & 3) * 1024) : "memory");
}
template <int SC, int SL>
__device__ __forceinline__ void a_issue(__amdgpu_buffer_rsrc_t srd, unsigned av0, unsigned av1, unsigned so) {
    a_piece<SC, SL, 0>(srd, av0, av1, so); a_piece<SC, SL, 1>(srd, av0, av1, so); a_piece<SC, SL, 2>(srd, av0, av1, so);
    a_piece<SC, SL, 3>(srd, av0, av1, so); a_piece<SC, SL, 4>(srd, av0, av1, so);
}
template <int N> __device__ __forceinline__ void vm_wait() { asm volatile("s_waitcnt vmcnt(%0)" :: "n"(N) : "memory"); }
template <int R>
__device__ __forceinline__ void affine4(f32x4& d, u32x2 al, f32x4 be) {
    asm volatile("v_mfma_f32_4x4x4_16b_bf16 %0, %1, a[%c2:%c3], %4" : "=&v"(d) : "v"(al), "i"(R), "i"(R + 1), "v"(be));
}
template <int INIT>
__device__ __forceinline__ void mma_vv(f32x4& c, u32x4 a, u32x4 b, f32x4 c0) {
    if (INIT == 0) asm volatile("v_mfma_f32_16x16x32_bf16 %0, %1, %2, %0" : "+v"(c) : "v"(a), "v"(b));
    if (INIT == 1) asm volatile("v_mfma_f32_16x16x32_bf16 %0, %1, %2, 0" : "=&v"(c) : "v"(a), "v"(b));
    if (INIT == 2) asm volatile("v_mfma_f32_16x16x32_bf16 %0, %1, %2, %3" : "=&v"(c) : "v"(a), "v"(b), "v"(c0));
}
template <int INIT, int R>
__device__ __forceinline__ void mma_av(f32x4& c, u32x4 b, f32x4 c0) {
    if (INIT == 0) asm volatile("v_mfma_f32_16x16x32_bf16 %0, a[%c1:%c2], %3, %0" : "+v"(c) : "i"(R), "i"(R + 3), "v"(b));
    if (INIT == 1) asm volatile("v_mfma_f32_16x16x32_bf16 %0, a[%c1:%c2], %3, 0" : "=&v"(c) : "i"(R), "i"(R + 3), "v"(b));
    if (INIT == 2) asm volatile("v_mfma_f32_16x16x32_bf16 %0, a[%c1:%c2], %3, %4" : "=&v"(c) : "i"(R), "i"(R + 3), "v"(b), "v"(c0));
}
template <int INIT, int R, int Q>
__device__ __forceinline__ void mma_aa(f32x4& c, f32x4 c0) {
    if (INIT == 0) asm volatile("v_mfma_f32_16x16x32_bf16 %0, a[%c1:%c2], a[%c3:%c4], %0" : "+v"(c) : "i"(R), "i"(R + 3), "i"(Q), "i"(Q + 3));
    if (INIT == 1) asm volatile("v_mfma_f32_16x16x32_bf16 %0, a[%c1:%c2], a[%c3:%c4], 0" : "=&v"(c) : "i"(R), "i"(R + 3), "i"(Q), "i"(Q + 3));
    if (INIT == 2) asm volatile("v_mfma_f32_16x16x32_bf16 %0, a[%c1:%c2], a[%c3:%c4], %5" : "=&v"(c) : "i"(R), "i"(R + 3), "i"(Q), "i"(Q + 3), "v"(c0));
}
#define SB() __builtin_amdgcn_sched_barrier(0)
struct Nrm { u32x2 a0, a1; f32x4 b0, b1; };
struct KbCtx {
    __amdgpu_buffer_rsrc_t srdW1, srdW2, srdL, srdA, srdH;
    unsigned wv1[4], wv2[4], av0, av1, ring, w1o, a1o, w2o, a2o, wlo[2], alo[2];
    const char* tp[4];
    unsigned tpa[4];
    const char* ta;
    const char* tb;
    char* red;
    f32x4 bias;
};
__device__ __forceinline__ unsigned w1_off(const KbCtx& c, int g) { return c.w1o + g * (32 * F_ * 2); }
__device__ __forceinline__ unsigned a1_off(const KbCtx& c, int g) { return c.a1o + g * 40960; }
__device__ __forceinline__ unsigned w2_off(const KbCtx& c, int g) { return g >= 12 ? c.wlo[g & 1] : c.w2o + g * (32 * L_ * 2); }
__device__ __forceinline__ unsigned a2_off(const KbCtx& c, int g) { return g >= 12 ? c.alo[g & 1] : c.a2o + g * 40960; }
template <int R>
__device__ __forceinline__ void w_read(u32x4 (&wf)[4], const KbCtx& c, int SL) {
    const u32x2 v = tr_read(c.tp[R >> 1] + SL * 4096 + (R & 1) * 512);
    if (R & 1) { wf[R >> 1].z = v.x; wf[R >> 1].w = v.y; } else { wf[R >> 1].x = v.x; wf[R >> 1].y = v.y; }
}
template <int I>
__device__ __forceinline__ void nrm_read(Nrm& t, const KbCtx& c, int S) {
    if (I == 0) { t.a0 = *(const u32x2*)(c.ta + S * 256); t.a1 = *(const u32x2*)(c.ta + S * 256 + 32); }
    if (I == 1) t.b0 = *(const f32x4*)(c.tb + S * 128);
    if (I == 2) t.b1 = *(const f32x4*)(c.tb + S * 128 + 16);
}
template <int T>
__device__ __forceinline__ void red_put(const f32x4 (&acc)[4][5], const KbCtx& c) {
    if constexpr (T >= 8) *(f32x4*)(c.red + (T - 8) * 1024) = acc[(T - 8) / 5 + 1][(T - 8) % 5];
    if constexpr (T == 19) {
        SB();
#pragma unroll
        for (int j = 2; j < 5; ++j) *(f32x4*)(c.red + (10 + j) * 1024) = acc[3][j];
    }
}
constexpr int G1Q = (16 - NPRO) * 9;
constexpr int g1_cum(int s) {
    return s <= RSTEPS ? (G1Q * s + RSTEPS - 1) / RSTEPS : G1Q + (s >= 15 ? 4 * WL : 4 * WL * (s - RSTEPS) / (15 - RSTEPS));
}
template <int S, int P>
__device__ __forceinline__ void g1_req(const KbCtx& c) {
    constexpr int q = g1_cum(S) + P;
    static_assert(g1_cum(S + 1) - g1_cum(S) <= 9, "");
    if constexpr (q < g1_cum(S + 1)) {
        if constexpr (q < G1Q) {
            constexpr int g = NPRO + q / 9, r = q % 9;
            static_assert(g - 8 <= S && g >= S + 2, "");
            if constexpr (r < 4) w_piece<r>(c.srdW1, c.wv1[r], w1_off(c, g), c.ring + (g & 7) * 4096);
            else a_piece<0, g & 7, r - 4>(c.srdH, c.av0, c.av1, a1_off(c, g));
        } else {
            constexpr int g = (q - G1Q) / 4, r = (q - G1Q) % 4;
            static_assert(g + 8 <= S, "");
            w_piece<r>(c.srdW2, c.wv2[r], w2_off(c, g), c.ring + g * 4096);
        }
    }
}
template <int SL, int R>
__device__ __forceinline__ void w_park(const KbCtx& c) {
    asm volatile("ds_read_b64_tr_b16 a[%c0:%c1], %2 offset:%3"
                 :: "i"(160 + SL * 16 + R * 2), "i"(161 + SL * 16 + R * 2), "v"(c.tpa[R >> 1]), "n"(SL * 4096 + (R & 1) * 512) : "memory");
}
template <int SL> __device__ __forceinline__ void w_park8(const KbCtx& c) {
    w_park<SL, 0>(c); w_park<SL, 1>(c); w_park<SL, 2>(c); w_park<SL, 3>(c); w_park<SL, 4>(c); w_park<SL, 5>(c); w_park<SL, 6>(c); w_park<SL, 7>(c);
}
template <int S, int T>
__device__ __forceinline__ void g1_mma(f32x4 (&acc)[4][5], u32x4 (&n)[2][5], u32x4 (&wf)[2][4], const KbCtx& c) {
    mma_vv<S ? 0 : (T & 3) ? 1 : 2>(acc[T & 3][T >> 2], wf[S & 1][T & 3], n[S & 1][T >> 2], c.bias);
    SB();
}
constexpr int g1_aux(int t) {
    constexpr int a[20] = {0, 1, 2, 3, 4, -1, 5, -2, 6, -3, 7, -4, 8, 9, 10, -5, -6, -7, -8, -9};
    return a[t];
}
template <int S, int T>
__device__ __forceinline__ void g1_body(f32x4 (&acc)[4][5], u32x4 (&n)[2][5], u32x4 (&wf)[2][4], f32x4 (&d)[10], Nrm& t, const KbCtx& c) {
    g1_mma<S, T>(acc, n, wf, c);
    n[(S + 1) & 1][T >> 2][T & 3] = pk2(d[T >> 1][(T & 1) * 2], d[T >> 1][(T & 1) * 2 + 1]);
    constexpr int a = g1_aux(T);
    if constexpr (a < 0) g1_req<S, -1 - a>(c);
    if constexpr (a >= 3) w_read<a - 3>(wf[(S + 1) & 1], c, (S + 1) & 7);
    if constexpr (a >= 0 && a < 3 && S < 14) nrm_read<a>(t, c, S + 2);
    SB();
    if constexpr (T < 19) g1_body<S, T + 1>(acc, n, wf, d, t, c);
}
template <int S, int T>
__device__ __forceinline__ void g1_mid(f32x4 (&d)[10], const Nrm& t) {
    affine4<(S & 7) * 20 + T * 2>(d[T], (T & 1) ? t.a1 : t.a0, (T & 1) ? t.b1 : t.b0);
    SB();
    if constexpr (T < 9) g1_mid<S, T + 1>(d, t);
}
template <int T>
__device__ __forceinline__ void g1_last(f32x4 (&acc)[4][5], u32x4 (&n)[5], u32x4 (&wf)[4], const KbCtx& c) {
    constexpr int i = (T / 5 + 1) & 3, j = T % 5;
    mma_vv<0>(acc[i][j], wf[i], n[j], c.bias);
    SB();
    if constexpr (T < 8) { w_park<T / 4, (T & 3) * 2>(c); w_park<T / 4, (T & 3) * 2 + 1>(c); }
    red_put<T>(acc, c);
    constexpr int q0 = (24 - 4 * WL) * T / 20, q1 = (24 - 4 * WL) * (T + 1) / 20;
    if constexpr (q1 > q0) w_piece<q0 & 3>(c.srdW2, c.wv2[q0 & 3], w2_off(c, WL + q0 / 4), c.ring + (WL + q0 / 4) * 4096);
    SB();
    if constexpr (T < 19) g1_last<T + 1>(acc, n, wf, c);
}
template <int N> __device__ __forceinline__ void all_wait() {
    asm volatile("" ::: "memory");
    __builtin_amdgcn_s_waitcnt((N & 15) | 0x70 | ((N >> 4) << 14));
    asm volatile("" ::: "memory");
    SB();
}
template <int S>
__device__ __forceinline__ void g1_step(f32x4 (&acc)[4][5], u32x4 (&n)[2][5], u32x4 (&wf)[2][4], Nrm& t, const KbCtx& c) {
    if constexpr (S == 15) {
        all_wait<4 * WL - 8>();
        g1_last<0>(acc, n[1], wf[1], c);
    } else {
        constexpr int young = NPRO * 9 + g1_cum(S) - (S + 2) * 9;
        static_assert(young >= 0 && young + g1_cum(S + 1) - g1_cum(S) <= 63, "");
        all_wait<young>();
        f32x4 d[10];
        g1_mid<S + 1, 0>(d, t);
        g1_body<S, 0>(acc, n, wf, d, t, c);
        g1_step<S + 1>(acc, n, wf, t, c);
    }
}
constexpr int g2_slot(int s) { return s < 6 ? s : s < 10 ? s - 6 : s - 4; }
template <int S, int P>
__device__ __forceinline__ void g2_req(const KbCtx& c) {
    if constexpr (S < 14) a_piece<1, S & 7, P>(c.srdA, c.av0, c.av1, a2_off(c, S));
}
template <int S, int T>
__device__ __forceinline__ void g2_body(f32x4 (&acc)[4][5], u32x4 (&wf)[2][4], const KbCtx& c) {
    constexpr int i = (T / 5 + 1) & 3, j = T % 5;
    if constexpr (S < 4) mma_aa<S ? 0 : i ? 1 : 2, (S & 7) * 20 + j * 4, 160 + S * 16 + i * 4>(acc[i][j], c.bias);
    else mma_av<0, (S & 7) * 20 + j * 4>(acc[i][j], wf[S & 1][i], c.bias);
    SB();
    if constexpr (S == 13) red_put<T>(acc, c);
    else {
        if constexpr (T % 4 == 0) g2_req<S + G2D, T / 4>(c);
        else if constexpr (S >= 3 && T > 5 && T < 17) w_read<(T - T / 4 - 3) & 7>(wf[(S + 1) & 1], c, g2_slot(S + 1));
    }
    SB();
    if constexpr (T < 19) g2_body<S, T + 1>(acc, wf, c);
}
template <int S>
__device__ __forceinline__ void g2_step(f32x4 (&acc)[4][5], u32x4 (&wf)[2][4], const KbCtx& c) {
    vm_wait<5 * (S + G2D > 14 ? 13 - S : G2D - 1)>();
    if (S == 0) STAMPX(4, 1);
    SB();
    g2_body<S, 0>(acc, wf, c);
    if constexpr (S < 13) g2_step<S + 1>(acc, wf, c);
}
__device__ __forceinline__ void kb_reduce(f32x4 (&r)[5], const f32x4 (&acc)[4][5], char* smem, int w, int lane) {
    asm volatile("s_waitcnt lgkmcnt(0)\n\ts_barrier" ::: "memory");
    f32x4 p[5][3];
#pragma unroll
    for (int j = 0; j < 5; ++j)
#pragma unroll
        for (int i = 1; i < 4; ++i)
            p[j][i - 1] = *(const f32x4*)(smem + ((w - i) & 3) * B_REG + B_RED + lane * 16 + (i - 1) * 5120 + j * 1024);
    SB();
#pragma unroll
    for (int j = 0; j < 5; ++j) r[j] = (acc[0][j] + p[j][0]) + (p[j][1] + p[j][2]);
}
template <int S0, int S1>
__device__ __forceinline__ void g1_groups(const KbCtx& c) {
    if constexpr (S0 < S1) {
        if (S0 >= 3) vm_wait<54>();
        w_issue(c.srdW1, c.wv1, w1_off(c, S0), c.ring + S0 * 4096);
        a_issue<0, S0>(c.srdH, c.av0, c.av1, a1_off(c, S0));
        g1_groups<S0 + 1, S1>(c);
    }
}
template <int Q0, int Q1>
__device__ __forceinline__ void g1_tail(const KbCtx& c) {
    if constexpr (Q0 < Q1 && Q0 < (NPRO - NP1) * 9) {
        constexpr int g = NP1 + Q0 / 9, r = Q0 % 9;
        if constexpr (r < 4) w_piece<r>(c.srdW1, c.wv1[r], w1_off(c, g), c.ring + g * 4096);
        else a_piece<0, g, r - 4>(c.srdH, c.av0, c.av1, a1_off(c, g));
        g1_tail<Q0 + 1, Q1>(c);
    }
    SB();
}
template <int T>
__device__ __forceinline__ void g1_mid0(f32x4 (&d)[10], const Nrm& t, const KbCtx& c) {
    affine4<T * 2>(d[T], (T & 1) ? t.a1 : t.a0, (T & 1) ? t.b1 : t.b0);
    SB();
    if constexpr (T & 1) g1_tail<TQ2 + T / 2, TQ2 + T / 2 + 1>(c);
    if constexpr (T < 9) g1_mid0<T + 1>(d, t, c);
}
template <int S>
__device__ __forceinline__ void g2_first(const KbCtx& c) {
    a_issue<1, S>(c.srdA, c.av0, c.av1, a2_off(c, S));
    if constexpr (S < G2D - 1) g2_first<S + 1>(c);
}
template <int CTRL> __device__ __forceinline__ f32x2 dpp_add2(f32x2 v) { return (f32x2){dpp_add<CTRL>(v.x), dpp_add<CTRL>(v.y)}; }

extern "C" __global__ __launch_bounds__(256, 1) void kb_mlp(const u16* __restrict__ w1, const u16* __restrict__ b1, const u16* __restrict__ w2,
                                                 const u16* __restrict__ b2, char* ws, u16* __restrict__ out) {
    __shared__ __attribute__((aligned(16))) char smem[4 * B_REG];
    const int tid = threadIdx.x, lane = tid & 63;
    const int w = __builtin_amdgcn_readfirstlane(tid >> 6);
    STAMP(2, 0);
    const int n = lane & 15, hg = lane >> 4;
    KbCtx c;
    c.srdA = __builtin_amdgcn_make_buffer_rsrc((void*)ws, (short)0, WS_END, 0x00020000);
    u32x4 hs[3];
    {
        const unsigned v = hg * 1024 + n * 16;
        asm volatile("buffer_load_dwordx4 %0, %3, %5, %6 offen\n\tbuffer_load_dwordx4 %1, %3, %5, %7 offen\n\tbuffer_load_dwordx4 %2, %4, %5, %7 offen"
                     : "=&v"(hs[0]), "=&v"(hs[1]), "=&v"(hs[2])
                     : "v"(v), "v"(v + 4096 + (lane >> 5) * WS_END), "s"(c.srdA),
                       "s"(WS_HB + ((blockIdx.x >> 2) * 40 + 10 * w) * 1024 + (blockIdx.x & 3) * 256),
                       "s"(WS_HB + ((blockIdx.x >> 2) * 40 + 10 * w + 4) * 1024 + (blockIdx.x & 3) * 256) : "memory");
    }
    const int xcd = blockIdx.x & 7, idx = blockIdx.x >> 3;
    const bool g1 = idx < 27;
    const int t1 = (xcd >> 1) * 27 + (g1 ? idx : 0), t2 = (xcd >> 1) * 32 + idx;
    const int mt1 = t1 >> 2, dt1 = (xcd & 1) * 4 + (t1 & 3), mt2 = mt2_of(xcd >> 1, idx >> 2), dt2 = (xcd & 1) * 4 + (t2 & 3);
    char* ring = smem + w * B_REG;
    c.ring = (unsigned)(size_t)(LDS_AS char*)ring;
    c.srdW1 = __builtin_amdgcn_make_buffer_rsrc((void*)w1, (short)0, g1 ? L_ * F_ * 2 : 0, 0x00020000);
    c.srdH = __builtin_amdgcn_make_buffer_rsrc((void*)ws, (short)0, g1 ? WS_END : 0, 0x00020000);
    c.srdW2 = __builtin_amdgcn_make_buffer_rsrc((void*)w2, (short)0, L_ * F_ * 2, 0x00020000);
    const bool idw = w == 3;
    c.srdL = __builtin_amdgcn_make_buffer_rsrc((void*)(idw ? ws + WS_ID : (const char*)w2), (short)0, L_ * F_ * 2, 0x00020000);
    {
        const int wr = lane >> 3, wslot = lane & 7;
        const unsigned s0 = ((((wslot >> 1) ^ ((wr >> 1) & 1)) * 2 + (wslot & 1)) * 8) * 2;
        const unsigned s1 = ((((wslot >> 1) ^ (((wr >> 1) & 1) | 2)) * 2 + (wslot & 1)) * 8) * 2;
        c.wv1[0] = wr * F_ * 2 + s0; c.wv1[1] = (wr + 8) * F_ * 2 + s1 - 1024;
        c.wv1[2] = c.wv1[0] + 16 * F_ * 2 - 2048; c.wv1[3] = c.wv1[1] + 16 * F_ * 2 - 2048;
        c.wv2[0] = wr * L_ * 2 + s0; c.wv2[1] = (wr + 8) * L_ * 2 + s1 - 1024;
        c.wv2[2] = c.wv2[0] + 16 * L_ * 2 - 2048; c.wv2[3] = c.wv2[1] + 16 * L_ * 2 - 2048;
    }
    c.av0 = lane * 16; c.av1 = lane * 16 + 4096;
    c.w1o = (w * 16 * 32 * F_ + mt1 * 64) * 2;
    c.a1o = WS_HB + (w * 16 * 40 + dt1 * 5) * 1024;
    c.w2o = (w * 14 * 32 * L_ + mt2 * 64) * 2;
    c.a2o = WS_GB + (w * 14 * 40 + dt2 * 5) * 1024;
#pragma unroll
    for (int i = 0; i < 2; ++i) {
        c.wlo[i] = idw ? i * (32 * L_ * 2) : c.w2o + (12 + i) * (32 * L_ * 2);
        c.alo[i] = idw ? WS_HB + ((mt2 * 2 + i) * 40 + dt2 * 5) * 1024 : c.a2o + (12 + i) * 40960;
    }
    u32x2 bb;
    asm volatile("global_load_dwordx2 %0, %1, %2" : "=v"(bb) : "v"(hg * 8), "s"(b1 + mt1 * 64 + 16 * w) : "memory");
    asm volatile("" ::: "a0", "a223");
    g1_groups<0, PRE>(c);
    {
        const int q4 = n >> 2, p4 = n & 3;
        const int fsw = ((q4 >> 1) & 1) | ((hg & 1) << 1);
        const char* tb = ring + (8 * hg + q4) * 128 + p4 * 8;
#pragma unroll
        for (int i = 0; i < 4; ++i) {
            c.tp[i] = tb + ((((i + w) & 3) ^ fsw) * 32);
            c.tpa[i] = (unsigned)(size_t)(LDS_AS const char*)c.tp[i];
        }
        c.ta = ring + B_TAB + (8 * hg + (lane & 3)) * 8;
        c.tb = ring + B_TAB + 4096 + hg * 32;
        c.red = ring + B_RED + lane * 16;
    }
    const __amdgpu_buffer_rsrc_t srdG = __builtin_amdgcn_make_buffer_rsrc((void*)(ws + WS_G2), (short)0, L_ * 8, 0x00020000);
    {
        const unsigned one = (n & 1) ? 0x3f800000u : 0x3f80u;
        const int dw = (hg & 1) == (n >> 3) ? (n >> 1) & 3 : 4;
        const u32x4 sel = {dw == 0 ? one : 0u, dw == 1 ? one : 0u, dw == 2 ? one : 0u, dw == 3 ? one : 0u};
        const u32x4 z4 = {0u, 0u, 0u, 0u};
        const u32x4 selA = hg < 2 ? sel : z4, selB = hg < 2 ? z4 : sel;
        f32x4 sum, x0, x1, x2, x3, x4;
        asm volatile("s_waitcnt vmcnt(%12)\n\t"
                     "v_mfma_f32_16x16x32_bf16 %1, %6, %9, 0\n\tv_mfma_f32_16x16x32_bf16 %2, %6, %10, 0\n\t"
                     "v_mfma_f32_16x16x32_bf16 %3, %7, %9, 0\n\tv_mfma_f32_16x16x32_bf16 %4, %7, %10, 0\n\t"
                     "v_mfma_f32_16x16x32_bf16 %5, %8, %9, 0\n\tv_mfma_f32_16x16x32_bf16 %0, %6, %11, 0\n\t"
                     "v_mfma_f32_16x16x32_bf16 %0, %7, %11, %0\n\tv_mfma_f32_16x16x32_bf16 %0, %8, %11, %0\n\ts_nop 4"
                     : "=&v"(sum), "=&v"(x0), "=&v"(x1), "=&v"(x2), "=&v"(x3), "=&v"(x4)
                     : "v"(hs[0]), "v"(hs[1]), "v"(hs[2]), "v"(selA), "v"(selB), "v"(sel), "n"(9 * PRE + 1) : "memory");
        STAMPX(2, 1);
        f32x4 q = (x0 * x0 + x1 * x1) + (x2 * x2 + x3 * x3) + x4 * x4;
        asm volatile("s_nop 2" : "+v"(sum), "+v"(q));
        char* sc = smem + B_TAB + 6144;
        *(f32x2*)(sc + (n & 7) * 256 + (w * 8 + hg * 2 + (n >> 3)) * 8) = (f32x2){(sum[0] + sum[1]) + (sum[2] + sum[3]), (q[0] + q[1]) + (q[2] + q[3])};
        asm volatile("s_waitcnt lgkmcnt(0)\n\ts_barrier" ::: "memory");
        if (w == 0) {
            const f32x4 p0 = *(const f32x4*)(sc + lane * 32), p1 = *(const f32x4*)(sc + lane * 32 + 16);
            f32x2 v = {(p0[0] + p0[2]) + (p1[0] + p1[2]), (p0[1] + p0[3]) + (p1[1] + p1[3])};
            v = dpp_add2<0xb1>(v);
            v = dpp_add2<0x4e>(v);
            v = dpp_add2<0x141>(v);
            const float mean = v.x * (1.0f / D_);
            const float var = fmaxf(__builtin_fmaf(-mean, mean, v.y * (1.0f / D_)), 0.f);
            const float m1 = rb(mean), d1 = rb(__builtin_amdgcn_sqrtf(rb(rb(var) + 1e-5f)));
            const float i1 = __builtin_amdgcn_rcpf(d1);
            const float m2 = rb((mean - m1) * i1), d2 = rb(__builtin_amdgcn_sqrtf(rb(rb(var * i1 * i1) + 1e-5f)));
            const float al = rb(i1 * __builtin_amdgcn_rcpf(d2));
            const float be = -__builtin_fmaf(m2, d1, m1) * al;
            asm volatile("buffer_store_dwordx2 %0, %1, %2, %3 offen sc1"
                         :: "v"((u32x2){__float_as_uint(be), __float_as_uint(al) | 1u}), "v"((lane & 7) * (L_ * 8) + lane), "s"(srdG), "s"(blockIdx.x * 64) : "memory");
            STAMP(2, 2);
        }
    }
    f32x4 acc[4][5];
    u32x4 wf[2][4];
    unsigned f, bq;
    const unsigned fo = (lane < (w == 3 ? 6 : 7) ? lane : 0) * 128;
    const char* fp = ws + WS_FL + (dt2 * 32 + 7 * w) * 128;
    if (g1) {
        g1_groups<PRE, PPOS>(c);
        u32x4 g[4];
        const unsigned gvo = lane * 64;
        asm volatile("s_waitcnt vmcnt(59)\n\t"
                     "buffer_load_dwordx4 %0, %4, %5, %6 offen sc1\n\tbuffer_load_dwordx4 %1, %4, %5, %6 offen offset:16 sc1\n\t"
                     "buffer_load_dwordx4 %2, %4, %5, %6 offen offset:32 sc1\n\tbuffer_load_dwordx4 %3, %4, %5, %6 offen offset:48 sc1"
                     : "=&v"(g[0]), "=&v"(g[1]), "=&v"(g[2]), "=&v"(g[3]) : "v"(gvo), "s"(srdG), "s"(w * 4096) : "memory");
        g1_groups<PPOS, NP1>(c);
        STAMPX(2, 3);
        asm volatile("s_waitcnt vmcnt(%5)" : "+v"(g[0]), "+v"(g[1]), "+v"(g[2]), "+v"(g[3]), "+v"(bb) : "n"(9 * (NP1 - PPOS)) : "memory");
        STAMPX(2, 4);
        unsigned spins = 0;
#pragma unroll 1
        for (; spins < (1u << 16); ++spins) {
            if (__all(g[0].y & g[0].w & g[1].y & g[1].w & g[2].y & g[2].w & g[3].y & g[3].w & 1u)) break;
            asm volatile("buffer_load_dwordx4 %0, %4, %5, %6 offen sc1\n\tbuffer_load_dwordx4 %1, %4, %5, %6 offen offset:16 sc1\n\t"
                         "buffer_load_dwordx4 %2, %4, %5, %6 offen offset:32 sc1\n\tbuffer_load_dwordx4 %3, %4, %5, %6 offen offset:48 sc1\n\t"
                         "s_waitcnt vmcnt(0)"
                         : "=&v"(g[0]), "=&v"(g[1]), "=&v"(g[2]), "=&v"(g[3]) : "v"(gvo), "s"(srdG), "s"(w * 4096) : "memory");
        }
        STAMPX(2, 5);
        {
            char* tw = ring + B_TAB + lane * 64;
#pragma unroll
            for (int k = 0; k < 4; ++k) {
                const unsigned lo = g[k].y >> 16, hi = g[k].w & 0xffff0000u;
                *(u32x4*)(tw + k * 16) = (k & 1) ? (u32x4){0u, lo, 0u, hi} : (u32x4){lo, 0u, hi, 0u};
            }
            *(u32x4*)(ring + B_TAB + 4096 + lane * 32) = (u32x4){g[0].x, g[0].z, g[1].x, g[1].z};
            *(u32x4*)(ring + B_TAB + 4096 + lane * 32 + 16) = (u32x4){g[2].x, g[2].z, g[3].x, g[3].z};
            asm volatile("" ::: "memory");
        }
        Nrm t;
        u32x4 nf[2][5];
        nrm_read<0>(t, c, 0); nrm_read<1>(t, c, 0); nrm_read<2>(t, c, 0);
        SB();
        g1_tail<0, TQ1>(c);
        w_read<0>(wf[0], c, 0); w_read<1>(wf[0], c, 0); w_read<2>(wf[0], c, 0); w_read<3>(wf[0], c, 0);
        w_read<4>(wf[0], c, 0); w_read<5>(wf[0], c, 0); w_read<6>(wf[0], c, 0); w_read<7>(wf[0], c, 0);
        SB();
        g1_tail<TQ1, TQ2>(c);
        {
            c.bias = (f32x4){bflo(bb.x), bfhi(bb.x), bflo(bb.y), bfhi(bb.y)};
            asm volatile("" : "+v"(c.bias));
            f32x4 d[10];
            SB();
            g1_mid0<0>(d, t, c);
            nrm_read<0>(t, c, 1); nrm_read<1>(t, c, 1); nrm_read<2>(t, c, 1);
            asm volatile("s_nop 4");
            SB();
#pragma unroll
            for (int k = 0; k < 20; ++k) nf[0][k >> 2][k & 3] = pk2(d[k >> 1][(k & 1) * 2], d[k >> 1][(k & 1) * 2 + 1]);
            SB();
            g1_tail<TQ2 + 5, 27>(c);
        }
        STAMP(3, 1);
        g1_step<0>(acc, nf, wf, t, c);
        STAMP(3, 2);
        f32x4 r[5];
        kb_reduce(r, acc, smem, w, lane);
        STAMPX(3, 3);
        const unsigned go = WS_GB + ((mt1 * 2 + (w >> 1)) * 40 + dt1 * 5) * 1024;
        const unsigned gv = (((w & 1) * 2 + (hg >> 1)) * 16 + n) * 16 + (hg & 1) * 8;
        if (!PK23) {
            vm_wait<8>();
            w_park8<2>(c);
            w_park8<3>(c);
        }
#pragma unroll
        for (int j = 0; j < 5; ++j) {
            if (j == 2) asm volatile("s_waitcnt lgkmcnt(0)" ::: "memory");
            if (j < EPW) w_issue(c.srdW2, c.wv2, w2_off(c, 6 + j), c.ring + j * 4096);
            SB();
            u32x2 o;
            o.x = pk2(gelu_tanh(r[j][0]), gelu_tanh(r[j][1]));
            o.y = pk2(gelu_tanh(r[j][2]), gelu_tanh(r[j][3]));
            __builtin_amdgcn_raw_buffer_store_b64(o, c.srdA, gv + j * 1024, go, 16);
            SB();
        }
        STAMPX(3, 4);
        asm volatile("s_waitcnt vmcnt(0)" ::: "memory");
        if (PK23) {
            w_park8<2>(c);
            w_park8<3>(c);
        }
        asm volatile("s_barrier" ::: "memory");
        if (tid == 0) __hip_atomic_store((gu32*)(ws + WS_FL + (dt1 * 32 + mt1) * 128), 1u, __ATOMIC_RELAXED, __HIP_MEMORY_SCOPE_AGENT);
        STAMP(3, 5);
        asm volatile("s_waitcnt lgkmcnt(0)" ::: "memory");
#pragma unroll
        for (int s = 6 + EPW; s < 14; ++s) {
            if (s == 14 - PLATE)
                asm volatile("global_load_ushort %1, %4, %5\n\tglobal_load_dword %0, %2, %3 sc1"
                             : "=&v"(f), "=&v"(bq) : "v"(fo), "s"(fp), "v"(n * 2), "s"(b2 + mt2 * 64 + 16 * w) : "memory");
            w_issue(s < 12 ? c.srdW2 : c.srdL, c.wv2, w2_off(c, s), c.ring + g2_slot(s) * 4096);
        }
        asm volatile("s_waitcnt vmcnt(%2)" : "+v"(f), "+v"(bq) : "n"(4 * PLATE) : "memory");
    } else {
        const unsigned t0 = (unsigned)__builtin_amdgcn_s_memrealtime();
#pragma unroll
        for (int s = 0; s < 6; ++s) w_issue(c.srdW2, c.wv2, w2_off(c, s), c.ring + s * 4096);
        vm_wait<0>();
        w_park8<0>(c); w_park8<1>(c); w_park8<2>(c); w_park8<3>(c);
        asm volatile("s_waitcnt lgkmcnt(0)" ::: "memory");
#pragma unroll
        for (int s = 6; s < 13; ++s) w_issue(s < 12 ? c.srdW2 : c.srdL, c.wv2, w2_off(c, s), c.ring + g2_slot(s) * 4096);
        fetch_w2(w2, W2N + ((idx - 27) * 4 + w) * 8 + xcd, 864, 160, c.ring + 9 * 4096, lane);
        pause_until(t0, KT);
        warm_w<1>(w2, b2, xcd, (idx - 27) * 4 + w, 20, c.ring + 9 * 4096, lane);
        vm_wait<0>();
        w_issue(c.srdL, c.wv2, w2_off(c, 13), c.ring + g2_slot(13) * 4096);
        asm volatile("global_load_ushort %1, %4, %5\n\tglobal_load_dword %0, %2, %3 sc1\n\ts_waitcnt vmcnt(0)"
                     : "=&v"(f), "=&v"(bq) : "v"(fo), "s"(fp), "v"(n * 2), "s"(b2 + mt2 * 64 + 16 * w) : "memory");
        STAMP(3, 6);
    }
    {
        unsigned spins = 0;
#pragma unroll 1
        for (; spins < (1u << 16); ++spins) {
            if (__all(f != 0)) break;
            asm volatile("global_load_dword %0, %1, %2 sc1\n\ts_waitcnt vmcnt(0)" : "=&v"(f) : "v"(fo), "s"(fp) : "memory");
        }
    }
    STAMP(4, 0);
    g2_first<0>(c);
    {
        const float b = bflo(bq);
        c.bias = (f32x4){b, b, b, b};
        asm volatile("" : "+v"(c.bias));
    }
    SB();
    g2_step<0>(acc, wf, c);
    STAMP(4, 2);
    f32x4 r[5];
    kb_reduce(r, acc, smem, w, lane);
    STAMPX(4, 3);
    {
        u16* op = out + (size_t)(mt2 * 64 + 16 * w + n) * D_ + dt2 * 80 + 20 * hg;
        typedef unsigned u32x4u __attribute__((ext_vector_type(4), aligned(8)));
        u32x4u o0 = {pk2(r[0][0], r[0][1]), pk2(r[0][2], r[0][3]), pk2(r[1][0], r[1][1]), pk2(r[1][2], r[1][3])};
        u32x4u o1 = {pk2(r[2][0], r[2][1]), pk2(r[2][2], r[2][3]), pk2(r[3][0], r[3][1]), pk2(r[3][2], r[3][3])};
        u32x2 o2 = {pk2(r[4][0], r[4][1]), pk2(r[4][2], r[4][3])};
        *(u32x4u*)op = o0;
        *(u32x4u*)(op + 8) = o1;
        *(u32x2*)(op + 16) = o2;
    }
    STAMP(4, 4);
    asm volatile(".Lkb_mlp_last:");
}

extern "C" void kernel_launch(void* const* d_in, const int* in_sizes, int n_in, void* d_out, int out_size,
                              void* d_ws, size_t ws_size, hipStream_t stream) {
    const u16* x = (const u16*)d_in[0];
    const u16* logA = (const u16*)d_in[1];
    const u16* Aim = (const u16*)d_in[2];
    const u16* Cre = (const u16*)d_in[3];
    const u16* Cim = (const u16*)d_in[4];
    const u16* logdt = (const u16*)d_in[5];
    const u16* dskip = (const u16*)d_in[6];
    const u16* w1 = (const u16*)d_in[8];
    const u16* b1 = (const u16*)d_in[9];
    const u16* w2 = (const u16*)d_in[10];
    const u16* b2 = (const u16*)d_in[11];
    char* ws = (char*)d_ws;
    ka_conv<<<256, 512, 0, stream>>>(x, logA, Aim, Cre, Cim, logdt, dskip, ws, w1, b1, w2, b2);
    kb_mlp<<<256, 256, 0, stream>>>(w1, b1, w2, b2, ws, (u16*)d_out);
}
```

```cpp
#include <hip/hip_runtime.h>
#include <hip/hip_bf16.h>
#include <stdint.h>
#pragma clang diagnostic ignored "-Winline-asm"

typedef unsigned short u16;
typedef __bf16 bf16x8 __attribute__((ext_vector_type(8)));
typedef __bf16 bf16x2 __attribute__((ext_vector_type(2)));
typedef short s16x4 __attribute__((ext_vector_type(4)));
typedef float f32x4 __attribute__((ext_vector_type(4)));
typedef float f32x2 __attribute__((ext_vector_type(2)));
typedef unsigned u32x4 __attribute__((ext_vector_type(4)));
typedef unsigned u32x2 __attribute__((ext_vector_type(2)));
#define LDS_AS __attribute__((address_space(3)))

#define STAMP(k, i) do { } while (0)
#define STAMPT(k, i, T) do { } while (0)

#define TS(i) do { } while (0)
#define TSPUT(k, T) do { } while (0)
#define STAMPX(k, i) do { } while (0)
#define FSTAMP STAMP
constexpr int L_ = 2048;
constexpr int D_ = 640;
constexpr int N_ = 128;
constexpr int F_ = 1728;
constexpr unsigned SZ_ = L_ * D_ * 2;
constexpr unsigned WS_HB = 0, WS_GB = SZ_, WS_END = 2 * SZ_;
constexpr unsigned WS_ID = 2 * SZ_, WS_GR = WS_ID + 64 * L_ * 2, WS_EP = WS_GR + 32768;
constexpr unsigned WS_G2 = WS_EP + 4096, WS_FL = WS_G2 + L_ * 8;

__device__ __forceinline__ float bf2f(unsigned v) { return __uint_as_float(v << 16); }
__device__ __forceinline__ float bflo(unsigned v) { return __uint_as_float(v << 16); }
__device__ __forceinline__ float bfhi(unsigned v) { return __uint_as_float(v & 0xffff0000u); }
__device__ __forceinline__ unsigned pk2(float lo, float hi) {
    f32x2 f = {lo, hi};
    bf16x2 b = __builtin_convertvector(f, bf16x2);
    return __builtin_bit_cast(unsigned, b);
}
__device__ __forceinline__ float rb(float x) { return bflo(pk2(x, x)); }

__device__ __forceinline__ f32x4 mfma16(u32x4 a, u32x4 b, f32x4 c) {
    return __builtin_amdgcn_mfma_f32_16x16x32_bf16(__builtin_bit_cast(bf16x8, a), __builtin_bit_cast(bf16x8, b), c, 0, 0, 0);
}
__device__ __forceinline__ u32x2 tr_read(const char* p) {
    s16x4 v = __builtin_amdgcn_ds_read_tr16_b64_v4i16((LDS_AS s16x4*)p);
    return __builtin_bit_cast(u32x2, v);
}
template <int CTRL>
__device__ __forceinline__ float dpp_add(float v) {
    int t = __builtin_amdgcn_update_dpp(0, __float_as_int(v), CTRL, 0xf, 0xf, true);
    return v + __int_as_float(t);
}
__device__ __forceinline__ float row16_sum(float v) {
    v = dpp_add<0x128>(v);
    v = dpp_add<0x124>(v);
    v = dpp_add<0x122>(v);
    v = dpp_add<0x121>(v);
    return v;
}

template <int NT = 0>
__device__ __forceinline__ void touch4(__amdgpu_buffer_rsrc_t srd, unsigned voff, unsigned soff, unsigned m0v) {
    if (NT) asm volatile("s_mov_b32 m0, %0\n\ts_nop 0\n\tbuffer_load_dword %1, %2, %3 offen nt lds"
                 :: "s"(m0v), "v"(voff), "s"(srd), "s"(soff) : "m0", "memory");
    else asm volatile("s_mov_b32 m0, %0\n\ts_nop 0\n\tbuffer_load_dword %1, %2, %3 offen lds"
                 :: "s"(m0v), "v"(voff), "s"(srd), "s"(soff) : "m0", "memory");
}
#define MT2S 1
__device__ __forceinline__ int mt2_of(int p, int i) { return MT2S == 1 ? 8 * p + i : MT2S == 2 ? 16 * (p >> 1) + 2 * i + (p & 1) : 4 * i + p; }
#define W2M 1
#define W2N 864
#define XT 1
#define W1SL 3
#define KT 300
#define W1X 8
#define BAL 1
#define W2C 320
#define CW 27
#define CODE_SRD(srd, sym) do { unsigned lo_, hi_, n_; \
    asm("s_getpc_b64 vcc\n\ts_add_u32 %0, vcc_lo, " sym "@rel32@lo+4\n\ts_addc_u32 %1, vcc_hi, " sym "@rel32@hi+12\n\t" \
        "s_mov_b32 %2, .L" sym "_last-" sym : "=s"(lo_), "=s"(hi_), "=s"(n_) :: "vcc", "scc"); \
    srd = __builtin_amdgcn_make_buffer_rsrc((void*)(((unsigned long long)hi_ << 32) | lo_), (short)0, n_, 0x00020000); } while (0)
__device__ __forceinline__ void pause_until(unsigned t0, unsigned ticks) {
#pragma unroll 1
    for (int i = 0; i < 64 && (unsigned)__builtin_amdgcn_s_memrealtime() - t0 < ticks; ++i) __builtin_amdgcn_s_sleep(4);
}
__device__ __forceinline__ void fetch_w2(const u16* W, int j, int j1, int nw, unsigned lds, int lane) {
    const __amdgpu_buffer_rsrc_t srdW = __builtin_amdgcn_make_buffer_rsrc((void*)W, (short)0, L_ * F_ * 2, 0x00020000);
    for (; j < j1; j += nw) touch4(srdW, lane * 128, j * 8192, lds);
}
__device__ __forceinline__ void touch_kb(int part, unsigned lds, int lane) {
    __amdgpu_buffer_rsrc_t srdC;
    CODE_SRD(srdC, "kb_mlp");
    touch4(srdC, lane * 128, part * 8192, lds);
}
template <int MODE>
__device__ __forceinline__ void warm_w(const u16* W, const u16* bias, int xcd, int gw, int nw, unsigned lds, int lane) {
    constexpr int LDW = MODE == 0 ? F_ : L_;
    const __amdgpu_buffer_rsrc_t srdW = __builtin_amdgcn_make_buffer_rsrc((void*)W, (short)0, L_ * F_ * 2, 0x00020000);
    if (gw == 0) touch4(__builtin_amdgcn_make_buffer_rsrc((void*)bias, (short)0, LDW * 2, 0x00020000), lane * 128, 0, lds);
    const int mt0 = MODE == 0 ? ((xcd >> 1) * 27) >> 2 : mt2_of(xcd >> 1, 0);
    const unsigned voff = ((lane >> 3) * LDW + (lane & 7) * (MODE == 0 ? 64 : 64 * MT2S)) * 2;
    int r = 0;
    for (int j = gw; j < (MODE == 0 ? 256 : 224); j += nw) {
        if (MODE == 0 && r < W1SL) asm volatile("s_waitcnt vmcnt(0)" ::: "memory");
        if (MODE == 1 && (CW & 16) && r == (5 * (gw >> 2)) >> 1) touch_kb(gw & 3, lds, lane);
        ++r;
        const int s = MODE == 0 ? (j >> 4) ^ ((xcd & 1) * W1X) : j >> 4, w = (j >> 2) & 3;
        const int ks = MODE == 0 ? w * 16 + s : w * 14 + s;
        if (MODE != 0 && ks >= 54) continue;
        touch4(srdW, voff, ((ks * 32 + (j & 3) * 8) * LDW + mt0 * 64) * 2, lds);
    }
}

constexpr int KA_ROWB = 528;
constexpr int KA_LO = 32 * KA_ROWB;
constexpr int KA_HI0 = 4 * KA_LO;
constexpr int KA_HI = 13312;
constexpr int KA_MB = 20000;
constexpr int CV_COPY = 4160;
constexpr int CV_UB = 4608;
constexpr int KA_U = KA_HI0 + 4 * KA_HI;
#define XC1 4
#define XCS 3
#define GFIRST " sc1"
typedef __attribute__((address_space(1))) unsigned gu32;

__device__ __forceinline__ void cm4(f32x2& r0, f32x2 a0, f32x2 b0, f32x2& r1, f32x2 a1, f32x2 b1,
                                    f32x2& r2, f32x2 a2, f32x2 b2, f32x2& r3, f32x2 a3, f32x2 b3) {
    asm("v_pk_mul_f32 %0, %4, %5 op_sel:[0,0] op_sel_hi:[1,0]\n\t"
        "v_pk_mul_f32 %1, %6, %7 op_sel:[0,0] op_sel_hi:[1,0]\n\t"
        "v_pk_mul_f32 %2, %8, %9 op_sel:[0,0] op_sel_hi:[1,0]\n\t"
        "v_pk_mul_f32 %3, %10, %11 op_sel:[0,0] op_sel_hi:[1,0]\n\t"
        "v_pk_fma_f32 %0, %4, %5, %0 op_sel:[1,1,0] op_sel_hi:[0,1,1] neg_lo:[1,0,0]\n\t"
        "v_pk_fma_f32 %1, %6, %7, %1 op_sel:[1,1,0] op_sel_hi:[0,1,1] neg_lo:[1,0,0]\n\t"
        "v_pk_fma_f32 %2, %8, %9, %2 op_sel:[1,1,0] op_sel_hi:[0,1,1] neg_lo:[1,0,0]\n\t"
        "v_pk_fma_f32 %3, %10, %11, %3 op_sel:[1,1,0] op_sel_hi:[0,1,1] neg_lo:[1,0,0]"
        : "=&v"(r0), "=&v"(r1), "=&v"(r2), "=&v"(r3)
        : "v"(a0), "v"(b0), "v"(a1), "v"(b1), "v"(a2), "v"(b2), "v"(a3), "v"(b3));
}
constexpr int ka_off(int r) { return r < 32 ? r * KA_ROWB : KA_HI0 - KA_MB + (r - 32) * KA_ROWB; }
template <int R0, int R1, int R2, int R3>
__device__ __forceinline__ void put4(const unsigned (&m0v)[2], f32x2 v0, f32x2 v1, f32x2 v2, f32x2 v3) {
    static_assert((R0 < 32) == (R1 < 32) && (R0 < 32) == (R2 < 32) && (R0 < 32) == (R3 < 32), "");
    unsigned t0, t1, t2, t3;
    asm volatile("s_mov_b32 m0, %4\n\t"
                 "v_cvt_pk_bf16_f32 %0, %5, %6\n\tv_cvt_pk_bf16_f32 %1, %7, %8\n\t"
                 "v_cvt_pk_bf16_f32 %2, %9, %10\n\tv_cvt_pk_bf16_f32 %3, %11, %12\n\t"
                 "ds_write_addtid_b32 %0 offset:%13\n\tds_write_addtid_b32 %1 offset:%14\n\t"
                 "ds_write_addtid_b32 %2 offset:%15\n\tds_write_addtid_b32 %3 offset:%16"
                 : "=&v"(t0), "=&v"(t1), "=&v"(t2), "=&v"(t3)
                 : "s"(m0v[R0 >= 32]), "v"(v0.x), "v"(v0.y), "v"(v1.x), "v"(v1.y), "v"(v2.x), "v"(v2.y), "v"(v3.x), "v"(v3.y),
                   "n"(ka_off(R0)), "n"(ka_off(R1)), "n"(ka_off(R2)), "n"(ka_off(R3))
                 : "m0", "memory");
}
__device__ __forceinline__ f32x2 cexp_p(float zr, float zi, float p) {
    const float mag = __builtin_amdgcn_exp2f(zr * p);
    const float rev = __builtin_amdgcn_fractf(zi * p);
    f32x2 r;
    r.x = mag * __builtin_amdgcn_cosf(rev);
    r.y = mag * __builtin_amdgcn_sinf(rev);
    return r;
}
__device__ __forceinline__ float wave_sum(float v) {
    v = row16_sum(v);
    const int i = __float_as_int(v);
    return (__int_as_float(__builtin_amdgcn_readlane(i, 0)) + __int_as_float(__builtin_amdgcn_readlane(i, 16))) +
           (__int_as_float(__builtin_amdgcn_readlane(i, 32)) + __int_as_float(__builtin_amdgcn_readlane(i, 48)));
}
__device__ __forceinline__ unsigned next_lane(unsigned v, unsigned last) {
    return (unsigned)__builtin_amdgcn_update_dpp((int)last, (int)v, 0x130, 0xf, 0xf, false);
}
__device__ __forceinline__ void put_copies(char* p, unsigned r0, unsigned r1, unsigned r2, unsigned r3, unsigned r4, unsigned r5) {
    const unsigned s0 = __builtin_amdgcn_alignbit(r1, r0, 16), s1 = __builtin_amdgcn_alignbit(r2, r1, 16),
                   s2 = __builtin_amdgcn_alignbit(r3, r2, 16), s3 = __builtin_amdgcn_alignbit(r4, r3, 16),
                   s4 = __builtin_amdgcn_alignbit(r5, r4, 16);
    *(u32x4*)(p + 0 * CV_COPY) = (u32x4){r0, r1, r2, r3};
    *(u32x4*)(p + 1 * CV_COPY) = (u32x4){s0, s1, s2, s3};
    *(u32x4*)(p + 2 * CV_COPY) = (u32x4){r1, r2, r3, r4};
    *(u32x4*)(p + 3 * CV_COPY) = (u32x4){s1, s2, s3, s4};
}
template <int O> __device__ __forceinline__ void rd64(u32x2& v, unsigned a) {
    asm volatile("ds_read_b64 %0, %1 offset:%2" : "=v"(v) : "v"(a), "n"(O) : "memory");
}
template <int O> __device__ __forceinline__ void rd128(u32x4& v, unsigned a) {
    asm volatile("ds_read_b128 %0, %1 offset:%2" : "=v"(v) : "v"(a), "n"(O) : "memory");
}
struct CvFrag { u32x2 lo[8], hi[8]; u32x4 b[8]; };
__device__ __forceinline__ void cv_wait(CvFrag& f) {
    asm volatile("s_waitcnt lgkmcnt(0)"
                 : "+v"(f.lo[0]), "+v"(f.lo[1]), "+v"(f.lo[2]), "+v"(f.lo[3]), "+v"(f.lo[4]), "+v"(f.lo[5]), "+v"(f.lo[6]), "+v"(f.lo[7]),
                   "+v"(f.hi[0]), "+v"(f.hi[1]), "+v"(f.hi[2]), "+v"(f.hi[3]), "+v"(f.hi[4]), "+v"(f.hi[5]), "+v"(f.hi[6]), "+v"(f.hi[7]),
                   "+v"(f.b[0]), "+v"(f.b[1]), "+v"(f.b[2]), "+v"(f.b[3]), "+v"(f.b[4]), "+v"(f.b[5]), "+v"(f.b[6]), "+v"(f.b[7])
                 :: "memory");
}
template <int T>
__device__ __forceinline__ void cv_mma(const CvFrag& f, f32x4 (&acc)[8]) {
    constexpr int q = T < 1 ? 0 : T < 3 ? 1 : T < 6 ? 2 : T < 10 ? 3 : T < 15 ? 4 : T < 21 ? 5 : T < 28 ? 6 : 7;
    constexpr int g1 = T - q * (q + 1) / 2;
    const u32x4 a = {f.lo[g1].x, f.lo[g1].y, f.hi[g1].x, f.hi[g1].y};
    acc[q] = mfma16(a, f.b[q - g1], acc[q]);
}
template <int G0, int R>
__device__ __forceinline__ void cv_read(CvFrag& f, unsigned aa, unsigned ba) {
    constexpr int G1 = R / 3;
    if constexpr (R % 3 == 0) rd64<(63 - 8 * G1 - G0) * 64>(f.lo[G1], aa);
    else if constexpr (R % 3 == 1) rd64<(63 - 8 * G1 - G0) * 64 + 8>(f.hi[G1], aa);
    else rd128<(8 * G1 - G0 + 7) * 64>(f.b[G1], ba);
}
template <int G0, int T>
__device__ __forceinline__ void cv_group(const CvFrag& cur, CvFrag& nxt, f32x4 (&acc)[8], unsigned aa, unsigned ba) {
    cv_mma<T>(cur, acc);
    __builtin_amdgcn_sched_barrier(0);
    if constexpr (G0 < 7 && T < 30 && T % 5 != 4) {
        cv_read<G0 + 1, T - T / 5>(nxt, aa, ba);
        __builtin_amdgcn_sched_barrier(0);
    }
    if constexpr (T < 35) cv_group<G0, T + 1>(cur, nxt, acc, aa, ba);
}
template <int G0>
__device__ __forceinline__ void cv_round(CvFrag& cur, CvFrag& nxt, f32x4 (&acc)[8], unsigned aa, unsigned ba) {
    cv_wait(cur);
    if constexpr (G0 == 5) __builtin_amdgcn_s_barrier();
    __builtin_amdgcn_sched_barrier(0);
    cv_group<G0, 0>(cur, nxt, acc, aa, ba);
    if constexpr (G0 < 7) cv_round<G0 + 1>(nxt, cur, acc, aa, ba);
}
template <int I>
__device__ __forceinline__ void cv_first(CvFrag& f, unsigned aa, unsigned ba) {
    cv_read<0, I>(f, aa, ba);
    if constexpr (I < 23) cv_first<I + 1>(f, aa, ba);
}

template <class Hook>
__device__ __forceinline__ void ka_chain(const unsigned (&m0v)[2], float p_la, float p_ai, float p_cr, float p_ci, float p_dt, Hook hook) {
    const float dt = rb(__builtin_amdgcn_exp2f(p_dt * 1.44269504088896341f));
    const float ar = -rb(__builtin_amdgcn_exp2f(p_la * 1.44269504088896341f));
    const float zr = ar * dt * 1.44269504088896341f, zi = p_ai * dt * 0.15915494309189535f;
    f32x2 a[17], b[16], e[8], F1, F2, F4, F8, r0, r1, r2, r3;
    a[0] = (f32x2){2.0f * (p_cr * dt), -2.0f * (p_ci * dt)};
    e[1] = cexp_p(zr, -zi, 1.f);
    b[1] = cexp_p(zr, zi, 8.f);
    F1 = cexp_p(zr, -zi, 128.f);
    cm4(e[2], e[1], e[1], b[2], b[1], b[1], F2, F1, F1, a[1], a[0], F1);
    cm4(e[4], e[2], e[2], b[4], b[2], b[2], F4, F2, F2, e[3], e[2], e[1]);
    put4<41, 42, 43, 45>(m0v, (f32x2){1.f, 0.f}, b[1], b[2], b[4]);
    hook(0);
    cm4(a[2], a[0], F2, a[3], a[1], F2, b[3], b[2], b[1], r0, a[0], e[1]);
    cm4(F8, F4, F4, b[8], b[4], b[4], r1, a[1], e[1], r2, a[0], e[2]);
    put4<16, 20, 1, 9>(m0v, a[2], a[3], r0, r1);
    hook(1);
    cm4(a[4], a[0], F4, a[5], a[1], F4, a[6], a[2], F4, a[7], a[3], F4);
    put4<24, 26, 28, 30>(m0v, a[4], a[5], a[6], a[7]);
    hook(2);
    cm4(e[5], e[4], e[1], e[6], e[4], e[2], e[7], e[4], e[3], r3, a[1], e[2]);
    put4<0, 8, 2, 10>(m0v, a[0], a[1], r2, r3);
    hook(3);
    cm4(b[5], b[4], b[1], b[6], b[4], b[2], b[7], b[4], b[3], r0, a[0], e[3]);
    put4<44, 49, 46, 47>(m0v, b[3], b[8], b[5], b[6]);
    hook(4);
    cm4(a[8], a[0], F8, a[9], a[1], F8, a[10], a[2], F8, a[11], a[3], F8);
    put4<32, 33, 34, 35>(m0v, a[8], a[9], a[10], a[11]);
    hook(5);
    cm4(a[12], a[4], F8, a[13], a[5], F8, a[14], a[6], F8, a[15], a[7], F8);
    put4<36, 37, 38, 39>(m0v, a[12], a[13], a[14], a[15]);
    hook(6);
    cm4(a[16], a[8], F8, r1, a[1], e[3], r2, a[0], e[4], r3, a[1], e[4]);
    put4<3, 11, 4, 12>(m0v, r0, r1, r2, r3);
    hook(7);
    cm4(b[9], b[8], b[1], b[10], b[8], b[2], b[11], b[8], b[3], b[12], b[8], b[4]);
    put4<50, 51, 52, 53>(m0v, b[9], b[10], b[11], b[12]);
    hook(8);
    cm4(b[13], b[8], b[5], b[14], b[8], b[6], b[15], b[8], b[7], r0, a[0], e[5]);
    put4<54, 55, 56, 48>(m0v, b[13], b[14], b[15], b[7]);
    asm volatile("ds_write_addtid_b32 %0 offset:%1" :: "v"(pk2(a[16].x, a[16].y)), "n"(ka_off(40)) : "memory");
    hook(9);
    cm4(r1, a[1], e[5], r2, a[0], e[6], r3, a[1], e[6], F1, a[0], e[7]);
    put4<5, 13, 6, 14>(m0v, r0, r1, r2, r3);
    hook(10);
    cm4(r0, a[1], e[7], r1, a[2], e[2], r2, a[3], e[2], F2, a[2], e[4]);
    put4<7, 15, 17, 21>(m0v, F1, r0, r1, r2);
    hook(11);
    cm4(r3, a[3], e[4], F1, a[2], e[6], r0, a[3], e[6], r1, a[4], e[4]);
    put4<18, 22, 19, 23>(m0v, F2, r3, F1, r0);
    hook(12);
    cm4(r2, a[5], e[4], r3, a[6], e[4], F1, a[7], e[4], F2, a[7], e[4]);
    put4<25, 27, 29, 31>(m0v, r1, r2, r3, F1);
    asm volatile("s_waitcnt lgkmcnt(0)" ::: "memory");
}
__device__ __forceinline__ void unpack4(float (&o)[4], u32x2 v) {
    asm volatile("v_lshlrev_b32 %0, 16, %4\n\tv_and_b32 %1, 0xffff0000, %4\n\tv_lshlrev_b32 %2, 16, %5\n\tv_and_b32 %3, 0xffff0000, %5"
                 : "=&v"(o[0]), "=&v"(o[1]), "=&v"(o[2]), "=&v"(o[3]) : "v"(v.x), "v"(v.y));
}

extern "C" __global__ __launch_bounds__(512, 2) void ka_conv(const u16* __restrict__ x, const u16* __restrict__ logA,
                                                  const u16* __restrict__ Aim, const u16* __restrict__ Cre,
                                                  const u16* __restrict__ Cim, const u16* __restrict__ logdt,
                                                  const u16* __restrict__ dskip, char* ws,
                                                  const u16* __restrict__ w1, const u16* __restrict__ b1,
                                                  const u16* __restrict__ w2, const u16* __restrict__ b2) {
    __shared__ __attribute__((aligned(16))) char smem[KA_U + 4 * CV_UB + 256];
    const int tid = threadIdx.x, lane = tid & 63;
    const int w = __builtin_amdgcn_readfirstlane(tid >> 6);
    STAMP(0, 0);
    if ((CW & 1) && w >= 6) {
        __amdgpu_buffer_rsrc_t srdC;
        CODE_SRD(srdC, "ka_conv");
        touch4(srdC, lane * 128, (w - 6) * 8192, (unsigned)(size_t)(LDS_AS char*)(smem + KA_U + 4 * CV_UB));
    }
    asm volatile("" :: "s"(x), "s"(logA), "s"(Aim), "s"(Cre), "s"(Cim), "s"(logdt), "s"(dskip), "s"(ws));
    const int xcd = blockIdx.x & 7, idx = blockIdx.x >> 3;
    const bool conv = idx < 20;
    const int grp = ((xcd & 1) * 4 + (xcd >> 1)) * 20 + (conv ? idx : 0);
    const int d0 = grp * 4;
    const int row = (xcd * 32 + idx) * 8 + w;
    unsigned sv[5], epoch, pp[5];
    float pf[5];
    asm volatile("global_load_dword %0, %11, %12\n\tglobal_load_dword %1, %11, %12 offset:256\n\t"
                 "global_load_dword %2, %11, %12 offset:512\n\tglobal_load_dword %3, %11, %12 offset:768\n\t"
                 "global_load_dword %4, %11, %12 offset:1024\n\tglobal_load_dword %5, %13, %14\n\t"
                 "global_load_ushort %6, %15, %16\n\tglobal_load_ushort %7, %15, %17\n\t"
                 "global_load_ushort %8, %15, %18\n\tglobal_load_ushort %9, %15, %19\n\t"
                 "global_load_ushort %10, %13, %20"
                 : "=&v"(sv[0]), "=&v"(sv[1]), "=&v"(sv[2]), "=&v"(sv[3]), "=&v"(sv[4]), "=&v"(epoch),
                   "=&v"(pp[0]), "=&v"(pp[1]), "=&v"(pp[2]), "=&v"(pp[3]), "=&v"(pp[4])
                 : "v"(lane * 4), "s"(x + (size_t)row * D_), "v"(0), "s"(ws + WS_EP),
                   "v"((d0 * N_ + tid) * 2), "s"(logA), "s"(Aim), "s"(Cre), "s"(Cim), "s"(logdt + d0 + (w >> 1))
                 : "memory");
    const int t = tid & 255;
    const __amdgpu_buffer_rsrc_t srdG = __builtin_amdgcn_make_buffer_rsrc((void*)(ws + WS_GR), (short)0, L_ * 8, 0x00020000);
    const __amdgpu_buffer_rsrc_t srdX = __builtin_amdgcn_make_buffer_rsrc((void*)x, (short)0, SZ_, 0x00020000);
    {
        const int tw = XT == 1 ? (BAL ? (conv ? 64 : (7 - w) * 12 + idx - 20) : (idx - 20) * 8 + w) : XT == 2 ? (w < 4 ? idx * 4 + w : 64) : XT == 3 ? (w < 2 ? idx * 2 + w : 64) : 64;
        touch4(srdX, (lane >> 1) * (D_ * 2) + (lane & 1) * 156, (unsigned)tw < 64u ? tw * (32 * D_ * 2) + (grp / 20) * 160 : SZ_,
               (unsigned)(size_t)(LDS_AS char*)(smem + KA_U + 4 * CV_UB));
    }
    const int owner = conv ? w >> 2 : 0;
    u32x2 xc[8], xd;
    asm volatile("s_barrier\n\ts_cmp_eq_u32 %5, 0\n\ts_cbranch_scc1 .Lka_a%=\n\t"
                 "global_load_dwordx2 %4, %6, %7\n\t"
                 "buffer_load_dwordx2 %0, %8, %9, %10 offen\n\tbuffer_load_dwordx2 %1, %8, %9, %10 offen offset:1280\n\t"
                 "buffer_load_dwordx2 %2, %8, %9, %10 offen offset:2560\n\tbuffer_load_dwordx2 %3, %8, %9, %10 offen offset:3840\n\t"
                 "s_waitcnt vmcnt(6)\n\ts_branch .Lka_b%=\n"
                 ".Lka_a%=:\n\ts_waitcnt vmcnt(1)\n"
                 ".Lka_b%=:"
                 : "=&v"(xc[0]), "=&v"(xc[1]), "=&v"(xc[2]), "=&v"(xc[3]), "=&v"(xd)
                 : "s"(owner), "v"(0), "s"(dskip + d0), "v"(t * (8 * D_ * 2)), "s"(srdX), "s"(d0 * 2) : "scc", "memory");
    unsigned etag;
    {
        float v[12];
        unpack4(*(float(*)[4])v, (u32x2){sv[0], sv[1]});
        unpack4(*(float(*)[4])(v + 4), (u32x2){sv[2], sv[3]});
        asm volatile("v_lshlrev_b32 %0, 16, %3\n\tv_and_b32 %1, 0xffff0000, %3\n\tv_xor_b32 %2, 0x9e3779b9, %4"
                     : "=&v"(v[8]), "=&v"(v[9]), "=&v"(etag) : "v"(sv[4]), "v"(epoch));
        const float s = ((v[0] + v[1]) + (v[2] + v[3])) + ((v[4] + v[5]) + (v[6] + v[7])) + (v[8] + v[9]);
        const float mean = wave_sum(s) * (1.0f / D_);
        float q0 = 0.f, q1 = 0.f;
#pragma unroll
        for (int j = 0; j < 5; ++j) {
            const float a = v[2 * j] - mean, b = v[2 * j + 1] - mean;
            q0 = __builtin_fmaf(a, a, q0);
            q1 = __builtin_fmaf(b, b, q1);
        }
        const float q = wave_sum(q0 + q1);
        const float den = __builtin_amdgcn_sqrtf(rb(rb(q * (1.0f / D_)) + 1e-5f));
        asm volatile("buffer_store_dwordx2 %10, %11, %12, %13 offen sc1\n\t"
                     "v_lshlrev_b32 %0, 16, %5\n\tv_lshlrev_b32 %1, 16, %6\n\tv_lshlrev_b32 %2, 16, %7\n\t"
                     "v_lshlrev_b32 %3, 16, %8\n\tv_lshlrev_b32 %4, 16, %9"
                     : "=&v"(pf[0]), "=&v"(pf[1]), "=&v"(pf[2]), "=&v"(pf[3]), "=&v"(pf[4])
                     : "v"(pp[0]), "v"(pp[1]), "v"(pp[2]), "v"(pp[3]), "v"(pp[4]),
                       "v"((u32x2){pk2(mean, den), etag * 0x85ebca6bu}), "v"(lane * (L_ * 8)), "s"(srdG), "s"(row * 8) : "memory");
    }
    TS(0);
    if (!conv) {
        const unsigned lds = (unsigned)(size_t)(LDS_AS char*)(smem + w * 1024);
        if ((CW & 2) && idx == 28 && w < 3) touch_kb(w, lds, lane);
        const int gw = BAL ? w * 12 + idx - 20 : (idx - 20) * 8 + w;
        warm_w<0>(w1, b1, xcd, gw, 96, lds, lane);
        if (W2M & 1) asm volatile("s_waitcnt vmcnt(0)" ::: "memory");
        fetch_w2(w2, W2C + (95 - gw) * 8 + xcd, W2N, 768, lds, lane);
        if ((CW & 8) && idx == 27 && w >= 5) touch_kb(w - 5, lds, lane);
        if (idx == 31 && tid < 256) {
            const int r = xcd * 8 + (tid >> 5), c = tid & 31;
            *(unsigned*)(ws + WS_ID + r * (L_ * 2) + 4 * c) = (r >> 1) == c ? ((r & 1) ? 0x3f800000u : 0x3f80u) : 0u;
        }
        if (idx == 30 && tid < 128) *(u32x4*)(ws + WS_G2 + (xcd * 128 + tid) * 16) = (u32x4){0u, 0u, 0u, 0u};
        if (idx == 29 && tid < 32) *(unsigned*)(ws + WS_FL + (xcd * 32 + tid) * 128) = 0u;
        asm volatile("s_waitcnt vmcnt(0)" ::: "memory");
        STAMP(0, 7);
        return;
    }
    const unsigned lds0 = (unsigned)(size_t)(LDS_AS char*)smem;
    const int m = lane & 15, hg = lane >> 4;
    char* mine = smem + (w & 3) * KA_LO;
    char* minh = smem + KA_HI0 + (w & 3) * KA_HI;
    const unsigned m0v[2] = {lds0 + (w >> 1) * KA_LO + (w & 1) * 256, lds0 + KA_MB + (w >> 1) * KA_HI + (w & 1) * 256};
    ka_chain(m0v, pf[0], pf[1], pf[2], pf[3], pf[4], [&](int i) {
        if (i % XCS == 0 && i / XCS < 4)
            asm volatile("s_cmp_eq_u32 %1, 0\n\ts_cbranch_scc1 .Lka_c%=\n\tbuffer_load_dwordx2 %0, %2, %3, %4 offen offset:%5\n.Lka_c%=:"
                         : "=v"(xc[4 + i / XCS])
                         : "s"(owner), "v"(t * (8 * D_ * 2)), "s"(srdX), "s"(d0 * 2 + 4 * D_ * 2), "n"((i / XCS) * (D_ * 2)) : "scc", "memory");
    });
    TS(1);
    __builtin_amdgcn_s_barrier();
    if (w < 4) {
        TS(2);
        u16* kd = (u16*)(minh + 8192);
        {
            const char* fb = mine + m * KA_ROWB + hg * 16;
            const char* fh = minh + m * KA_ROWB + hg * 16;
            u32x4 fa[8][3], fl[8];
#pragma unroll
            for (int ks = 0; ks < 8; ++ks) {
                fl[ks] = *(const u32x4*)(fh + 9 * KA_ROWB + ks * 64);
                fa[ks][0] = *(const u32x4*)(fb + ks * 64);
                fa[ks][1] = *(const u32x4*)(fb + 16 * KA_ROWB + ks * 64);
                fa[ks][2] = *(const u32x4*)(fh + ks * 64);
            }
            __builtin_amdgcn_sched_barrier(0);
            f32x4 acc0 = {0.f, 0.f, 0.f, 0.f}, acc1 = acc0, acc2 = acc0;
#pragma unroll
            for (int ks = 0; ks < 8; ++ks) {
                acc0 = mfma16(fa[ks][0], fl[ks], acc0);
                acc1 = mfma16(fa[ks][1], fl[ks], acc1);
                acc2 = mfma16(fa[ks][2], fl[ks], acc2);
            }
            const int c = m;
            *(u32x2*)(kd + 128 * (hg >> 1) + 4 * (hg & 1) + 8 * c) = (u32x2){pk2(acc0[0], acc0[1]), pk2(acc0[2], acc0[3])};
            const unsigned p01 = pk2(acc1[0], acc1[1]), p23 = pk2(acc1[2], acc1[3]);
            const int i0 = hg < 2 ? 256 + 64 * hg + 4 * c : 256 + 64 * hg + 2 * c;
            *(unsigned*)(kd + i0) = p01;
            *(unsigned*)(kd + i0 + (hg < 2 ? 2 : 32)) = p23;
            const unsigned q01 = pk2(acc2[0], acc2[1]), q23 = pk2(acc2[2], acc2[3]);
            kd[512 + 64 * hg + c] = (u16)(q01 & 0xffff);
            kd[528 + 64 * hg + c] = (u16)(q01 >> 16);
            kd[544 + 64 * hg + c] = (u16)(q23 & 0xffff);
            kd[560 + 64 * hg + c] = (u16)(q23 >> 16);
            asm volatile("" ::: "memory");
        }
        const unsigned* kw = (const unsigned*)kd;
        const unsigned dw0 = kw[319 - lane], dw1 = kw[320 - lane];
        const unsigned cw0 = kw[255 - lane], cw1 = kw[256 - lane];
        const bool isA = lane >= 32;
        const char* bp = (const char*)kd + (isA ? 16 * (63 - lane) : 256 + 8 * (63 - lane));
        const u32x2 b01 = *(const u32x2*)bp, b23 = *(const u32x2*)(bp + 8);
        const unsigned P = __builtin_amdgcn_perm(0, dw1, 0x01000100), Q = __builtin_amdgcn_perm(0, dw0, 0x03020302),
                       M = __builtin_amdgcn_perm(0, dw0, 0x01000302), Z = __builtin_amdgcn_perm(0, dw0, 0x01000100);
        const unsigned c0 = __builtin_amdgcn_perm(0, cw1, 0x01000100), c1 = __builtin_amdgcn_perm(0, cw0, 0x03020302),
                       c2 = __builtin_amdgcn_perm(0, cw0, 0x01000302), c3 = __builtin_amdgcn_perm(0, cw0, 0x01000100);
        const unsigned o0 = __builtin_amdgcn_perm(b23.x, isA ? b23.y : b01.y, isA ? 0x01000302u : 0x03020504u),
                       o1 = __builtin_amdgcn_perm(b23.x, b01.y, isA ? 0x05040706u : 0x01000100u),
                       o2 = __builtin_amdgcn_perm(b01.y, b01.x, isA ? 0x05040706u : 0x03020504u),
                       o3 = __builtin_amdgcn_perm(0, b01.x, isA ? 0x01000302u : 0x01000100u);
        put_copies(mine + 3072 + lane * 16, o0, o1, o2, o3, next_lane(o0, 0), next_lane(o1, 0));
        put_copies(mine + 2048 + lane * 16, c0, c1, c2, c3, next_lane(c0, __builtin_amdgcn_readlane(o0, 0)),
                   next_lane(c1, __builtin_amdgcn_readlane(o1, 0)));
        put_copies(mine + lane * 32, P, P, Q, Q, Q, M);
        put_copies(mine + lane * 32 + 16, Q, M, Z, Z, next_lane(P, __builtin_amdgcn_readlane(c0, 0)),
                   next_lane(P, __builtin_amdgcn_readlane(c1, 0)));
        if (lane < 8) *(u32x4*)(mine + (lane >> 1) * CV_COPY + 4096 + (lane & 1) * 16) = (u32x4){0u, 0u, 0u, 0u};
        asm volatile("s_waitcnt vmcnt(0)" ::: "memory");
        TS(5);
        __syncthreads();
        TS(6);
        const unsigned aa = (unsigned)(size_t)(LDS_AS char*)(mine + (3 - (m & 3)) * CV_COPY + 2 * (2028 - 32 * 63 - 4 * (m >> 2) + 8 * hg));
        const unsigned ba = (unsigned)(size_t)(LDS_AS char*)(smem + KA_U + w * CV_UB + (2 * m + hg) * 16);
        f32x4 acc[8];
#pragma unroll
        for (int q = 0; q < 8; ++q) acc[q] = (f32x4){0.f, 0.f, 0.f, 0.f};
        CvFrag f0, f1;
        cv_first<0>(f0, aa, ba);
        cv_round<0>(f0, f1, acc, aa, ba);
        TS(7);
        const char* rp = minh + (16 * m + 4 * hg) * 4;
        char* hp = mine + (16 * m + 4 * hg) * 2;
#pragma unroll
        for (int q = 0; q < 8; ++q) {
            const f32x4 h = acc[q] + *(const f32x4*)(rp + q * 1024);
            *(u32x2*)(hp + q * 512) = (u32x2){pk2(h[0], h[1]), pk2(h[2], h[3])};
        }
    } else {
        const unsigned tag = etag * 0x85ebca6bu;
        u32x4 g[4];
        asm volatile("buffer_load_dwordx4 %0, %4, %5, 0 offen" GFIRST "\n\tbuffer_load_dwordx4 %1, %4, %5, 0 offen offset:16" GFIRST "\n\t"
                     "buffer_load_dwordx4 %2, %4, %5, 0 offen offset:32" GFIRST "\n\tbuffer_load_dwordx4 %3, %4, %5, 0 offen offset:48" GFIRST "\n\t"
                     "s_waitcnt vmcnt(4)"
                     : "=&v"(g[0]), "=&v"(g[1]), "=&v"(g[2]), "=&v"(g[3]) : "v"(t * 64), "s"(srdG) : "memory");
        float xf[8][4], dk[4];
#pragma unroll
        for (int k = 0; k < 8; ++k) unpack4(xf[k], xc[k]);
        unpack4(dk, xd);
        if (lane < 30) *(u32x4*)(smem + KA_U + (w - 4) * CV_UB + lane * 16) = (u32x4){0u, 0u, 0u, 0u};
        TS(3);
        asm volatile("s_waitcnt vmcnt(0)" : "+v"(g[0]), "+v"(g[1]), "+v"(g[2]), "+v"(g[3]) :: "memory");
        unsigned spins = 0;
#pragma unroll 1
        for (; spins < (1u << 16); ++spins) {
            const bool bad = ((g[0].y ^ tag) | (g[0].w ^ tag) | (g[1].y ^ tag) | (g[1].w ^ tag) |
                              (g[2].y ^ tag) | (g[2].w ^ tag) | (g[3].y ^ tag) | (g[3].w ^ tag)) != 0;
            if (!__any(bad)) break;
            if (bad)
                asm volatile("buffer_load_dwordx4 %0, %4, %5, 0 offen sc1\n\tbuffer_load_dwordx4 %1, %4, %5, 0 offen offset:16 sc1\n\t"
                             "buffer_load_dwordx4 %2, %4, %5, 0 offen offset:32 sc1\n\tbuffer_load_dwordx4 %3, %4, %5, 0 offen offset:48 sc1\n\t"
                             "s_waitcnt vmcnt(0)"
                             : "+v"(g[0]), "+v"(g[1]), "+v"(g[2]), "+v"(g[3]) : "v"(t * 64), "s"(srdG) : "memory");
        }
        TS(4);
        unsigned up[4][4];
#pragma unroll
        for (int k = 0; k < 4; ++k) {
            const float i0 = __builtin_amdgcn_rcpf(bfhi(g[k].x)), i1 = __builtin_amdgcn_rcpf(bfhi(g[k].z));
            const float m0 = bflo(g[k].x), m1 = bflo(g[k].z);
#pragma unroll
            for (int c = 0; c < 4; ++c) up[c][k] = pk2((xf[2 * k][c] - m0) * i0, (xf[2 * k + 1][c] - m1) * i1);
        }
#pragma unroll
        for (int c = 0; c < 4; ++c)
            *(u32x4*)(smem + KA_U + c * CV_UB + 480 + t * 16) = (u32x4){up[c][0], up[c][1], up[c][2], up[c][3]};
        TS(5);
        __syncthreads();
        if (blockIdx.x == 0 && tid == 256)
            __hip_atomic_store((gu32*)(ws + WS_EP), (etag ^ 0x9e3779b9u) + 1u, __ATOMIC_RELAXED, __HIP_MEMORY_SCOPE_AGENT);
#pragma unroll
        for (int c = 0; c < 4; ++c) {
            f32x4 r0, r1;
#pragma unroll
            for (int k = 0; k < 2; ++k) {
                r0[2 * k] = __builtin_fmaf(bflo(up[c][k]), dk[c], xf[2 * k][c]);
                r0[2 * k + 1] = __builtin_fmaf(bfhi(up[c][k]), dk[c], xf[2 * k + 1][c]);
                r1[2 * k] = __builtin_fmaf(bflo(up[c][2 + k]), dk[c], xf[4 + 2 * k][c]);
                r1[2 * k + 1] = __builtin_fmaf(bfhi(up[c][2 + k]), dk[c], xf[5 + 2 * k][c]);
            }
            *(f32x4*)(smem + KA_HI0 + c * KA_HI + t * 32) = r0;
            *(f32x4*)(smem + KA_HI0 + c * KA_HI + t * 32 + 16) = r1;
        }
        fetch_w2(w2, (w - 4) * 160 + blockIdx.x, W2C, 640, lds0 + KA_U + 4 * CV_UB, lane);
        asm volatile("s_waitcnt lgkmcnt(0)" ::: "memory");
        __builtin_amdgcn_s_barrier();
        asm volatile("s_waitcnt vmcnt(0)" ::: "memory");
    }
    __syncthreads();
    {
        const int ch = tid & 3;
        const int dt = grp / 20, rem = grp % 20, mq = rem / 5, j = rem % 5;
#pragma unroll
        for (int i = 0; i < 2; ++i) {
            const int piece = (tid >> 2) + 128 * i;
            const int unit = ((piece >> 2) * 40 + dt * 5 + j) * 64 + (piece & 3) * 16 + 4 * mq + ch;
            *(u32x4*)(ws + WS_HB + (size_t)unit * 16) = *(const u32x4*)(smem + ch * KA_LO + piece * 16);
        }
    }
    STAMP(0, 6);
    TSPUT(1, 0);
    TSPUT(5, 448);
    asm volatile(".Lka_conv_last:");
}

constexpr int B_REG = 40960;
constexpr int B_RED = 24576;
constexpr int B_TAB = 32768;
#define PPOS 5
#define NP1 6
#define NPRO 8
#define WL 4
#define PK23 1
#define TQ1 4
#define TQ2 9
#define RSTEPS 11
#define PLATE 2
#define EPW 1
#define G2D 4
#define PRE 2

__device__ __forceinline__ float gelu_tanh(float x) {
    const float p = __builtin_fmaf(x * x, -2.302208198f * 0.044715f, -2.302208198f);
    const float e = __builtin_amdgcn_exp2f(p * x);
    return x * __builtin_amdgcn_rcpf(1.0f + e);
}
template <int I>
__device__ __forceinline__ void w_piece(__amdgpu_buffer_rsrc_t srd, unsigned v, unsigned so, unsigned m0v) {
    if (I == 0) asm volatile("s_mov_b32 m0, %0\n\ts_nop 0\n\tbuffer_load_dwordx4 %1, %2, %3 offen lds"
                             :: "s"(m0v), "v"(v), "s"(srd), "s"(so) : "m0", "memory");
    else asm volatile("buffer_load_dwordx4 %0, %1, %2 offen offset:%3 lds"
                      :: "v"(v), "s"(srd), "s"(so), "n"(I * 1024) : "memory");
}
__device__ __forceinline__ void w_issue(__amdgpu_buffer_rsrc_t srd, const unsigned (&v)[4], unsigned so, unsigned m0v) {
    m0v = __builtin_amdgcn_readfirstlane(m0v);
    asm volatile("s_mov_b32 m0, %0\n\ts_nop 0\n\t"
                 "buffer_load_dwordx4 %1, %5, %6 offen lds\n\tbuffer_load_dwordx4 %2, %5, %6 offen offset:1024 lds\n\t"
                 "buffer_load_dwordx4 %3, %5, %6 offen offset:2048 lds\n\tbuffer_load_dwordx4 %4, %5, %6 offen offset:3072 lds"
                 :: "s"(m0v), "v"(v[0]), "v"(v[1]), "v"(v[2]), "v"(v[3]), "s"(srd), "s"(so) : "m0", "memory");
}
template <int SC, int SL, int J>
__device__ __forceinline__ void a_piece(__amdgpu_buffer_rsrc_t srd, unsigned av0, unsigned av1, unsigned so) {
    if (SC) asm volatile("buffer_load_dwordx4 a[%c0:%c1], %2, %3, %4 offen offset:%5 sc1"
                         :: "i"(SL * 20 + J * 4), "i"(SL * 20 + J * 4 + 3), "v"(J < 4 ? av0 : av1), "s"(srd), "s"(so), "n"((J & 3) * 1024) : "memory");
    else asm volatile("buffer_load_dwordx4 a[%c0:%c1], %2, %3, %4 offen offset:%5"
                      :: "i"(SL * 20 + J * 4), "i"(SL * 20 + J * 4 + 3), "v"(J < 4 ? av0 : av1), "s"(srd), "s"(so), "n"((J & 3) * 1024) : "memory");
}
template <int SC, int SL>
__device__ __forceinline__ void a_issue(__amdgpu_buffer_rsrc_t srd, unsigned av0, unsigned av1, unsigned so) {
    a_piece<SC, SL, 0>(srd, av0, av1, so); a_piece<SC, SL, 1>(srd, av0, av1, so); a_piece<SC, SL, 2>(srd, av0, av1, so);
    a_piece<SC, SL, 3>(srd, av0, av1, so); a_piece<SC, SL, 4>(srd, av0, av1, so);
}
template <int N> __device__ __forceinline__ void vm_wait() { asm volatile("s_waitcnt vmcnt(%0)" :: "n"(N) : "memory"); }
template <int R>
__device__ __forceinline__ void affine4(f32x4& d, u32x2 al, f32x4 be) {
    asm volatile("v_mfma_f32_4x4x4_16b_bf16 %0, %1, a[%c2:%c3], %4" : "=&v"(d) : "v"(al), "i"(R), "i"(R + 1), "v"(be));
}
template <int INIT>
__device__ __forceinline__ void mma_vv(f32x4& c, u32x4 a, u32x4 b, f32x4 c0) {
    if (INIT == 0) asm volatile("v_mfma_f32_16x16x32_bf16 %0, %1, %2, %0" : "+v"(c) : "v"(a), "v"(b));
    if (INIT == 1) asm volatile("v_mfma_f32_16x16x32_bf16 %0, %1, %2, 0" : "=&v"(c) : "v"(a), "v"(b));
    if (INIT == 2) asm volatile("v_mfma_f32_16x16x32_bf16 %0, %1, %2, %3" : "=&v"(c) : "v"(a), "v"(b), "v"(c0));
}
template <int INIT, int R>
__device__ __forceinline__ void mma_av(f32x4& c, u32x4 b, f32x4 c0) {
    if (INIT == 0) asm volatile("v_mfma_f32_16x16x32_bf16 %0, a[%c1:%c2], %3, %0" : "+v"(c) : "i"(R), "i"(R + 3), "v"(b));
    if (INIT == 1) asm volatile("v_mfma_f32_16x16x32_bf16 %0, a[%c1:%c2], %3, 0" : "=&v"(c) : "i"(R), "i"(R + 3), "v"(b));
    if (INIT == 2) asm volatile("v_mfma_f32_16x16x32_bf16 %0, a[%c1:%c2], %3, %4" : "=&v"(c) : "i"(R), "i"(R + 3), "v"(b), "v"(c0));
}
template <int INIT, int R, int Q>
__device__ __forceinline__ void mma_aa(f32x4& c, f32x4 c0) {
    if (INIT == 0) asm volatile("v_mfma_f32_16x16x32_bf16 %0, a[%c1:%c2], a[%c3:%c4], %0" : "+v"(c) : "i"(R), "i"(R + 3), "i"(Q), "i"(Q + 3));
    if (INIT == 1) asm volatile("v_mfma_f32_16x16x32_bf16 %0, a[%c1:%c2], a[%c3:%c4], 0" : "=&v"(c) : "i"(R), "i"(R + 3), "i"(Q), "i"(Q + 3));
    if (INIT == 2) asm volatile("v_mfma_f32_16x16x32_bf16 %0, a[%c1:%c2], a[%c3:%c4], %5" : "=&v"(c) : "i"(R), "i"(R + 3), "i"(Q), "i"(Q + 3), "v"(c0));
}
#define SB() __builtin_amdgcn_sched_barrier(0)
struct Nrm { u32x2 a0, a1; f32x4 b0, b1; };
struct KbCtx {
    __amdgpu_buffer_rsrc_t srdW1, srdW2, srdL, srdA, srdH;
    unsigned wv1[4], wv2[4], av0, av1, ring, w1o, a1o, w2o, a2o, wlo[2], alo[2];
    const char* tp[4];
    unsigned tpa[4];
    const char* ta;
    const char* tb;
    char* red;
    f32x4 bias;
};
__device__ __forceinline__ unsigned w1_off(const KbCtx& c, int g) { return c.w1o + g * (32 * F_ * 2); }
__device__ __forceinline__ unsigned a1_off(const KbCtx& c, int g) { return c.a1o + g * 40960; }
__device__ __forceinline__ unsigned w2_off(const KbCtx& c, int g) { return g >= 12 ? c.wlo[g & 1] : c.w2o + g * (32 * L_ * 2); }
__device__ __forceinline__ unsigned a2_off(const KbCtx& c, int g) { return g >= 12 ? c.alo[g & 1] : c.a2o + g * 40960; }
template <int R>
__device__ __forceinline__ void w_read(u32x4 (&wf)[4], const KbCtx& c, int SL) {
    const u32x2 v = tr_read(c.tp[R >> 1] + SL * 4096 + (R & 1) * 512);
    if (R & 1) { wf[R >> 1].z = v.x; wf[R >> 1].w = v.y; } else { wf[R >> 1].x = v.x; wf[R >> 1].y = v.y; }
}
template <int I>
__device__ __forceinline__ void nrm_read(Nrm& t, const KbCtx& c, int S) {
    if (I == 0) { t.a0 = *(const u32x2*)(c.ta + S * 256); t.a1 = *(const u32x2*)(c.ta + S * 256 + 32); }
    if (I == 1) t.b0 = *(const f32x4*)(c.tb + S * 128);
    if (I == 2) t.b1 = *(const f32x4*)(c.tb + S * 128 + 16);
}
template <int T>
__device__ __forceinline__ void red_put(const f32x4 (&acc)[4][5], const KbCtx& c) {
    if constexpr (T >= 8) *(f32x4*)(c.red + (T - 8) * 1024) = acc[(T - 8) / 5 + 1][(T - 8) % 5];
    if constexpr (T == 19) {
        SB();
#pragma unroll
        for (int j = 2; j < 5; ++j) *(f32x4*)(c.red + (10 + j) * 1024) = acc[3][j];
    }
}
constexpr int G1Q = (16 - NPRO) * 9;
constexpr int g1_cum(int s) {
    return s <= RSTEPS ? (G1Q * s + RSTEPS - 1) / RSTEPS : G1Q + (s >= 15 ? 4 * WL : 4 * WL * (s - RSTEPS) / (15 - RSTEPS));
}
template <int S, int P>
__device__ __forceinline__ void g1_req(const KbCtx& c) {
    constexpr int q = g1_cum(S) + P;
    static_assert(g1_cum(S + 1) - g1_cum(S) <= 9, "");
    if constexpr (q < g1_cum(S + 1)) {
        if constexpr (q < G1Q) {
            constexpr int g = NPRO + q / 9, r = q % 9;
            static_assert(g - 8 <= S && g >= S + 2, "");
            if constexpr (r < 4) w_piece<r>(c.srdW1, c.wv1[r], w1_off(c, g), c.ring + (g & 7) * 4096);
            else a_piece<0, g & 7, r - 4>(c.srdH, c.av0, c.av1, a1_off(c, g));
        } else {
            constexpr int g = (q - G1Q) / 4, r = (q - G1Q) % 4;
            static_assert(g + 8 <= S, "");
            w_piece<r>(c.srdW2, c.wv2[r], w2_off(c, g), c.ring + g * 4096);
        }
    }
}
template <int SL, int R>
__device__ __forceinline__ void w_park(const KbCtx& c) {
    asm volatile("ds_read_b64_tr_b16 a[%c0:%c1], %2 offset:%3"
                 :: "i"(160 + SL * 16 + R * 2), "i"(161 + SL * 16 + R * 2), "v"(c.tpa[R >> 1]), "n"(SL * 4096 + (R & 1) * 512) : "memory");
}
template <int SL> __device__ __forceinline__ void w_park8(const KbCtx& c) {
    w_park<SL, 0>(c); w_park<SL, 1>(c); w_park<SL, 2>(c); w_park<SL, 3>(c); w_park<SL, 4>(c); w_park<SL, 5>(c); w_park<SL, 6>(c); w_park<SL, 7>(c);
}
template <int S, int T>
__device__ __forceinline__ void g1_mma(f32x4 (&acc)[4][5], u32x4 (&n)[2][5], u32x4 (&wf)[2][4], const KbCtx& c) {
    mma_vv<S ? 0 : (T & 3) ? 1 : 2>(acc[T & 3][T >> 2], wf[S & 1][T & 3], n[S & 1][T >> 2], c.bias);
    SB();
}
constexpr int g1_aux(int t) {
    constexpr int a[20] = {0, 1, 2, 3, 4, -1, 5, -2, 6, -3, 7, -4, 8, 9, 10, -5, -6, -7, -8, -9};
    return a[t];
}
template <int S, int T>
__device__ __forceinline__ void g1_body(f32x4 (&acc)[4][5], u32x4 (&n)[2][5], u32x4 (&wf)[2][4], f32x4 (&d)[10], Nrm& t, const KbCtx& c) {
    g1_mma<S, T>(acc, n, wf, c);
    n[(S + 1) & 1][T >> 2][T & 3] = pk2(d[T >> 1][(T & 1) * 2], d[T >> 1][(T & 1) * 2 + 1]);
    constexpr int a = g1_aux(T);
    if constexpr (a < 0) g1_req<S, -1 - a>(c);
    if constexpr (a >= 3) w_read<a - 3>(wf[(S + 1) & 1], c, (S + 1) & 7);
    if constexpr (a >= 0 && a < 3 && S < 14) nrm_read<a>(t, c, S + 2);
    SB();
    if constexpr (T < 19) g1_body<S, T + 1>(acc, n, wf, d, t, c);
}
template <int S, int T>
__device__ __forceinline__ void g1_mid(f32x4 (&d)[10], const Nrm& t) {
    affine4<(S & 7) * 20 + T * 2>(d[T], (T & 1) ? t.a1 : t.a0, (T & 1) ? t.b1 : t.b0);
    SB();
    if constexpr (T < 9) g1_mid<S, T + 1>(d, t);
}
template <int T>
__device__ __forceinline__ void g1_last(f32x4 (&acc)[4][5], u32x4 (&n)[5], u32x4 (&wf)[4], const KbCtx& c) {
    constexpr int i = (T / 5 + 1) & 3, j = T % 5;
    mma_vv<0>(acc[i][j], wf[i], n[j], c.bias);
    SB();
    if constexpr (T < 8) { w_park<T / 4, (T & 3) * 2>(c); w_park<T / 4, (T & 3) * 2 + 1>(c); }
    red_put<T>(acc, c);
    constexpr int q0 = (24 - 4 * WL) * T / 20, q1 = (24 - 4 * WL) * (T + 1) / 20;
    if constexpr (q1 > q0) w_piece<q0 & 3>(c.srdW2, c.wv2[q0 & 3], w2_off(c, WL + q0 / 4), c.ring + (WL + q0 / 4) * 4096);
    SB();
    if constexpr (T < 19) g1_last<T + 1>(acc, n, wf, c);
}
template <int N> __device__ __forceinline__ void all_wait() {
    asm volatile("" ::: "memory");
    __builtin_amdgcn_s_waitcnt((N & 15) | 0x70 | ((N >> 4) << 14));
    asm volatile("" ::: "memory");
    SB();
}
template <int S>
__device__ __forceinline__ void g1_step(f32x4 (&acc)[4][5], u32x4 (&n)[2][5], u32x4 (&wf)[2][4], Nrm& t, const KbCtx& c) {
    if constexpr (S == 15) {
        all_wait<4 * WL - 8>();
        g1_last<0>(acc, n[1], wf[1], c);
    } else {
        constexpr int young = NPRO * 9 + g1_cum(S) - (S + 2) * 9;
        static_assert(young >= 0 && young + g1_cum(S + 1) - g1_cum(S) <= 63, "");
        all_wait<young>();
        f32x4 d[10];
        g1_mid<S + 1, 0>(d, t);
        g1_body<S, 0>(acc, n, wf, d, t, c);
        g1_step<S + 1>(acc, n, wf, t, c);
    }
}
constexpr int g2_slot(int s) { return s < 6 ? s : s < 10 ? s - 6 : s - 4; }
template <int S, int P>
__device__ __forceinline__ void g2_req(const KbCtx& c) {
    if constexpr (S < 14) a_piece<1, S & 7, P>(c.srdA, c.av0, c.av1, a2_off(c, S));
}
template <int S, int T>
__device__ __forceinline__ void g2_body(f32x4 (&acc)[4][5], u32x4 (&wf)[2][4], const KbCtx& c) {
    constexpr int i = (T / 5 + 1) & 3, j = T % 5;
    if constexpr (S < 4) mma_aa<S ? 0 : i ? 1 : 2, (S & 7) * 20 + j * 4, 160 + S * 16 + i * 4>(acc[i][j], c.bias);
    else mma_av<0, (S & 7) * 20 + j * 4>(acc[i][j], wf[S & 1][i], c.bias);
    SB();
    if constexpr (S == 13) red_put<T>(acc, c);
    else {
        if constexpr (T % 4 == 0) g2_req<S + G2D, T / 4>(c);
        else if constexpr (S >= 3 && T > 5 && T < 17) w_read<(T - T / 4 - 3) & 7>(wf[(S + 1) & 1], c, g2_slot(S + 1));
    }
    SB();
    if constexpr (T < 19) g2_body<S, T + 1>(acc, wf, c);
}
template <int S>
__device__ __forceinline__ void g2_step(f32x4 (&acc)[4][5], u32x4 (&wf)[2][4], const KbCtx& c) {
    vm_wait<5 * (S + G2D > 14 ? 13 - S : G2D - 1)>();
    if (S == 0) STAMPX(4, 1);
    SB();
    g2_body<S, 0>(acc, wf, c);
    if constexpr (S < 13) g2_step<S + 1>(acc, wf, c);
}
__device__ __forceinline__ void kb_reduce(f32x4 (&r)[5], const f32x4 (&acc)[4][5], char* smem, int w, int lane) {
    asm volatile("s_waitcnt lgkmcnt(0)\n\ts_barrier" ::: "memory");
    f32x4 p[5][3];
#pragma unroll
    for (int j = 0; j < 5; ++j)
#pragma unroll
        for (int i = 1; i < 4; ++i)
            p[j][i - 1] = *(const f32x4*)(smem + ((w - i) & 3) * B_REG + B_RED + lane * 16 + (i - 1) * 5120 + j * 1024);
    SB();
#pragma unroll
    for (int j = 0; j < 5; ++j) r[j] = (acc[0][j] + p[j][0]) + (p[j][1] + p[j][2]);
}
template <int S0, int S1>
__device__ __forceinline__ void g1_groups(const KbCtx& c) {
    if constexpr (S0 < S1) {
        if (S0 >= 3) vm_wait<54>();
        w_issue(c.srdW1, c.wv1, w1_off(c, S0), c.ring + S0 * 4096);
        a_issue<0, S0>(c.srdH, c.av0, c.av1, a1_off(c, S0));
        g1_groups<S0 + 1, S1>(c);
    }
}
template <int Q0, int Q1>
__device__ __forceinline__ void g1_tail(const KbCtx& c) {
    if constexpr (Q0 < Q1 && Q0 < (NPRO - NP1) * 9) {
        constexpr int g = NP1 + Q0 / 9, r = Q0 % 9;
        if constexpr (r < 4) w_piece<r>(c.srdW1, c.wv1[r], w1_off(c, g), c.ring + g * 4096);
        else a_piece<0, g, r - 4>(c.srdH, c.av0, c.av1, a1_off(c, g));
        g1_tail<Q0 + 1, Q1>(c);
    }
    SB();
}
template <int T>
__device__ __forceinline__ void g1_mid0(f32x4 (&d)[10], const Nrm& t, const KbCtx& c) {
    affine4<T * 2>(d[T], (T & 1) ? t.a1 : t.a0, (T & 1) ? t.b1 : t.b0);
    SB();
    if constexpr (T & 1) g1_tail<TQ2 + T / 2, TQ2 + T / 2 + 1>(c);
    if constexpr (T < 9) g1_mid0<T + 1>(d, t, c);
}
template <int S>
__device__ __forceinline__ void g2_first(const KbCtx& c) {
    a_issue<1, S>(c.srdA, c.av0, c.av1, a2_off(c, S));
    if constexpr (S < G2D - 1) g2_first<S + 1>(c);
}
template <int CTRL> __device__ __forceinline__ f32x2 dpp_add2(f32x2 v) { return (f32x2){dpp_add<CTRL>(v.x), dpp_add<CTRL>(v.y)}; }

extern "C" __global__ __launch_bounds__(256, 1) void kb_mlp(const u16* __restrict__ w1, const u16* __restrict__ b1, const u16* __restrict__ w2,
                                                 const u16* __restrict__ b2, char* ws, u16* __restrict__ out) {
    __shared__ __attribute__((aligned(16))) char smem[4 * B_REG];
    const int tid = threadIdx.x, lane = tid & 63;
    const int w = __builtin_amdgcn_readfirstlane(tid >> 6);
    STAMP(2, 0);
    const int n = lane & 15, hg = lane >> 4;
    KbCtx c;
    c.srdA = __builtin_amdgcn_make_buffer_rsrc((void*)ws, (short)0, WS_END, 0x00020000);
    u32x4 hs[3];
    {
        const unsigned v = hg * 1024 + n * 16;
        asm volatile("buffer_load_dwordx4 %0, %3, %5, %6 offen\n\tbuffer_load_dwordx4 %1, %3, %5, %7 offen\n\tbuffer_load_dwordx4 %2, %4, %5, %7 offen"
                     : "=&v"(hs[0]), "=&v"(hs[1]), "=&v"(hs[2])
                     : "v"(v), "v"(v + 4096 + (lane >> 5) * WS_END), "s"(c.srdA),
                       "s"(WS_HB + ((blockIdx.x >> 2) * 40 + 10 * w) * 1024 + (blockIdx.x & 3) * 256),
                       "s"(WS_HB + ((blockIdx.x >> 2) * 40 + 10 * w + 4) * 1024 + (blockIdx.x & 3) * 256) : "memory");
    }
    const int xcd = blockIdx.x & 7, idx = blockIdx.x >> 3;
    const bool g1 = idx < 27;
    const int t1 = (xcd >> 1) * 27 + (g1 ? idx : 0), t2 = (xcd >> 1) * 32 + idx;
    const int mt1 = t1 >> 2, dt1 = (xcd & 1) * 4 + (t1 & 3), mt2 = mt2_of(xcd >> 1, idx >> 2), dt2 = (xcd & 1) * 4 + (t2 & 3);
    char* ring = smem + w * B_REG;
    c.ring = (unsigned)(size_t)(LDS_AS char*)ring;
    c.srdW1 = __builtin_amdgcn_make_buffer_rsrc((void*)w1, (short)0, g1 ? L_ * F_ * 2 : 0, 0x00020000);
    c.srdH = __builtin_amdgcn_make_buffer_rsrc((void*)ws, (short)0, g1 ? WS_END : 0, 0x00020000);
    c.srdW2 = __builtin_amdgcn_make_buffer_rsrc((void*)w2, (short)0, L_ * F_ * 2, 0x00020000);
    const bool idw = w == 3;
    c.srdL = __builtin_amdgcn_make_buffer_rsrc((void*)(idw ? ws + WS_ID : (const char*)w2), (short)0, L_ * F_ * 2, 0x00020000);
    {
        const int wr = lane >> 3, wslot = lane & 7;
        const unsigned s0 = ((((wslot >> 1) ^ ((wr >> 1) & 1)) * 2 + (wslot & 1)) * 8) * 2;
        const unsigned s1 = ((((wslot >> 1) ^ (((wr >> 1) & 1) | 2)) * 2 + (wslot & 1)) * 8) * 2;
        c.wv1[0] = wr * F_ * 2 + s0; c.wv1[1] = (wr + 8) * F_ * 2 + s1 - 1024;
        c.wv1[2] = c.wv1[0] + 16 * F_ * 2 - 2048; c.wv1[3] = c.wv1[1] + 16 * F_ * 2 - 2048;
        c.wv2[0] = wr * L_ * 2 + s0; c.wv2[1] = (wr + 8) * L_ * 2 + s1 - 1024;
        c.wv2[2] = c.wv2[0] + 16 * L_ * 2 - 2048; c.wv2[3] = c.wv2[1] + 16 * L_ * 2 - 2048;
    }
    c.av0 = lane * 16; c.av1 = lane * 16 + 4096;
    c.w1o = (w * 16 * 32 * F_ + mt1 * 64) * 2;
    c.a1o = WS_HB + (w * 16 * 40 + dt1 * 5) * 1024;
    c.w2o = (w * 14 * 32 * L_ + mt2 * 64) * 2;
    c.a2o = WS_GB + (w * 14 * 40 + dt2 * 5) * 1024;
#pragma unroll
    for (int i = 0; i < 2; ++i) {
        c.wlo[i] = idw ? i * (32 * L_ * 2) : c.w2o + (12 + i) * (32 * L_ * 2);
        c.alo[i] = idw ? WS_HB + ((mt2 * 2 + i) * 40 + dt2 * 5) * 1024 : c.a2o + (12 + i) * 40960;
    }
    u32x2 bb;
    asm volatile("global_load_dwordx2 %0, %1, %2" : "=v"(bb) : "v"(hg * 8), "s"(b1 + mt1 * 64 + 16 * w) : "memory");
    asm volatile("" ::: "a0", "a223");
    g1_groups<0, PRE>(c);
    {
        const int q4 = n >> 2, p4 = n & 3;
        const int fsw = ((q4 >> 1) & 1) | ((hg & 1) << 1);
        const char* tb = ring + (8 * hg + q4) * 128 + p4 * 8;
#pragma unroll
        for (int i = 0; i < 4; ++i) {
            c.tp[i] = tb + ((((i + w) & 3) ^ fsw) * 32);
            c.tpa[i] = (unsigned)(size_t)(LDS_AS const char*)c.tp[i];
        }
        c.ta = ring + B_TAB + (8 * hg + (lane & 3)) * 8;
        c.tb = ring + B_TAB + 4096 + hg * 32;
        c.red = ring + B_RED + lane * 16;
    }
    const __amdgpu_buffer_rsrc_t srdG = __builtin_amdgcn_make_buffer_rsrc((void*)(ws + WS_G2), (short)0, L_ * 8, 0x00020000);
    {
        const unsigned one = (n & 1) ? 0x3f800000u : 0x3f80u;
        const int dw = (hg & 1) == (n >> 3) ? (n >> 1) & 3 : 4;
        const u32x4 sel = {dw == 0 ? one : 0u, dw == 1 ? one : 0u, dw == 2 ? one : 0u, dw == 3 ? one : 0u};
        const u32x4 z4 = {0u, 0u, 0u, 0u};
        const u32x4 selA = hg < 2 ? sel : z4, selB = hg < 2 ? z4 : sel;
        f32x4 sum, x0, x1, x2, x3, x4;
        asm volatile("s_waitcnt vmcnt(%12)\n\t"
                     "v_mfma_f32_16x16x32_bf16 %1, %6, %9, 0\n\tv_mfma_f32_16x16x32_bf16 %2, %6, %10, 0\n\t"
                     "v_mfma_f32_16x16x32_bf16 %3, %7, %9, 0\n\tv_mfma_f32_16x16x32_bf16 %4, %7, %10, 0\n\t"
                     "v_mfma_f32_16x16x32_bf16 %5, %8, %9, 0\n\tv_mfma_f32_16x16x32_bf16 %0, %6, %11, 0\n\t"
                     "v_mfma_f32_16x16x32_bf16 %0, %7, %11, %0\n\tv_mfma_f32_16x16x32_bf16 %0, %8, %11, %0\n\ts_nop 4"
                     : "=&v"(sum), "=&v"(x0), "=&v"(x1), "=&v"(x2), "=&v"(x3), "=&v"(x4)
                     : "v"(hs[0]), "v"(hs[1]), "v"(hs[2]), "v"(selA), "v"(selB), "v"(sel), "n"(9 * PRE + 1) : "memory");
        STAMPX(2, 1);
        f32x4 q = (x0 * x0 + x1 * x1) + (x2 * x2 + x3 * x3) + x4 * x4;
        asm volatile("s_nop 2" : "+v"(sum), "+v"(q));
        char* sc = smem + B_TAB + 6144;
        *(f32x2*)(sc + (n & 7) * 256 + (w * 8 + hg * 2 + (n >> 3)) * 8) = (f32x2){(sum[0] + sum[1]) + (sum[2] + sum[3]), (q[0] + q[1]) + (q[2] + q[3])};
        asm volatile("s_waitcnt lgkmcnt(0)\n\ts_barrier" ::: "memory");
        if (w == 0) {
            const f32x4 p0 = *(const f32x4*)(sc + lane * 32), p1 = *(const f32x4*)(sc + lane * 32 + 16);
            f32x2 v = {(p0[0] + p0[2]) + (p1[0] + p1[2]), (p0[1] + p0[3]) + (p1[1] + p1[3])};
            v = dpp_add2<0xb1>(v);
            v = dpp_add2<0x4e>(v);
            v = dpp_add2<0x141>(v);
            const float mean = v.x * (1.0f / D_);
            const float var = fmaxf(__builtin_fmaf(-mean, mean, v.y * (1.0f / D_)), 0.f);
            const float m1 = rb(mean), d1 = rb(__builtin_amdgcn_sqrtf(rb(rb(var) + 1e-5f)));
            const float i1 = __builtin_amdgcn_rcpf(d1);
            const float m2 = rb((mean - m1) * i1), d2 = rb(__builtin_amdgcn_sqrtf(rb(rb(var * i1 * i1) + 1e-5f)));
            const float al = rb(i1 * __builtin_amdgcn_rcpf(d2));
            const float be = -__builtin_fmaf(m2, d1, m1) * al;
            asm volatile("buffer_store_dwordx2 %0, %1, %2, %3 offen sc1"
                         :: "v"((u32x2){__float_as_uint(be), __float_as_uint(al) | 1u}), "v"((lane & 7) * (L_ * 8) + lane), "s"(srdG), "s"(blockIdx.x * 64) : "memory");
            STAMP(2, 2);
        }
    }
    f32x4 acc[4][5];
    u32x4 wf[2][4];
    unsigned f, bq;
    const unsigned fo = (lane < (w == 3 ? 6 : 7) ? lane : 0) * 128;
    const char* fp = ws + WS_FL + (dt2 * 32 + 7 * w) * 128;
    if (g1) {
        g1_groups<PRE, PPOS>(c);
        u32x4 g[4];
        const unsigned gvo = lane * 64;
        asm volatile("s_waitcnt vmcnt(59)\n\t"
                     "buffer_load_dwordx4 %0, %4, %5, %6 offen sc1\n\tbuffer_load_dwordx4 %1, %4, %5, %6 offen offset:16 sc1\n\t"
                     "buffer_load_dwordx4 %2, %4, %5, %6 offen offset:32 sc1\n\tbuffer_load_dwordx4 %3, %4, %5, %6 offen offset:48 sc1"
                     : "=&v"(g[0]), "=&v"(g[1]), "=&v"(g[2]), "=&v"(g[3]) : "v"(gvo), "s"(srdG), "s"(w * 4096) : "memory");
        g1_groups<PPOS, NP1>(c);
        STAMPX(2, 3);
        asm volatile("s_waitcnt vmcnt(%5)" : "+v"(g[0]), "+v"(g[1]), "+v"(g[2]), "+v"(g[3]), "+v"(bb) : "n"(9 * (NP1 - PPOS)) : "memory");
        STAMPX(2, 4);
        unsigned spins = 0;
#pragma unroll 1
        for (; spins < (1u << 16); ++spins) {
            if (__all(g[0].y & g[0].w & g[1].y & g[1].w & g[2].y & g[2].w & g[3].y & g[3].w & 1u)) break;
            asm volatile("buffer_load_dwordx4 %0, %4, %5, %6 offen sc1\n\tbuffer_load_dwordx4 %1, %4, %5, %6 offen offset:16 sc1\n\t"
                         "buffer_load_dwordx4 %2, %4, %5, %6 offen offset:32 sc1\n\tbuffer_load_dwordx4 %3, %4, %5, %6 offen offset:48 sc1\n\t"
                         "s_waitcnt vmcnt(0)"
                         : "=&v"(g[0]), "=&v"(g[1]), "=&v"(g[2]), "=&v"(g[3]) : "v"(gvo), "s"(srdG), "s"(w * 4096) : "memory");
        }
        STAMPX(2, 5);
        {
            char* tw = ring + B_TAB + lane * 64;
#pragma unroll
            for (int k = 0; k < 4; ++k) {
                const unsigned lo = g[k].y >> 16, hi = g[k].w & 0xffff0000u;
                *(u32x4*)(tw + k * 16) = (k & 1) ? (u32x4){0u, lo, 0u, hi} : (u32x4){lo, 0u, hi, 0u};
            }
            *(u32x4*)(ring + B_TAB + 4096 + lane * 32) = (u32x4){g[0].x, g[0].z, g[1].x, g[1].z};
            *(u32x4*)(ring + B_TAB + 4096 + lane * 32 + 16) = (u32x4){g[2].x, g[2].z, g[3].x, g[3].z};
            asm volatile("" ::: "memory");
        }
        Nrm t;
        u32x4 nf[2][5];
        nrm_read<0>(t, c, 0); nrm_read<1>(t, c, 0); nrm_read<2>(t, c, 0);
        SB();
        g1_tail<0, TQ1>(c);
        w_read<0>(wf[0], c, 0); w_read<1>(wf[0], c, 0); w_read<2>(wf[0], c, 0); w_read<3>(wf[0], c, 0);
        w_read<4>(wf[0], c, 0); w_read<5>(wf[0], c, 0); w_read<6>(wf[0], c, 0); w_read<7>(wf[0], c, 0);
        SB();
        g1_tail<TQ1, TQ2>(c);
        {
            c.bias = (f32x4){bflo(bb.x), bfhi(bb.x), bflo(bb.y), bfhi(bb.y)};
            asm volatile("" : "+v"(c.bias));
            f32x4 d[10];
            SB();
            g1_mid0<0>(d, t, c);
            nrm_read<0>(t, c, 1); nrm_read<1>(t, c, 1); nrm_read<2>(t, c, 1);
            asm volatile("s_nop 4");
            SB();
#pragma unroll
            for (int k = 0; k < 20; ++k) nf[0][k >> 2][k & 3] = pk2(d[k >> 1][(k & 1) * 2], d[k >> 1][(k & 1) * 2 + 1]);
            SB();
            g1_tail<TQ2 + 5, 27>(c);
        }
        STAMP(3, 1);
        g1_step<0>(acc, nf, wf, t, c);
        STAMP(3, 2);
        f32x4 r[5];
        kb_reduce(r, acc, smem, w, lane);
        STAMPX(3, 3);
        const unsigned go = WS_GB + ((mt1 * 2 + (w >> 1)) * 40 + dt1 * 5) * 1024;
        const unsigned gv = (((w & 1) * 2 + (hg >> 1)) * 16 + n) * 16 + (hg & 1) * 8;
        if (!PK23) {
            vm_wait<8>();
            w_park8<2>(c);
            w_park8<3>(c);
        }
#pragma unroll
        for (int j = 0; j < 5; ++j) {
            if (j == 2) asm volatile("s_waitcnt lgkmcnt(0)" ::: "memory");
            if (j < EPW) w_issue(c.srdW2, c.wv2, w2_off(c, 6 + j), c.ring + j * 4096);
            SB();
            u32x2 o;
            o.x = pk2(gelu_tanh(r[j][0]), gelu_tanh(r[j][1]));
            o.y = pk2(gelu_tanh(r[j][2]), gelu_tanh(r[j][3]));
            __builtin_amdgcn_raw_buffer_store_b64(o, c.srdA, gv + j * 1024, go, 16);
            SB();
        }
        STAMPX(3, 4);
        asm volatile("s_waitcnt vmcnt(0)" ::: "memory");
        if (PK23) {
            w_park8<2>(c);
            w_park8<3>(c);
        }
        asm volatile("s_barrier" ::: "memory");
        if (tid == 0) __hip_atomic_store((gu32*)(ws + WS_FL + (dt1 * 32 + mt1) * 128), 1u, __ATOMIC_RELAXED, __HIP_MEMORY_SCOPE_AGENT);
        STAMP(3, 5);
        asm volatile("s_waitcnt lgkmcnt(0)" ::: "memory");
#pragma unroll
        for (int s = 6 + EPW; s < 14; ++s) {
            if (s == 14 - PLATE)
                asm volatile("global_load_ushort %1, %4, %5\n\tglobal_load_dword %0, %2, %3 sc1"
                             : "=&v"(f), "=&v"(bq) : "v"(fo), "s"(fp), "v"(n * 2), "s"(b2 + mt2 * 64 + 16 * w) : "memory");
            w_issue(s < 12 ? c.srdW2 : c.srdL, c.wv2, w2_off(c, s), c.ring + g2_slot(s) * 4096);
        }
        asm volatile("s_waitcnt vmcnt(%2)" : "+v"(f), "+v"(bq) : "n"(4 * PLATE) : "memory");
    } else {
        const unsigned t0 = (unsigned)__builtin_amdgcn_s_memrealtime();
#pragma unroll
        for (int s = 0; s < 6; ++s) w_issue(c.srdW2, c.wv2, w2_off(c, s), c.ring + s * 4096);
        vm_wait<0>();
        w_park8<0>(c); w_park8<1>(c); w_park8<2>(c); w_park8<3>(c);
        asm volatile("s_waitcnt lgkmcnt(0)" ::: "memory");
#pragma unroll
        for (int s = 6; s < 13; ++s) w_issue(s < 12 ? c.srdW2 : c.srdL, c.wv2, w2_off(c, s), c.ring + g2_slot(s) * 4096);
        if ((CW & 16) && idx == 31) touch_kb(w, c.ring + 9 * 4096, lane);
        fetch_w2(w2, W2N + ((idx - 27) * 4 + w) * 8 + xcd, 864, 160, c.ring + 9 * 4096, lane);
        pause_until(t0, KT);
        warm_w<1>(w2, b2, xcd, (idx - 27) * 4 + w, 20, c.ring + 9 * 4096, lane);
        vm_wait<0>();
        w_issue(c.srdL, c.wv2, w2_off(c, 13), c.ring + g2_slot(13) * 4096);
        asm volatile("global_load_ushort %1, %4, %5\n\tglobal_load_dword %0, %2, %3 sc1\n\ts_waitcnt vmcnt(0)"
                     : "=&v"(f), "=&v"(bq) : "v"(fo), "s"(fp), "v"(n * 2), "s"(b2 + mt2 * 64 + 16 * w) : "memory");
        STAMP(3, 6);
    }
    {
        unsigned spins = 0;
#pragma unroll 1
        for (; spins < (1u << 16); ++spins) {
            if (__all(f != 0)) break;
            asm volatile("global_load_dword %0, %1, %2 sc1\n\ts_waitcnt vmcnt(0)" : "=&v"(f) : "v"(fo), "s"(fp) : "memory");
        }
    }
    STAMP(4, 0);
    g2_first<0>(c);
    {
        const float b = bflo(bq);
        c.bias = (f32x4){b, b, b, b};
        asm volatile("" : "+v"(c.bias));
    }
    SB();
    g2_step<0>(acc, wf, c);
    STAMP(4, 2);
    f32x4 r[5];
    kb_reduce(r, acc, smem, w, lane);
    STAMPX(4, 3);
    {
        u16* op = out + (size_t)(mt2 * 64 + 16 * w + n) * D_ + dt2 * 80 + 20 * hg;
        typedef unsigned u32x4u __attribute__((ext_vector_type(4), aligned(8)));
        u32x4u o0 = {pk2(r[0][0], r[0][1]), pk2(r[0][2], r[0][3]), pk2(r[1][0], r[1][1]), pk2(r[1][2], r[1][3])};
        u32x4u o1 = {pk2(r[2][0], r[2][1]), pk2(r[2][2], r[2][3]), pk2(r[3][0], r[3][1]), pk2(r[3][2], r[3][3])};
        u32x2 o2 = {pk2(r[4][0], r[4][1]), pk2(r[4][2], r[4][3])};
        *(u32x4u*)op = o0;
        *(u32x4u*)(op + 8) = o1;
        *(u32x2*)(op + 16) = o2;
    }
    STAMP(4, 4);
    asm volatile(".Lkb_mlp_last:");
}

extern "C" void kernel_launch(void* const* d_in, const int* in_sizes, int n_in, void* d_out, int out_size,
                              void* d_ws, size_t ws_size, hipStream_t stream) {
    const u16* x = (const u16*)d_in[0];
    const u16* logA = (const u16*)d_in[1];
    const u16* Aim = (const u16*)d_in[2];
    const u16* Cre = (const u16*)d_in[3];
    const u16* Cim = (const u16*)d_in[4];
    const u16* logdt = (const u16*)d_in[5];
    const u16* dskip = (const u16*)d_in[6];
    const u16* w1 = (const u16*)d_in[8];
    const u16* b1 = (const u16*)d_in[9];
    const u16* w2 = (const u16*)d_in[10];
    const u16* b2 = (const u16*)d_in[11];
    char* ws = (char*)d_ws;
    ka_conv<<<256, 512, 0, stream>>>(x, logA, Aim, Cre, Cim, logdt, dskip, ws, w1, b1, w2, b2);
    kb_mlp<<<256, 256, 0, stream>>>(w1, b1, w2, b2, ws, (u16*)d_out);
}
```

```cpp
#include <hip/hip_runtime.h>
#include <hip/hip_bf16.h>
#include <stdint.h>
#pragma clang diagnostic ignored "-Winline-asm"

typedef unsigned short u16;
typedef __bf16 bf16x8 __attribute__((ext_vector_type(8)));
typedef __bf16 bf16x2 __attribute__((ext_vector_type(2)));
typedef short s16x4 __attribute__((ext_vector_type(4)));
typedef float f32x4 __attribute__((ext_vector_type(4)));
typedef float f32x2 __attribute__((ext_vector_type(2)));
typedef unsigned u32x4 __attribute__((ext_vector_type(4)));
typedef unsigned u32x2 __attribute__((ext_vector_type(2)));
#define LDS_AS __attribute__((address_space(3)))

#define STAMP(k, i) do { } while (0)
#define STAMPT(k, i, T) do { } while (0)

#define TS(i) do { } while (0)
#define TSPUT(k, T) do { } while (0)
#define STAMPX(k, i) do { } while (0)
#define FSTAMP STAMP
constexpr int L_ = 2048;
constexpr int D_ = 640;
constexpr int N_ = 128;
constexpr int F_ = 1728;
constexpr unsigned SZ_ = L_ * D_ * 2;
constexpr unsigned WS_HB = 0, WS_GB = SZ_, WS_END = 2 * SZ_;
constexpr unsigned WS_ID = 2 * SZ_, WS_GR = WS_ID + 64 * L_ * 2, WS_EP = WS_GR + 32768;
constexpr unsigned WS_G2 = WS_EP + 4096, WS_FL = WS_G2 + L_ * 8;

__device__ __forceinline__ unsigned gran_pos(unsigned r) { return (r >> 9) * 4096 + ((r >> 1) & 3) * 1024 + ((r >> 3) & 63) * 16 + (r & 1) * 8; }
__device__ __forceinline__ float bf2f(unsigned v) { return __uint_as_float(v << 16); }
__device__ __forceinline__ float bflo(unsigned v) { return __uint_as_float(v << 16); }
__device__ __forceinline__ float bfhi(unsigned v) { return __uint_as_float(v & 0xffff0000u); }
__device__ __forceinline__ unsigned pk2(float lo, float hi) {
    f32x2 f = {lo, hi};
    bf16x2 b = __builtin_convertvector(f, bf16x2);
    return __builtin_bit_cast(unsigned, b);
}
__device__ __forceinline__ float rb(float x) { return bflo(pk2(x, x)); }

__device__ __forceinline__ f32x4 mfma16(u32x4 a, u32x4 b, f32x4 c) {
    return __builtin_amdgcn_mfma_f32_16x16x32_bf16(__builtin_bit_cast(bf16x8, a), __builtin_bit_cast(bf16x8, b), c, 0, 0, 0);
}
__device__ __forceinline__ u32x2 tr_read(const char* p) {
    s16x4 v = __builtin_amdgcn_ds_read_tr16_b64_v4i16((LDS_AS s16x4*)p);
    return __builtin_bit_cast(u32x2, v);
}
template <int CTRL>
__device__ __forceinline__ float dpp_add(float v) {
    int t = __builtin_amdgcn_update_dpp(0, __float_as_int(v), CTRL, 0xf, 0xf, true);
    return v + __int_as_float(t);
}
__device__ __forceinline__ float row16_sum(float v) {
    v = dpp_add<0x128>(v);
    v = dpp_add<0x124>(v);
    v = dpp_add<0x122>(v);
    v = dpp_add<0x121>(v);
    return v;
}

template <int NT = 0>
__device__ __forceinline__ void touch4(__amdgpu_buffer_rsrc_t srd, unsigned voff, unsigned soff, unsigned m0v) {
    if (NT) asm volatile("s_mov_b32 m0, %0\n\ts_nop 0\n\tbuffer_load_dword %1, %2, %3 offen nt lds"
                 :: "s"(m0v), "v"(voff), "s"(srd), "s"(soff) : "m0", "memory");
    else asm volatile("s_mov_b32 m0, %0\n\ts_nop 0\n\tbuffer_load_dword %1, %2, %3 offen lds"
                 :: "s"(m0v), "v"(voff), "s"(srd), "s"(soff) : "m0", "memory");
}
#define MT2S 1
__device__ __forceinline__ int mt2_of(int p, int i) { return MT2S == 1 ? 8 * p + i : MT2S == 2 ? 16 * (p >> 1) + 2 * i + (p & 1) : 4 * i + p; }
#define W2M 0
#define W2N 864
#define XT 1
#define W1SL 2
#define KT 300
#define W1X 8
#define BAL 1
#define W2C 616
#define CW 11
#define CODE_SRD(srd, sym) do { unsigned lo_, hi_, n_; \
    asm("s_getpc_b64 vcc\n\ts_add_u32 %0, vcc_lo, " sym "@rel32@lo+4\n\ts_addc_u32 %1, vcc_hi, " sym "@rel32@hi+12\n\t" \
        "s_mov_b32 %2, .L" sym "_last-" sym : "=s"(lo_), "=s"(hi_), "=s"(n_) :: "vcc", "scc"); \
    srd = __builtin_amdgcn_make_buffer_rsrc((void*)(((unsigned long long)hi_ << 32) | lo_), (short)0, n_, 0x00020000); } while (0)
__device__ __forceinline__ void pause_until(unsigned t0, unsigned ticks) {
#pragma unroll 1
    for (int i = 0; i < 64 && (unsigned)__builtin_amdgcn_s_memrealtime() - t0 < ticks; ++i) __builtin_amdgcn_s_sleep(4);
}
__device__ __forceinline__ void fetch_w2(const u16* W, int j, int j1, int nw, unsigned lds, int lane) {
    const __amdgpu_buffer_rsrc_t srdW = __builtin_amdgcn_make_buffer_rsrc((void*)W, (short)0, L_ * F_ * 2, 0x00020000);
    for (; j < j1; j += nw) touch4(srdW, lane * 128, j * 8192, lds);
}
__device__ __forceinline__ void touch_kb(int part, unsigned lds, int lane) {
    __amdgpu_buffer_rsrc_t srdC;
    CODE_SRD(srdC, "kb_mlp");
    touch4(srdC, lane * 128, part * 8192, lds);
}
template <int MODE>
__device__ __forceinline__ void warm_w(const u16* W, const u16* bias, int xcd, int gw, int nw, unsigned lds, int lane) {
    constexpr int LDW = MODE == 0 ? F_ : L_;
    const __amdgpu_buffer_rsrc_t srdW = __builtin_amdgcn_make_buffer_rsrc((void*)W, (short)0, L_ * F_ * 2, 0x00020000);
    if (gw == 0) touch4(__builtin_amdgcn_make_buffer_rsrc((void*)bias, (short)0, LDW * 2, 0x00020000), lane * 128, 0, lds);
    const int mt0 = MODE == 0 ? ((xcd >> 1) * 27) >> 2 : mt2_of(xcd >> 1, 0);
    const unsigned voff = ((lane >> 3) * LDW + (lane & 7) * (MODE == 0 ? 64 : 64 * MT2S)) * 2;
    int r = 0;
    for (int j = gw; j < (MODE == 0 ? 256 : 224); j += nw) {
        if (MODE == 0 && r < W1SL) asm volatile("s_waitcnt vmcnt(0)" ::: "memory");
        if (MODE == 1 && (CW & 16) && r == (5 * (gw >> 2)) >> 1) touch_kb(gw & 3, lds, lane);
        ++r;
        const int s = MODE == 0 ? (j >> 4) ^ ((xcd & 1) * W1X) : j >> 4, w = (j >> 2) & 3;
        const int ks = MODE == 0 ? w * 16 + s : w * 14 + s;
        if (MODE != 0 && ks >= 54) continue;
        touch4(srdW, voff, ((ks * 32 + (j & 3) * 8) * LDW + mt0 * 64) * 2, lds);
    }
}

constexpr int KA_ROWB = 528;
constexpr int KA_LO = 32 * KA_ROWB;
constexpr int KA_HI0 = 4 * KA_LO;
constexpr int KA_HI = 13312;
constexpr int KA_MB = 20000;
constexpr int CV_COPY = 4160;
constexpr int CV_UB = 4608;
constexpr int KA_U = KA_HI0 + 4 * KA_HI;
#define XC1 4
#define XCS 3
#define IB 1
#define BG 64
#define FE 2
#define PPS 1
#define ROT 0
#define NX0 4
#define GFIRST " sc1"
typedef __attribute__((address_space(1))) unsigned gu32;

__device__ __forceinline__ void cm4(f32x2& r0, f32x2 a0, f32x2 b0, f32x2& r1, f32x2 a1, f32x2 b1,
                                    f32x2& r2, f32x2 a2, f32x2 b2, f32x2& r3, f32x2 a3, f32x2 b3) {
    asm("v_pk_mul_f32 %0, %4, %5 op_sel:[0,0] op_sel_hi:[1,0]\n\t"
        "v_pk_mul_f32 %1, %6, %7 op_sel:[0,0] op_sel_hi:[1,0]\n\t"
        "v_pk_mul_f32 %2, %8, %9 op_sel:[0,0] op_sel_hi:[1,0]\n\t"
        "v_pk_mul_f32 %3, %10, %11 op_sel:[0,0] op_sel_hi:[1,0]\n\t"
        "v_pk_fma_f32 %0, %4, %5, %0 op_sel:[1,1,0] op_sel_hi:[0,1,1] neg_lo:[1,0,0]\n\t"
        "v_pk_fma_f32 %1, %6, %7, %1 op_sel:[1,1,0] op_sel_hi:[0,1,1] neg_lo:[1,0,0]\n\t"
        "v_pk_fma_f32 %2, %8, %9, %2 op_sel:[1,1,0] op_sel_hi:[0,1,1] neg_lo:[1,0,0]\n\t"
        "v_pk_fma_f32 %3, %10, %11, %3 op_sel:[1,1,0] op_sel_hi:[0,1,1] neg_lo:[1,0,0]"
        : "=&v"(r0), "=&v"(r1), "=&v"(r2), "=&v"(r3)
        : "v"(a0), "v"(b0), "v"(a1), "v"(b1), "v"(a2), "v"(b2), "v"(a3), "v"(b3));
}
constexpr int ka_off(int r) { return r < 32 ? r * KA_ROWB : KA_HI0 - KA_MB + (r - 32) * KA_ROWB; }
template <int R0, int R1, int R2, int R3>
__device__ __forceinline__ void put4(const unsigned (&m0v)[2], f32x2 v0, f32x2 v1, f32x2 v2, f32x2 v3) {
    static_assert((R0 < 32) == (R1 < 32) && (R0 < 32) == (R2 < 32) && (R0 < 32) == (R3 < 32), "");
    unsigned t0, t1, t2, t3;
    asm volatile("s_mov_b32 m0, %4\n\t"
                 "v_cvt_pk_bf16_f32 %0, %5, %6\n\tv_cvt_pk_bf16_f32 %1, %7, %8\n\t"
                 "v_cvt_pk_bf16_f32 %2, %9, %10\n\tv_cvt_pk_bf16_f32 %3, %11, %12\n\t"
                 "ds_write_addtid_b32 %0 offset:%13\n\tds_write_addtid_b32 %1 offset:%14\n\t"
                 "ds_write_addtid_b32 %2 offset:%15\n\tds_write_addtid_b32 %3 offset:%16"
                 : "=&v"(t0), "=&v"(t1), "=&v"(t2), "=&v"(t3)
                 : "s"(m0v[R0 >= 32]), "v"(v0.x), "v"(v0.y), "v"(v1.x), "v"(v1.y), "v"(v2.x), "v"(v2.y), "v"(v3.x), "v"(v3.y),
                   "n"(ka_off(R0)), "n"(ka_off(R1)), "n"(ka_off(R2)), "n"(ka_off(R3))
                 : "m0", "memory");
}
__device__ __forceinline__ f32x2 cexp_p(float zr, float zi, float p) {
    const float mag = __builtin_amdgcn_exp2f(zr * p);
    const float rev = __builtin_amdgcn_fractf(zi * p);
    f32x2 r;
    r.x = mag * __builtin_amdgcn_cosf(rev);
    r.y = mag * __builtin_amdgcn_sinf(rev);
    return r;
}
__device__ __forceinline__ unsigned next_lane(unsigned v, unsigned last) {
    return (unsigned)__builtin_amdgcn_update_dpp((int)last, (int)v, 0x130, 0xf, 0xf, false);
}
__device__ __forceinline__ void put_copies(char* p, unsigned r0, unsigned r1, unsigned r2, unsigned r3, unsigned r4, unsigned r5) {
    const unsigned s0 = __builtin_amdgcn_alignbit(r1, r0, 16), s1 = __builtin_amdgcn_alignbit(r2, r1, 16),
                   s2 = __builtin_amdgcn_alignbit(r3, r2, 16), s3 = __builtin_amdgcn_alignbit(r4, r3, 16),
                   s4 = __builtin_amdgcn_alignbit(r5, r4, 16);
    *(u32x4*)(p + 0 * CV_COPY) = (u32x4){r0, r1, r2, r3};
    *(u32x4*)(p + 1 * CV_COPY) = (u32x4){s0, s1, s2, s3};
    *(u32x4*)(p + 2 * CV_COPY) = (u32x4){r1, r2, r3, r4};
    *(u32x4*)(p + 3 * CV_COPY) = (u32x4){s1, s2, s3, s4};
}
template <int O> __device__ __forceinline__ void rd64(u32x2& v, unsigned a) {
    asm volatile("ds_read_b64 %0, %1 offset:%2" : "=v"(v) : "v"(a), "n"(O) : "memory");
}
template <int O> __device__ __forceinline__ void rd128(u32x4& v, unsigned a) {
    asm volatile("ds_read_b128 %0, %1 offset:%2" : "=v"(v) : "v"(a), "n"(O) : "memory");
}
struct CvFrag { u32x2 lo[8], hi[8]; u32x4 b[8]; };
__device__ __forceinline__ void cv_wait(CvFrag& f) {
    asm volatile("s_waitcnt lgkmcnt(0)"
                 : "+v"(f.lo[0]), "+v"(f.lo[1]), "+v"(f.lo[2]), "+v"(f.lo[3]), "+v"(f.lo[4]), "+v"(f.lo[5]), "+v"(f.lo[6]), "+v"(f.lo[7]),
                   "+v"(f.hi[0]), "+v"(f.hi[1]), "+v"(f.hi[2]), "+v"(f.hi[3]), "+v"(f.hi[4]), "+v"(f.hi[5]), "+v"(f.hi[6]), "+v"(f.hi[7]),
                   "+v"(f.b[0]), "+v"(f.b[1]), "+v"(f.b[2]), "+v"(f.b[3]), "+v"(f.b[4]), "+v"(f.b[5]), "+v"(f.b[6]), "+v"(f.b[7])
                 :: "memory");
}
template <int T>
__device__ __forceinline__ void cv_mma(const CvFrag& f, f32x4 (&acc)[8]) {
    constexpr int q = T < 1 ? 0 : T < 3 ? 1 : T < 6 ? 2 : T < 10 ? 3 : T < 15 ? 4 : T < 21 ? 5 : T < 28 ? 6 : 7;
    constexpr int g1 = T - q * (q + 1) / 2;
    const u32x4 a = {f.lo[g1].x, f.lo[g1].y, f.hi[g1].x, f.hi[g1].y};
    acc[q] = mfma16(a, f.b[q - g1], acc[q]);
}
template <int G0, int R>
__device__ __forceinline__ void cv_read(CvFrag& f, unsigned aa, unsigned ba) {
    constexpr int G1 = R / 3;
    if constexpr (R % 3 == 0) rd64<(63 - 8 * G1 - G0) * 64>(f.lo[G1], aa);
    else if constexpr (R % 3 == 1) rd64<(63 - 8 * G1 - G0) * 64 + 8>(f.hi[G1], aa);
    else rd128<(8 * G1 - G0 + 7) * 64>(f.b[G1], ba);
}
template <int G0, int T>
__device__ __forceinline__ void cv_group(const CvFrag& cur, CvFrag& nxt, f32x4 (&acc)[8], unsigned aa, unsigned ba) {
    cv_mma<T>(cur, acc);
    __builtin_amdgcn_sched_barrier(0);
    if constexpr (G0 < 7 && T < 30 && T % 5 != 4) {
        cv_read<G0 + 1, T - T / 5>(nxt, aa, ba);
        __builtin_amdgcn_sched_barrier(0);
    }
    if constexpr (T < 35) cv_group<G0, T + 1>(cur, nxt, acc, aa, ba);
}
template <int G0>
__device__ __forceinline__ void cv_round(CvFrag& cur, CvFrag& nxt, f32x4 (&acc)[8], unsigned aa, unsigned ba) {
    cv_wait(cur);
    if constexpr (G0 == 5) __builtin_amdgcn_s_barrier();
    __builtin_amdgcn_sched_barrier(0);
    cv_group<G0, 0>(cur, nxt, acc, aa, ba);
    if constexpr (G0 < 7) cv_round<G0 + 1>(nxt, cur, acc, aa, ba);
}
template <int I>
__device__ __forceinline__ void cv_first(CvFrag& f, unsigned aa, unsigned ba) {
    cv_read<0, I>(f, aa, ba);
    if constexpr (I < 23) cv_first<I + 1>(f, aa, ba);
}

template <class Hook>
__device__ __forceinline__ void ka_chain(const unsigned (&m0v)[2], float p_la, float p_ai, float p_cr, float p_ci, float p_dt, Hook hook) {
    const float dt = rb(__builtin_amdgcn_exp2f(p_dt * 1.44269504088896341f));
    const float ar = -rb(__builtin_amdgcn_exp2f(p_la * 1.44269504088896341f));
    const float zr = ar * dt * 1.44269504088896341f, zi = p_ai * dt * 0.15915494309189535f;
    f32x2 a[17], b[16], e[8], F1, F2, F4, F8, r0, r1, r2, r3;
    a[0] = (f32x2){2.0f * (p_cr * dt), -2.0f * (p_ci * dt)};
    e[1] = cexp_p(zr, -zi, 1.f);
    b[1] = cexp_p(zr, zi, 8.f);
    F1 = cexp_p(zr, -zi, 128.f);
    cm4(e[2], e[1], e[1], b[2], b[1], b[1], F2, F1, F1, a[1], a[0], F1);
    cm4(e[4], e[2], e[2], b[4], b[2], b[2], F4, F2, F2, e[3], e[2], e[1]);
    put4<41, 42, 43, 45>(m0v, (f32x2){1.f, 0.f}, b[1], b[2], b[4]);
    hook(0);
    cm4(a[2], a[0], F2, a[3], a[1], F2, b[3], b[2], b[1], r0, a[0], e[1]);
    cm4(F8, F4, F4, b[8], b[4], b[4], r1, a[1], e[1], r2, a[0], e[2]);
    put4<16, 20, 1, 9>(m0v, a[2], a[3], r0, r1);
    hook(1);
    cm4(a[4], a[0], F4, a[5], a[1], F4, a[6], a[2], F4, a[7], a[3], F4);
    put4<24, 26, 28, 30>(m0v, a[4], a[5], a[6], a[7]);
    hook(2);
    cm4(e[5], e[4], e[1], e[6], e[4], e[2], e[7], e[4], e[3], r3, a[1], e[2]);
    put4<0, 8, 2, 10>(m0v, a[0], a[1], r2, r3);
    hook(3);
    cm4(b[5], b[4], b[1], b[6], b[4], b[2], b[7], b[4], b[3], r0, a[0], e[3]);
    put4<44, 49, 46, 47>(m0v, b[3], b[8], b[5], b[6]);
    hook(4);
    cm4(a[8], a[0], F8, a[9], a[1], F8, a[10], a[2], F8, a[11], a[3], F8);
    put4<32, 33, 34, 35>(m0v, a[8], a[9], a[10], a[11]);
    hook(5);
    cm4(a[12], a[4], F8, a[13], a[5], F8, a[14], a[6], F8, a[15], a[7], F8);
    put4<36, 37, 38, 39>(m0v, a[12], a[13], a[14], a[15]);
    hook(6);
    cm4(a[16], a[8], F8, r1, a[1], e[3], r2, a[0], e[4], r3, a[1], e[4]);
    put4<3, 11, 4, 12>(m0v, r0, r1, r2, r3);
    hook(7);
    cm4(b[9], b[8], b[1], b[10], b[8], b[2], b[11], b[8], b[3], b[12], b[8], b[4]);
    put4<50, 51, 52, 53>(m0v, b[9], b[10], b[11], b[12]);
    hook(8);
    cm4(b[13], b[8], b[5], b[14], b[8], b[6], b[15], b[8], b[7], r0, a[0], e[5]);
    put4<54, 55, 56, 48>(m0v, b[13], b[14], b[15], b[7]);
    asm volatile("ds_write_addtid_b32 %0 offset:%1" :: "v"(pk2(a[16].x, a[16].y)), "n"(ka_off(40)) : "memory");
    hook(9);
    cm4(r1, a[1], e[5], r2, a[0], e[6], r3, a[1], e[6], F1, a[0], e[7]);
    put4<5, 13, 6, 14>(m0v, r0, r1, r2, r3);
    hook(10);
    cm4(r0, a[1], e[7], r1, a[2], e[2], r2, a[3], e[2], F2, a[2], e[4]);
    put4<7, 15, 17, 21>(m0v, F1, r0, r1, r2);
    hook(11);
    cm4(r3, a[3], e[4], F1, a[2], e[6], r0, a[3], e[6], r1, a[4], e[4]);
    put4<18, 22, 19, 23>(m0v, F2, r3, F1, r0);
    hook(12);
    cm4(r2, a[5], e[4], r3, a[6], e[4], F1, a[7], e[4], F2, a[7], e[4]);
    put4<25, 27, 29, 31>(m0v, r1, r2, r3, F1);
    asm volatile("s_waitcnt lgkmcnt(0)" ::: "memory");
}
__device__ __forceinline__ void unpack4(float (&o)[4], u32x2 v) {
    asm volatile("v_lshlrev_b32 %0, 16, %4\n\tv_and_b32 %1, 0xffff0000, %4\n\tv_lshlrev_b32 %2, 16, %5\n\tv_and_b32 %3, 0xffff0000, %5"
                 : "=&v"(o[0]), "=&v"(o[1]), "=&v"(o[2]), "=&v"(o[3]) : "v"(v.x), "v"(v.y));
}

extern "C" __global__ __launch_bounds__(512, 2) void ka_conv(const u16* __restrict__ x, const u16* __restrict__ logA,
                                                  const u16* __restrict__ Aim, const u16* __restrict__ Cre,
                                                  const u16* __restrict__ Cim, const u16* __restrict__ logdt,
                                                  const u16* __restrict__ dskip, char* ws,
                                                  const u16* __restrict__ w1, const u16* __restrict__ b1,
                                                  const u16* __restrict__ w2, const u16* __restrict__ b2) {
    __shared__ __attribute__((aligned(16))) char smem[KA_U + 4 * CV_UB + 256];
    const int tid = threadIdx.x, lane = tid & 63;
    const int w = __builtin_amdgcn_readfirstlane(tid >> 6);
    STAMP(0, 0);
    if ((CW & 1) && w >= 6) {
        __amdgpu_buffer_rsrc_t srdC;
        CODE_SRD(srdC, "ka_conv");
        touch4(srdC, lane * 128, (w - 6) * 8192, (unsigned)(size_t)(LDS_AS char*)(smem + KA_U + 4 * CV_UB));
    }
    asm volatile("" :: "s"(x), "s"(logA), "s"(Aim), "s"(Cre), "s"(Cim), "s"(logdt), "s"(dskip), "s"(ws));
    const int xcd = blockIdx.x & 7, idx = ((blockIdx.x >> 3) + ROT) & 31;
    const int bid = idx * 8 + xcd;
    const bool conv = idx < 20;
    const int grp = ((xcd & 1) * 4 + (xcd >> 1)) * 20 + (conv ? idx : 0);
    const int d0 = grp * 4;
    const int sw = conv ? 512 : w * 96 + xcd * 12 + idx - 20;
    const int row = (sw & 511) * 4;
    u32x4 sv[5];
    unsigned epoch, pp[5];
    float pf[5];
    asm volatile("s_cmpk_ge_u32 %21, 512\n\ts_cbranch_scc1 .Lka_s%=\n\t"
                 "global_load_dwordx4 %0, %11, %12\n\tglobal_load_dwordx4 %1, %11, %12 offset:256\n\t"
                 "global_load_dwordx4 %2, %11, %12 offset:512\n\tglobal_load_dwordx4 %3, %11, %12 offset:768\n\t"
                 "global_load_dwordx4 %4, %11, %12 offset:1024\n"
                 ".Lka_s%=:\n\tglobal_load_dword %5, %13, %14\n\t"
                 "s_cmp_ge_u32 %22, 20\n\ts_cbranch_scc1 .Lka_p%=\n\t"
                 "global_load_ushort %6, %15, %16\n\tglobal_load_ushort %7, %15, %17\n\t"
                 "global_load_ushort %8, %15, %18\n\tglobal_load_ushort %9, %15, %19\n\t"
                 "global_load_ushort %10, %13, %20\n.Lka_p%=:"
                 : "=&v"(sv[0]), "=&v"(sv[1]), "=&v"(sv[2]), "=&v"(sv[3]), "=&v"(sv[4]), "=&v"(epoch),
                   "=&v"(pp[0]), "=&v"(pp[1]), "=&v"(pp[2]), "=&v"(pp[3]), "=&v"(pp[4])
                 : "v"((lane >> 4) * (D_ * 2) + (lane & 15) * 16), "s"(x + (size_t)row * D_), "v"(0), "s"(ws + WS_EP),
                   "v"((d0 * N_ + tid) * 2), "s"(logA), "s"(Aim), "s"(Cre), "s"(Cim), "s"(logdt + d0 + (w >> 1)), "s"(sw), "s"(idx)
                 : "scc", "memory");
    const int t = tid & 255;
    const __amdgpu_buffer_rsrc_t srdG = __builtin_amdgcn_make_buffer_rsrc((void*)(ws + WS_GR), (short)0, L_ * 8, 0x00020000);
    const __amdgpu_buffer_rsrc_t srdX = __builtin_amdgcn_make_buffer_rsrc((void*)x, (short)0, SZ_, 0x00020000);
    {
        const int tw = XT == 1 ? (BAL ? (conv ? 64 : (7 - w) * 12 + idx - 20) : (idx - 20) * 8 + w) : XT == 2 ? (w < 4 ? idx * 4 + w : 64) : XT == 3 ? (w < 2 ? idx * 2 + w : 64) : 64;
        touch4(srdX, (lane >> 1) * (D_ * 2) + (lane & 1) * 156, (unsigned)tw < 64u ? tw * (32 * D_ * 2) + (grp / 20) * 160 : SZ_,
               (unsigned)(size_t)(LDS_AS char*)(smem + KA_U + 4 * CV_UB));
    }
    const int owner = conv ? w >> 2 : 0;
    u32x2 xc[8], xd;
    asm volatile("s_barrier\n\ts_cmp_eq_u32 %9, 0\n\ts_cbranch_scc1 .Lka_a%=\n\t"
                 "global_load_dwordx2 %8, %10, %11\n\t"
                 "buffer_load_dwordx2 %0, %12, %13, %14 offen\n\tbuffer_load_dwordx2 %1, %12, %13, %14 offen offset:1280\n\t"
                 "buffer_load_dwordx2 %2, %12, %13, %14 offen offset:2560\n\tbuffer_load_dwordx2 %3, %12, %13, %14 offen offset:3840\n\t"
                 "s_waitcnt vmcnt(%16)\n\ts_branch .Lka_b%=\n"
                 ".Lka_a%=:\n\ts_waitcnt vmcnt(1)\n"
                 ".Lka_b%=:"
                 : "=&v"(xc[0]), "=&v"(xc[1]), "=&v"(xc[2]), "=&v"(xc[3]), "=&v"(xc[4]), "=&v"(xc[5]), "=&v"(xc[6]), "=&v"(xc[7]), "=&v"(xd)
                 : "s"(owner), "v"(0), "s"(dskip + d0), "v"(t * (8 * D_ * 2)), "s"(srdX), "s"(d0 * 2), "s"(d0 * 2 + 4 * D_ * 2), "n"(NX0 + 2)
                 : "scc", "memory");
    asm volatile("v_lshlrev_b32 %0, 16, %5\n\tv_lshlrev_b32 %1, 16, %6\n\tv_lshlrev_b32 %2, 16, %7\n\t"
                 "v_lshlrev_b32 %3, 16, %8\n\tv_lshlrev_b32 %4, 16, %9"
                 : "=&v"(pf[0]), "=&v"(pf[1]), "=&v"(pf[2]), "=&v"(pf[3]), "=&v"(pf[4])
                 : "v"(pp[0]), "v"(pp[1]), "v"(pp[2]), "v"(pp[3]), "v"(pp[4]));
    unsigned etag;
    asm volatile("v_xor_b32 %0, 0x9e3779b9, %1" : "=v"(etag) : "v"(epoch));
    const unsigned tag = etag * 0x85ebca6bu;
    if (sw < 512) {
        float v[10][4];
#pragma unroll
        for (int j = 0; j < 5; ++j) {
            unpack4(v[2 * j], (u32x2){sv[j].x, sv[j].y});
            unpack4(v[2 * j + 1], (u32x2){sv[j].z, sv[j].w});
        }
        float s[4];
#pragma unroll
        for (int c = 0; c < 4; ++c)
            s[c] = (((v[0][c] + v[1][c]) + (v[2][c] + v[3][c])) + ((v[4][c] + v[5][c]) + (v[6][c] + v[7][c]))) + (v[8][c] + v[9][c]);
        const float mean = row16_sum((s[0] + s[1]) + (s[2] + s[3])) * (1.0f / D_);
        float q[4] = {0.f, 0.f, 0.f, 0.f};
#pragma unroll
        for (int j = 0; j < 10; ++j)
#pragma unroll
            for (int c = 0; c < 4; ++c) {
                const float a = v[j][c] - mean;
                q[c] = __builtin_fmaf(a, a, q[c]);
            }
        const float qs = row16_sum((q[0] + q[1]) + (q[2] + q[3]));
        const float den = __builtin_amdgcn_sqrtf(rb(rb(qs * (1.0f / D_)) + 1e-5f));
        asm volatile("buffer_store_dwordx2 %0, %1, %2, %3 offen sc1"
                     :: "v"((u32x2){pk2(mean, den), tag}), "v"((lane & 15) * (L_ * 8) + gran_pos(lane >> 4)), "s"(srdG),
                        "s"(gran_pos(row)) : "memory");
    }
    TS(0);
    if (!conv) {
        if (IB) asm volatile("s_barrier" ::: "memory");
        STAMP(0, 3);
        const unsigned lds = (unsigned)(size_t)(LDS_AS char*)(smem + w * 1024);
        if ((CW & 2) && idx == 28 && w < 3) touch_kb(w, lds, lane);
        const int gw = BAL ? w * 12 + idx - 20 : (idx - 20) * 8 + w;
        warm_w<0>(w1, b1, xcd, gw, 96, lds, lane);
        if (W2M & 1) asm volatile("s_waitcnt vmcnt(0)" ::: "memory");
        fetch_w2(w2, W2C + (95 - gw) * 8 + xcd, W2N, 768, lds, lane);
        if ((CW & 8) && idx == 27 && w >= 5) touch_kb(w - 5, lds, lane);
        if (idx == 31 && tid < 256) {
            const int r = xcd * 8 + (tid >> 5), c = tid & 31;
            *(unsigned*)(ws + WS_ID + r * (L_ * 2) + 4 * c) = (r >> 1) == c ? ((r & 1) ? 0x3f800000u : 0x3f80u) : 0u;
        }
        if (idx == 30 && tid < 128) *(u32x4*)(ws + WS_G2 + (xcd * 128 + tid) * 16) = (u32x4){0u, 0u, 0u, 0u};
        if (idx == 29 && tid < 32) *(unsigned*)(ws + WS_FL + (xcd * 32 + tid) * 128) = 0u;
        asm volatile("s_waitcnt vmcnt(0)" ::: "memory");
        if (xcd == 0 && gw == BG) {
#pragma unroll 1
            for (unsigned spins = 0; spins < (1u << 16); ++spins) {
                u32x4 a;
                asm volatile("global_load_dwordx4 %0, %1, %2 sc1\n\ts_waitcnt vmcnt(0)"
                             : "=&v"(a) : "v"((lane < 40 ? lane : 0) * 16), "s"(ws + WS_EP + 1024) : "memory");
                if (!__any(((a.x ^ tag) | (a.y ^ tag) | (a.z ^ tag) | (a.w ^ tag)) != 0)) break;
            }
            if (lane == 0) __hip_atomic_store((gu32*)(ws + WS_EP), (etag ^ 0x9e3779b9u) + 1u, __ATOMIC_RELAXED, __HIP_MEMORY_SCOPE_AGENT);
        }
        STAMP(0, 7);
        return;
    }
    const unsigned lds0 = (unsigned)(size_t)(LDS_AS char*)smem;
    const int m = lane & 15, hg = lane >> 4;
    char* mine = smem + (w & 3) * KA_LO;
    char* minh = smem + KA_HI0 + (w & 3) * KA_HI;
    const unsigned m0v[2] = {lds0 + (w >> 1) * KA_LO + (w & 1) * 256, lds0 + KA_MB + (w >> 1) * KA_HI + (w & 1) * 256};
    ka_chain(m0v, pf[0], pf[1], pf[2], pf[3], pf[4], [&](int i) {
        if (i % XCS == 0 && NX0 + i / XCS < 8)
            asm volatile("s_cmp_eq_u32 %1, 0\n\ts_cbranch_scc1 .Lka_c%=\n\tbuffer_load_dwordx2 %0, %2, %3, %4 offen offset:%5\n.Lka_c%=:"
                         : "=v"(xc[(NX0 + i / XCS) & 7])
                         : "s"(owner), "v"(t * (8 * D_ * 2)), "s"(srdX), "s"(d0 * 2 + 4 * D_ * 2), "n"(((NX0 - 4 + i / XCS) & 3) * (D_ * 2)) : "scc", "memory");
    });
    TS(1);
    __builtin_amdgcn_s_barrier();
    if (w < 4) {
        TS(2);
        u16* kd = (u16*)(minh + 8192);
        {
            const char* fb = mine + m * KA_ROWB + hg * 16;
            const char* fh = minh + m * KA_ROWB + hg * 16;
            u32x4 fa[8][3], fl[8];
#pragma unroll
            for (int ks = 0; ks < 8; ++ks) {
                fl[ks] = *(const u32x4*)(fh + 9 * KA_ROWB + ks * 64);
                fa[ks][0] = *(const u32x4*)(fb + ks * 64);
                fa[ks][1] = *(const u32x4*)(fb + 16 * KA_ROWB + ks * 64);
                fa[ks][2] = *(const u32x4*)(fh + ks * 64);
            }
            __builtin_amdgcn_sched_barrier(0);
            f32x4 acc0 = {0.f, 0.f, 0.f, 0.f}, acc1 = acc0, acc2 = acc0;
#pragma unroll
            for (int ks = 0; ks < 8; ++ks) {
                acc0 = mfma16(fa[ks][0], fl[ks], acc0);
                acc1 = mfma16(fa[ks][1], fl[ks], acc1);
                acc2 = mfma16(fa[ks][2], fl[ks], acc2);
            }
            const int c = m;
            *(u32x2*)(kd + 128 * (hg >> 1) + 4 * (hg & 1) + 8 * c) = (u32x2){pk2(acc0[0], acc0[1]), pk2(acc0[2], acc0[3])};
            const unsigned p01 = pk2(acc1[0], acc1[1]), p23 = pk2(acc1[2], acc1[3]);
            const int i0 = hg < 2 ? 256 + 64 * hg + 4 * c : 256 + 64 * hg + 2 * c;
            *(unsigned*)(kd + i0) = p01;
            *(unsigned*)(kd + i0 + (hg < 2 ? 2 : 32)) = p23;
            const unsigned q01 = pk2(acc2[0], acc2[1]), q23 = pk2(acc2[2], acc2[3]);
            kd[512 + 64 * hg + c] = (u16)(q01 & 0xffff);
            kd[528 + 64 * hg + c] = (u16)(q01 >> 16);
            kd[544 + 64 * hg + c] = (u16)(q23 & 0xffff);
            kd[560 + 64 * hg + c] = (u16)(q23 >> 16);
            asm volatile("" ::: "memory");
        }
        const unsigned* kw = (const unsigned*)kd;
        const unsigned dw0 = kw[319 - lane], dw1 = kw[320 - lane];
        const unsigned cw0 = kw[255 - lane], cw1 = kw[256 - lane];
        const bool isA = lane >= 32;
        const char* bp = (const char*)kd + (isA ? 16 * (63 - lane) : 256 + 8 * (63 - lane));
        const u32x2 b01 = *(const u32x2*)bp, b23 = *(const u32x2*)(bp + 8);
        const unsigned P = __builtin_amdgcn_perm(0, dw1, 0x01000100), Q = __builtin_amdgcn_perm(0, dw0, 0x03020302),
                       M = __builtin_amdgcn_perm(0, dw0, 0x01000302), Z = __builtin_amdgcn_perm(0, dw0, 0x01000100);
        const unsigned c0 = __builtin_amdgcn_perm(0, cw1, 0x01000100), c1 = __builtin_amdgcn_perm(0, cw0, 0x03020302),
                       c2 = __builtin_amdgcn_perm(0, cw0, 0x01000302), c3 = __builtin_amdgcn_perm(0, cw0, 0x01000100);
        const unsigned o0 = __builtin_amdgcn_perm(b23.x, isA ? b23.y : b01.y, isA ? 0x01000302u : 0x03020504u),
                       o1 = __builtin_amdgcn_perm(b23.x, b01.y, isA ? 0x05040706u : 0x01000100u),
                       o2 = __builtin_amdgcn_perm(b01.y, b01.x, isA ? 0x05040706u : 0x03020504u),
                       o3 = __builtin_amdgcn_perm(0, b01.x, isA ? 0x01000302u : 0x01000100u);
        put_copies(mine + 3072 + lane * 16, o0, o1, o2, o3, next_lane(o0, 0), next_lane(o1, 0));
        put_copies(mine + 2048 + lane * 16, c0, c1, c2, c3, next_lane(c0, __builtin_amdgcn_readlane(o0, 0)),
                   next_lane(c1, __builtin_amdgcn_readlane(o1, 0)));
        put_copies(mine + lane * 32, P, P, Q, Q, Q, M);
        put_copies(mine + lane * 32 + 16, Q, M, Z, Z, next_lane(P, __builtin_amdgcn_readlane(c0, 0)),
                   next_lane(P, __builtin_amdgcn_readlane(c1, 0)));
        if (lane < 8) *(u32x4*)(mine + (lane >> 1) * CV_COPY + 4096 + (lane & 1) * 16) = (u32x4){0u, 0u, 0u, 0u};
        asm volatile("s_waitcnt vmcnt(0)" ::: "memory");
        TS(5);
        __syncthreads();
        TS(6);
        const unsigned aa = (unsigned)(size_t)(LDS_AS char*)(mine + (3 - (m & 3)) * CV_COPY + 2 * (2028 - 32 * 63 - 4 * (m >> 2) + 8 * hg));
        const unsigned ba = (unsigned)(size_t)(LDS_AS char*)(smem + KA_U + w * CV_UB + (2 * m + hg) * 16);
        f32x4 acc[8];
#pragma unroll
        for (int q = 0; q < 8; ++q) acc[q] = (f32x4){0.f, 0.f, 0.f, 0.f};
        CvFrag f0, f1;
        cv_first<0>(f0, aa, ba);
        cv_round<0>(f0, f1, acc, aa, ba);
        TS(7);
        const char* rp = minh + (16 * m + 4 * hg) * 4;
        char* hp = mine + (16 * m + 4 * hg) * 2;
#pragma unroll
        for (int q = 0; q < 8; ++q) {
            const f32x4 h = acc[q] + *(const f32x4*)(rp + q * 1024);
            *(u32x2*)(hp + q * 512) = (u32x2){pk2(h[0], h[1]), pk2(h[2], h[3])};
        }
    } else {
        u32x4 g[4];
        asm volatile("buffer_load_dwordx4 %0, %4, %5, 0 offen" GFIRST "\n\tbuffer_load_dwordx4 %1, %4, %5, 0 offen offset:1024" GFIRST "\n\t"
                     "buffer_load_dwordx4 %2, %4, %5, 0 offen offset:2048" GFIRST "\n\tbuffer_load_dwordx4 %3, %4, %5, 0 offen offset:3072" GFIRST "\n\t"
                     "s_waitcnt vmcnt(4)"
                     : "=&v"(g[0]), "=&v"(g[1]), "=&v"(g[2]), "=&v"(g[3]) : "v"(gran_pos(8 * t)), "s"(srdG) : "memory");
        float xf[8][4], dk[4];
#pragma unroll
        for (int k = 0; k < 8; ++k) unpack4(xf[k], xc[k]);
        unpack4(dk, xd);
        if (lane < 30) *(u32x4*)(smem + KA_U + (w - 4) * CV_UB + lane * 16) = (u32x4){0u, 0u, 0u, 0u};
        TS(3);
        asm volatile("s_waitcnt vmcnt(0)" : "+v"(g[0]), "+v"(g[1]), "+v"(g[2]), "+v"(g[3]) :: "memory");
        unsigned spins = 0;
#pragma unroll 1
        for (; spins < (1u << 16); ++spins) {
            const bool bad = ((g[0].y ^ tag) | (g[0].w ^ tag) | (g[1].y ^ tag) | (g[1].w ^ tag) |
                              (g[2].y ^ tag) | (g[2].w ^ tag) | (g[3].y ^ tag) | (g[3].w ^ tag)) != 0;
            if (!__any(bad)) break;
            if (bad)
                asm volatile("buffer_load_dwordx4 %0, %4, %5, 0 offen sc1\n\tbuffer_load_dwordx4 %1, %4, %5, 0 offen offset:1024 sc1\n\t"
                             "buffer_load_dwordx4 %2, %4, %5, 0 offen offset:2048 sc1\n\tbuffer_load_dwordx4 %3, %4, %5, 0 offen offset:3072 sc1\n\t"
                             "s_waitcnt vmcnt(0)"
                             : "+v"(g[0]), "+v"(g[1]), "+v"(g[2]), "+v"(g[3]) : "v"(gran_pos(8 * t)), "s"(srdG) : "memory");
        }
        TS(4);
        if (FE == 1) fetch_w2(w2, (w - 4) * 160 + bid, W2C, 640, lds0 + KA_U + 4 * CV_UB, lane);
        unsigned up[4][4];
#pragma unroll
        for (int k = 0; k < 4; ++k) {
            const float i0 = __builtin_amdgcn_rcpf(bfhi(g[k].x)), i1 = __builtin_amdgcn_rcpf(bfhi(g[k].z));
            const float m0 = bflo(g[k].x), m1 = bflo(g[k].z);
#pragma unroll
            for (int c = 0; c < 4; ++c) up[c][k] = pk2((xf[2 * k][c] - m0) * i0, (xf[2 * k + 1][c] - m1) * i1);
        }
#pragma unroll
        for (int c = 0; c < 4; ++c)
            *(u32x4*)(smem + KA_U + c * CV_UB + 480 + t * 16) = (u32x4){up[c][0], up[c][1], up[c][2], up[c][3]};
        if (FE == 2) fetch_w2(w2, (w - 4) * 160 + bid, W2C, 640, lds0 + KA_U + 4 * CV_UB, lane);
        TS(5);
        asm volatile("s_waitcnt lgkmcnt(0)\n\ts_barrier" ::: "memory");
        if (tid == 256)
            __hip_atomic_store((gu32*)(ws + WS_EP + 1024 + bid * 4), tag, __ATOMIC_RELAXED, __HIP_MEMORY_SCOPE_AGENT);
        if (FE == 3) fetch_w2(w2, (w - 4) * 160 + bid, W2C, 640, lds0 + KA_U + 4 * CV_UB, lane);
#pragma unroll
        for (int c = 0; c < 4; ++c) {
            f32x4 r0, r1;
#pragma unroll
            for (int k = 0; k < 2; ++k) {
                r0[2 * k] = __builtin_fmaf(bflo(up[c][k]), dk[c], xf[2 * k][c]);
                r0[2 * k + 1] = __builtin_fmaf(bfhi(up[c][k]), dk[c], xf[2 * k + 1][c]);
                r1[2 * k] = __builtin_fmaf(bflo(up[c][2 + k]), dk[c], xf[4 + 2 * k][c]);
                r1[2 * k + 1] = __builtin_fmaf(bfhi(up[c][2 + k]), dk[c], xf[5 + 2 * k][c]);
            }
            *(f32x4*)(smem + KA_HI0 + c * KA_HI + t * 32) = r0;
            *(f32x4*)(smem + KA_HI0 + c * KA_HI + t * 32 + 16) = r1;
        }
        if (FE == 0) fetch_w2(w2, (w - 4) * 160 + bid, W2C, 640, lds0 + KA_U + 4 * CV_UB, lane);
        asm volatile("s_waitcnt lgkmcnt(0)" ::: "memory");
        __builtin_amdgcn_s_barrier();
        asm volatile("s_waitcnt vmcnt(0)" ::: "memory");
    }
    __syncthreads();
    {
        const int ch = tid & 3;
        const int dt = grp / 20, rem = grp % 20, mq = rem / 5, j = rem % 5;
#pragma unroll
        for (int i = 0; i < 2; ++i) {
            const int piece = (tid >> 2) + 128 * i;
            const int unit = ((piece >> 2) * 40 + dt * 5 + j) * 64 + (piece & 3) * 16 + 4 * mq + ch;
            *(u32x4*)(ws + WS_HB + (size_t)unit * 16) = *(const u32x4*)(smem + ch * KA_LO + piece * 16);
        }
    }
    STAMP(0, 6);
    TSPUT(1, 0);
    TSPUT(5, 448);
    asm volatile(".Lka_conv_last:");
}

constexpr int B_REG = 40960;
constexpr int B_RED = 24576;
constexpr int B_TAB = 32768;
#define PPOS 4
#define NP1 6
#define NPRO 8
#define WL 4
#define PK23 1
#define TQ1 4
#define TQ2 9
#define RSTEPS 11
#define PLATE 2
#define EPW 1
#define G2D 4
#define PRE 2

__device__ __forceinline__ float gelu_tanh(float x) {
    const float p = __builtin_fmaf(x * x, -2.302208198f * 0.044715f, -2.302208198f);
    const float e = __builtin_amdgcn_exp2f(p * x);
    return x * __builtin_amdgcn_rcpf(1.0f + e);
}
template <int I>
__device__ __forceinline__ void w_piece(__amdgpu_buffer_rsrc_t srd, unsigned v, unsigned so, unsigned m0v) {
    if (I == 0) asm volatile("s_mov_b32 m0, %0\n\ts_nop 0\n\tbuffer_load_dwordx4 %1, %2, %3 offen lds"
                             :: "s"(m0v), "v"(v), "s"(srd), "s"(so) : "m0", "memory");
    else asm volatile("buffer_load_dwordx4 %0, %1, %2 offen offset:%3 lds"
                      :: "v"(v), "s"(srd), "s"(so), "n"(I * 1024) : "memory");
}
__device__ __forceinline__ void w_issue(__amdgpu_buffer_rsrc_t srd, const unsigned (&v)[4], unsigned so, unsigned m0v) {
    m0v = __builtin_amdgcn_readfirstlane(m0v);
    asm volatile("s_mov_b32 m0, %0\n\ts_nop 0\n\t"
                 "buffer_load_dwordx4 %1, %5, %6 offen lds\n\tbuffer_load_dwordx4 %2, %5, %6 offen offset:1024 lds\n\t"
                 "buffer_load_dwordx4 %3, %5, %6 offen offset:2048 lds\n\tbuffer_load_dwordx4 %4, %5, %6 offen offset:3072 lds"
                 :: "s"(m0v), "v"(v[0]), "v"(v[1]), "v"(v[2]), "v"(v[3]), "s"(srd), "s"(so) : "m0", "memory");
}
template <int SC, int SL, int J>
__device__ __forceinline__ void a_piece(__amdgpu_buffer_rsrc_t srd, unsigned av0, unsigned av1, unsigned so) {
    if (SC) asm volatile("buffer_load_dwordx4 a[%c0:%c1], %2, %3, %4 offen offset:%5 sc1"
                         :: "i"(SL * 20 + J * 4), "i"(SL * 20 + J * 4 + 3), "v"(J < 4 ? av0 : av1), "s"(srd), "s"(so), "n"((J & 3) * 1024) : "memory");
    else asm volatile("buffer_load_dwordx4 a[%c0:%c1], %2, %3, %4 offen offset:%5"
                      :: "i"(SL * 20 + J * 4), "i"(SL * 20 + J * 4 + 3), "v"(J < 4 ? av0 : av1), "s"(srd), "s"(so), "n"((J & 3) * 1024) : "memory");
}
template <int SC, int SL>
__device__ __forceinline__ void a_issue(__amdgpu_buffer_rsrc_t srd, unsigned av0, unsigned av1, unsigned so) {
    a_piece<SC, SL, 0>(srd, av0, av1, so); a_piece<SC, SL, 1>(srd, av0, av1, so); a_piece<SC, SL, 2>(srd, av0, av1, so);
    a_piece<SC, SL, 3>(srd, av0, av1, so); a_piece<SC, SL, 4>(srd, av0, av1, so);
}
template <int N> __device__ __forceinline__ void vm_wait() { asm volatile("s_waitcnt vmcnt(%0)" :: "n"(N) : "memory"); }
template <int R>
__device__ __forceinline__ void affine4(f32x4& d, u32x2 al, f32x4 be) {
    asm volatile("v_mfma_f32_4x4x4_16b_bf16 %0, %1, a[%c2:%c3], %4" : "=&v"(d) : "v"(al), "i"(R), "i"(R + 1), "v"(be));
}
template <int INIT>
__device__ __forceinline__ void mma_vv(f32x4& c, u32x4 a, u32x4 b, f32x4 c0) {
    if (INIT == 0) asm volatile("v_mfma_f32_16x16x32_bf16 %0, %1, %2, %0" : "+v"(c) : "v"(a), "v"(b));
    if (INIT == 1) asm volatile("v_mfma_f32_16x16x32_bf16 %0, %1, %2, 0" : "=&v"(c) : "v"(a), "v"(b));
    if (INIT == 2) asm volatile("v_mfma_f32_16x16x32_bf16 %0, %1, %2, %3" : "=&v"(c) : "v"(a), "v"(b), "v"(c0));
}
template <int INIT, int R>
__device__ __forceinline__ void mma_av(f32x4& c, u32x4 b, f32x4 c0) {
    if (INIT == 0) asm volatile("v_mfma_f32_16x16x32_bf16 %0, a[%c1:%c2], %3, %0" : "+v"(c) : "i"(R), "i"(R + 3), "v"(b));
    if (INIT == 1) asm volatile("v_mfma_f32_16x16x32_bf16 %0, a[%c1:%c2], %3, 0" : "=&v"(c) : "i"(R), "i"(R + 3), "v"(b));
    if (INIT == 2) asm volatile("v_mfma_f32_16x16x32_bf16 %0, a[%c1:%c2], %3, %4" : "=&v"(c) : "i"(R), "i"(R + 3), "v"(b), "v"(c0));
}
template <int INIT, int R, int Q>
__device__ __forceinline__ void mma_aa(f32x4& c, f32x4 c0) {
    if (INIT == 0) asm volatile("v_mfma_f32_16x16x32_bf16 %0, a[%c1:%c2], a[%c3:%c4], %0" : "+v"(c) : "i"(R), "i"(R + 3), "i"(Q), "i"(Q + 3));
    if (INIT == 1) asm volatile("v_mfma_f32_16x16x32_bf16 %0, a[%c1:%c2], a[%c3:%c4], 0" : "=&v"(c) : "i"(R), "i"(R + 3), "i"(Q), "i"(Q + 3));
    if (INIT == 2) asm volatile("v_mfma_f32_16x16x32_bf16 %0, a[%c1:%c2], a[%c3:%c4], %5" : "=&v"(c) : "i"(R), "i"(R + 3), "i"(Q), "i"(Q + 3), "v"(c0));
}
#define SB() __builtin_amdgcn_sched_barrier(0)
struct Nrm { u32x2 a0, a1; f32x4 b0, b1; };
struct KbCtx {
    __amdgpu_buffer_rsrc_t srdW1, srdW2, srdL, srdA, srdH;
    unsigned wv1[4], wv2[4], av0, av1, ring, w1o, a1o, w2o, a2o, wlo[2], alo[2];
    const char* tp[4];
    unsigned tpa[4];
    const char* ta;
    const char* tb;
    char* red;
    f32x4 bias;
};
__device__ __forceinline__ unsigned w1_off(const KbCtx& c, int g) { return c.w1o + g * (32 * F_ * 2); }
__device__ __forceinline__ unsigned a1_off(const KbCtx& c, int g) { return c.a1o + g * 40960; }
__device__ __forceinline__ unsigned w2_off(const KbCtx& c, int g) { return g >= 12 ? c.wlo[g & 1] : c.w2o + g * (32 * L_ * 2); }
__device__ __forceinline__ unsigned a2_off(const KbCtx& c, int g) { return g >= 12 ? c.alo[g & 1] : c.a2o + g * 40960; }
template <int R>
__device__ __forceinline__ void w_read(u32x4 (&wf)[4], const KbCtx& c, int SL) {
    const u32x2 v = tr_read(c.tp[R >> 1] + SL * 4096 + (R & 1) * 512);
    if (R & 1) { wf[R >> 1].z = v.x; wf[R >> 1].w = v.y; } else { wf[R >> 1].x = v.x; wf[R >> 1].y = v.y; }
}
template <int I>
__device__ __forceinline__ void nrm_read(Nrm& t, const KbCtx& c, int S) {
    if (I == 0) { t.a0 = *(const u32x2*)(c.ta + S * 256); t.a1 = *(const u32x2*)(c.ta + S * 256 + 32); }
    if (I == 1) t.b0 = *(const f32x4*)(c.tb + S * 128);
    if (I == 2) t.b1 = *(const f32x4*)(c.tb + S * 128 + 16);
}
template <int T>
__device__ __forceinline__ void red_put(const f32x4 (&acc)[4][5], const KbCtx& c) {
    if constexpr (T >= 8) *(f32x4*)(c.red + (T - 8) * 1024) = acc[(T - 8) / 5 + 1][(T - 8) % 5];
    if constexpr (T == 19) {
        SB();
#pragma unroll
        for (int j = 2; j < 5; ++j) *(f32x4*)(c.red + (10 + j) * 1024) = acc[3][j];
    }
}
constexpr int G1Q = (16 - NPRO) * 9;
constexpr int g1_cum(int s) {
    return s <= RSTEPS ? (G1Q * s + RSTEPS - 1) / RSTEPS : G1Q + (s >= 15 ? 4 * WL : 4 * WL * (s - RSTEPS) / (15 - RSTEPS));
}
template <int S, int P>
__device__ __forceinline__ void g1_req(const KbCtx& c) {
    constexpr int q = g1_cum(S) + P;
    static_assert(g1_cum(S + 1) - g1_cum(S) <= 9, "");
    if constexpr (q < g1_cum(S + 1)) {
        if constexpr (q < G1Q) {
            constexpr int g = NPRO + q / 9, r = q % 9;
            static_assert(g - 8 <= S && g >= S + 2, "");
            if constexpr (r < 4) w_piece<r>(c.srdW1, c.wv1[r], w1_off(c, g), c.ring + (g & 7) * 4096);
            else a_piece<0, g & 7, r - 4>(c.srdH, c.av0, c.av1, a1_off(c, g));
        } else {
            constexpr int g = (q - G1Q) / 4, r = (q - G1Q) % 4;
            static_assert(g + 8 <= S, "");
            w_piece<r>(c.srdW2, c.wv2[r], w2_off(c, g), c.ring + g * 4096);
        }
    }
}
template <int SL, int R>
__device__ __forceinline__ void w_park(const KbCtx& c) {
    asm volatile("ds_read_b64_tr_b16 a[%c0:%c1], %2 offset:%3"
                 :: "i"(160 + SL * 16 + R * 2), "i"(161 + SL * 16 + R * 2), "v"(c.tpa[R >> 1]), "n"(SL * 4096 + (R & 1) * 512) : "memory");
}
template <int SL> __device__ __forceinline__ void w_park8(const KbCtx& c) {
    w_park<SL, 0>(c); w_park<SL, 1>(c); w_park<SL, 2>(c); w_park<SL, 3>(c); w_park<SL, 4>(c); w_park<SL, 5>(c); w_park<SL, 6>(c); w_park<SL, 7>(c);
}
template <int S, int T>
__device__ __forceinline__ void g1_mma(f32x4 (&acc)[4][5], u32x4 (&n)[2][5], u32x4 (&wf)[2][4], const KbCtx& c) {
    mma_vv<S ? 0 : (T & 3) ? 1 : 2>(acc[T & 3][T >> 2], wf[S & 1][T & 3], n[S & 1][T >> 2], c.bias);
    SB();
}
constexpr int g1_aux(int t) {
    constexpr int a[20] = {0, 1, 2, 3, 4, -1, 5, -2, 6, -3, 7, -4, 8, 9, 10, -5, -6, -7, -8, -9};
    return a[t];
}
template <int S, int T>
__device__ __forceinline__ void g1_body(f32x4 (&acc)[4][5], u32x4 (&n)[2][5], u32x4 (&wf)[2][4], f32x4 (&d)[10], Nrm& t, const KbCtx& c) {
    g1_mma<S, T>(acc, n, wf, c);
    n[(S + 1) & 1][T >> 2][T & 3] = pk2(d[T >> 1][(T & 1) * 2], d[T >> 1][(T & 1) * 2 + 1]);
    constexpr int a = g1_aux(T);
    if constexpr (a < 0) g1_req<S, -1 - a>(c);
    if constexpr (a >= 3) w_read<a - 3>(wf[(S + 1) & 1], c, (S + 1) & 7);
    if constexpr (a >= 0 && a < 3 && S < 14) nrm_read<a>(t, c, S + 2);
    SB();
    if constexpr (T < 19) g1_body<S, T + 1>(acc, n, wf, d, t, c);
}
template <int S, int T>
__device__ __forceinline__ void g1_mid(f32x4 (&d)[10], const Nrm& t) {
    affine4<(S & 7) * 20 + T * 2>(d[T], (T & 1) ? t.a1 : t.a0, (T & 1) ? t.b1 : t.b0);
    SB();
    if constexpr (T < 9) g1_mid<S, T + 1>(d, t);
}
template <int T>
__device__ __forceinline__ void g1_last(f32x4 (&acc)[4][5], u32x4 (&n)[5], u32x4 (&wf)[4], const KbCtx& c) {
    constexpr int i = (T / 5 + 1) & 3, j = T % 5;
    mma_vv<0>(acc[i][j], wf[i], n[j], c.bias);
    SB();
    if constexpr (T < 8) { w_park<T / 4, (T & 3) * 2>(c); w_park<T / 4, (T & 3) * 2 + 1>(c); }
    red_put<T>(acc, c);
    constexpr int q0 = (24 - 4 * WL) * T / 20, q1 = (24 - 4 * WL) * (T + 1) / 20;
    if constexpr (q1 > q0) w_piece<q0 & 3>(c.srdW2, c.wv2[q0 & 3], w2_off(c, WL + q0 / 4), c.ring + (WL + q0 / 4) * 4096);
    SB();
    if constexpr (T < 19) g1_last<T + 1>(acc, n, wf, c);
}
template <int N> __device__ __forceinline__ void all_wait() {
    asm volatile("" ::: "memory");
    __builtin_amdgcn_s_waitcnt((N & 15) | 0x70 | ((N >> 4) << 14));
    asm volatile("" ::: "memory");
    SB();
}
template <int S>
__device__ __forceinline__ void g1_step(f32x4 (&acc)[4][5], u32x4 (&n)[2][5], u32x4 (&wf)[2][4], Nrm& t, const KbCtx& c) {
    if constexpr (S == 15) {
        all_wait<4 * WL - 8>();
        g1_last<0>(acc, n[1], wf[1], c);
    } else {
        constexpr int young = NPRO * 9 + g1_cum(S) - (S + 2) * 9;
        static_assert(young >= 0 && young + g1_cum(S + 1) - g1_cum(S) <= 63, "");
        all_wait<young>();
        f32x4 d[10];
        g1_mid<S + 1, 0>(d, t);
        g1_body<S, 0>(acc, n, wf, d, t, c);
        g1_step<S + 1>(acc, n, wf, t, c);
    }
}
constexpr int g2_slot(int s) { return s < 6 ? s : s < 10 ? s - 6 : s - 4; }
template <int S, int P>
__device__ __forceinline__ void g2_req(const KbCtx& c) {
    if constexpr (S < 14) a_piece<1, S & 7, P>(c.srdA, c.av0, c.av1, a2_off(c, S));
}
template <int S, int T>
__device__ __forceinline__ void g2_body(f32x4 (&acc)[4][5], u32x4 (&wf)[2][4], const KbCtx& c) {
    constexpr int i = (T / 5 + 1) & 3, j = T % 5;
    if constexpr (S < 4) mma_aa<S ? 0 : i ? 1 : 2, (S & 7) * 20 + j * 4, 160 + S * 16 + i * 4>(acc[i][j], c.bias);
    else mma_av<0, (S & 7) * 20 + j * 4>(acc[i][j], wf[S & 1][i], c.bias);
    SB();
    if constexpr (S == 13) red_put<T>(acc, c);
    else {
        if constexpr (T % 4 == 0) g2_req<S + G2D, T / 4>(c);
        else if constexpr (S >= 3 && T > 5 && T < 17) w_read<(T - T / 4 - 3) & 7>(wf[(S + 1) & 1], c, g2_slot(S + 1));
    }
    SB();
    if constexpr (T < 19) g2_body<S, T + 1>(acc, wf, c);
}
template <int S>
__device__ __forceinline__ void g2_step(f32x4 (&acc)[4][5], u32x4 (&wf)[2][4], const KbCtx& c) {
    vm_wait<5 * (S + G2D > 14 ? 13 - S : G2D - 1)>();
    if (S == 0) STAMPX(4, 1);
    SB();
    g2_body<S, 0>(acc, wf, c);
    if constexpr (S < 13) g2_step<S + 1>(acc, wf, c);
}
__device__ __forceinline__ void kb_reduce(f32x4 (&r)[5], const f32x4 (&acc)[4][5], char* smem, int w, int lane) {
    asm volatile("s_waitcnt lgkmcnt(0)\n\ts_barrier" ::: "memory");
    f32x4 p[5][3];
#pragma unroll
    for (int j = 0; j < 5; ++j)
#pragma unroll
        for (int i = 1; i < 4; ++i)
            p[j][i - 1] = *(const f32x4*)(smem + ((w - i) & 3) * B_REG + B_RED + lane * 16 + (i - 1) * 5120 + j * 1024);
    SB();
#pragma unroll
    for (int j = 0; j < 5; ++j) r[j] = (acc[0][j] + p[j][0]) + (p[j][1] + p[j][2]);
}
template <int S0, int S1>
__device__ __forceinline__ void g1_groups(const KbCtx& c) {
    if constexpr (S0 < S1) {
        if (S0 >= 3) vm_wait<54>();
        w_issue(c.srdW1, c.wv1, w1_off(c, S0), c.ring + S0 * 4096);
        a_issue<0, S0>(c.srdH, c.av0, c.av1, a1_off(c, S0));
        g1_groups<S0 + 1, S1>(c);
    }
}
template <int Q0, int Q1>
__device__ __forceinline__ void g1_tail(const KbCtx& c) {
    if constexpr (Q0 < Q1 && Q0 < (NPRO - NP1) * 9) {
        constexpr int g = NP1 + Q0 / 9, r = Q0 % 9;
        if constexpr (r < 4) w_piece<r>(c.srdW1, c.wv1[r], w1_off(c, g), c.ring + g * 4096);
        else a_piece<0, g, r - 4>(c.srdH, c.av0, c.av1, a1_off(c, g));
        g1_tail<Q0 + 1, Q1>(c);
    }
    SB();
}
template <int T>
__device__ __forceinline__ void g1_mid0(f32x4 (&d)[10], const Nrm& t, const KbCtx& c) {
    affine4<T * 2>(d[T], (T & 1) ? t.a1 : t.a0, (T & 1) ? t.b1 : t.b0);
    SB();
    if constexpr (T & 1) g1_tail<TQ2 + T / 2, TQ2 + T / 2 + 1>(c);
    if constexpr (T < 9) g1_mid0<T + 1>(d, t, c);
}
template <int S>
__device__ __forceinline__ void g2_first(const KbCtx& c) {
    a_issue<1, S>(c.srdA, c.av0, c.av1, a2_off(c, S));
    if constexpr (S < G2D - 1) g2_first<S + 1>(c);
}
template <int CTRL> __device__ __forceinline__ f32x2 dpp_add2(f32x2 v) { return (f32x2){dpp_add<CTRL>(v.x), dpp_add<CTRL>(v.y)}; }

extern "C" __global__ __launch_bounds__(256, 1) void kb_mlp(const u16* __restrict__ w1, const u16* __restrict__ b1, const u16* __restrict__ w2,
                                                 const u16* __restrict__ b2, char* ws, u16* __restrict__ out) {
    __shared__ __attribute__((aligned(16))) char smem[4 * B_REG];
    const int tid = threadIdx.x, lane = tid & 63;
    const int w = __builtin_amdgcn_readfirstlane(tid >> 6);
    STAMP(2, 0);
    const int n = lane & 15, hg = lane >> 4;
    KbCtx c;
    c.srdA = __builtin_amdgcn_make_buffer_rsrc((void*)ws, (short)0, WS_END, 0x00020000);
    u32x4 hs[3];
    {
        const unsigned v = hg * 1024 + n * 16;
        asm volatile("buffer_load_dwordx4 %0, %3, %5, %6 offen\n\tbuffer_load_dwordx4 %1, %3, %5, %7 offen\n\tbuffer_load_dwordx4 %2, %4, %5, %7 offen"
                     : "=&v"(hs[0]), "=&v"(hs[1]), "=&v"(hs[2])
                     : "v"(v), "v"(v + 4096 + (lane >> 5) * WS_END), "s"(c.srdA),
                       "s"(WS_HB + ((blockIdx.x >> 2) * 40 + 10 * w) * 1024 + (blockIdx.x & 3) * 256),
                       "s"(WS_HB + ((blockIdx.x >> 2) * 40 + 10 * w + 4) * 1024 + (blockIdx.x & 3) * 256) : "memory");
    }
    const int xcd = blockIdx.x & 7, idx = blockIdx.x >> 3;
    const bool g1 = idx < 27;
    const int t1 = (xcd >> 1) * 27 + (g1 ? idx : 0), t2 = (xcd >> 1) * 32 + idx;
    const int mt1 = t1 >> 2, dt1 = (xcd & 1) * 4 + (t1 & 3), mt2 = mt2_of(xcd >> 1, idx >> 2), dt2 = (xcd & 1) * 4 + (t2 & 3);
    char* ring = smem + w * B_REG;
    c.ring = (unsigned)(size_t)(LDS_AS char*)ring;
    c.srdW1 = __builtin_amdgcn_make_buffer_rsrc((void*)w1, (short)0, g1 ? L_ * F_ * 2 : 0, 0x00020000);
    c.srdH = __builtin_amdgcn_make_buffer_rsrc((void*)ws, (short)0, g1 ? WS_END : 0, 0x00020000);
    c.srdW2 = __builtin_amdgcn_make_buffer_rsrc((void*)w2, (short)0, L_ * F_ * 2, 0x00020000);
    const bool idw = w == 3;
    c.srdL = __builtin_amdgcn_make_buffer_rsrc((void*)(idw ? ws + WS_ID : (const char*)w2), (short)0, L_ * F_ * 2, 0x00020000);
    {
        const int wr = lane >> 3, wslot = lane & 7;
        const unsigned s0 = ((((wslot >> 1) ^ ((wr >> 1) & 1)) * 2 + (wslot & 1)) * 8) * 2;
        const unsigned s1 = ((((wslot >> 1) ^ (((wr >> 1) & 1) | 2)) * 2 + (wslot & 1)) * 8) * 2;
        c.wv1[0] = wr * F_ * 2 + s0; c.wv1[1] = (wr + 8) * F_ * 2 + s1 - 1024;
        c.wv1[2] = c.wv1[0] + 16 * F_ * 2 - 2048; c.wv1[3] = c.wv1[1] + 16 * F_ * 2 - 2048;
        c.wv2[0] = wr * L_ * 2 + s0; c.wv2[1] = (wr + 8) * L_ * 2 + s1 - 1024;
        c.wv2[2] = c.wv2[0] + 16 * L_ * 2 - 2048; c.wv2[3] = c.wv2[1] + 16 * L_ * 2 - 2048;
    }
    c.av0 = lane * 16; c.av1 = lane * 16 + 4096;
    c.w1o = (w * 16 * 32 * F_ + mt1 * 64) * 2;
    c.a1o = WS_HB + (w * 16 * 40 + dt1 * 5) * 1024;
    c.w2o = (w * 14 * 32 * L_ + mt2 * 64) * 2;
    c.a2o = WS_GB + (w * 14 * 40 + dt2 * 5) * 1024;
#pragma unroll
    for (int i = 0; i < 2; ++i) {
        c.wlo[i] = idw ? i * (32 * L_ * 2) : c.w2o + (12 + i) * (32 * L_ * 2);
        c.alo[i] = idw ? WS_HB + ((mt2 * 2 + i) * 40 + dt2 * 5) * 1024 : c.a2o + (12 + i) * 40960;
    }
    u32x2 bb;
    asm volatile("global_load_dwordx2 %0, %1, %2" : "=v"(bb) : "v"(hg * 8), "s"(b1 + mt1 * 64 + 16 * w) : "memory");
    asm volatile("" ::: "a0", "a223");
    g1_groups<0, PRE>(c);
    {
        const int q4 = n >> 2, p4 = n & 3;
        const int fsw = ((q4 >> 1) & 1) | ((hg & 1) << 1);
        const char* tb = ring + (8 * hg + q4) * 128 + p4 * 8;
#pragma unroll
        for (int i = 0; i < 4; ++i) {
            c.tp[i] = tb + ((((i + w) & 3) ^ fsw) * 32);
            c.tpa[i] = (unsigned)(size_t)(LDS_AS const char*)c.tp[i];
        }
        c.ta = ring + B_TAB + (8 * hg + (lane & 3)) * 8;
        c.tb = ring + B_TAB + 4096 + hg * 32;
        c.red = ring + B_RED + lane * 16;
    }
    const __amdgpu_buffer_rsrc_t srdG = __builtin_amdgcn_make_buffer_rsrc((void*)(ws + WS_G2), (short)0, L_ * 8, 0x00020000);
    {
        const unsigned one = (n & 1) ? 0x3f800000u : 0x3f80u;
        const int dw = (hg & 1) == (n >> 3) ? (n >> 1) & 3 : 4;
        const u32x4 sel = {dw == 0 ? one : 0u, dw == 1 ? one : 0u, dw == 2 ? one : 0u, dw == 3 ? one : 0u};
        const u32x4 z4 = {0u, 0u, 0u, 0u};
        const u32x4 selA = hg < 2 ? sel : z4, selB = hg < 2 ? z4 : sel;
        f32x4 sum, x0, x1, x2, x3, x4;
        asm volatile("s_waitcnt vmcnt(%12)\n\t"
                     "v_mfma_f32_16x16x32_bf16 %1, %6, %9, 0\n\tv_mfma_f32_16x16x32_bf16 %2, %6, %10, 0\n\t"
                     "v_mfma_f32_16x16x32_bf16 %3, %7, %9, 0\n\tv_mfma_f32_16x16x32_bf16 %4, %7, %10, 0\n\t"
                     "v_mfma_f32_16x16x32_bf16 %5, %8, %9, 0\n\tv_mfma_f32_16x16x32_bf16 %0, %6, %11, 0\n\t"
                     "v_mfma_f32_16x16x32_bf16 %0, %7, %11, %0\n\tv_mfma_f32_16x16x32_bf16 %0, %8, %11, %0\n\ts_nop 4"
                     : "=&v"(sum), "=&v"(x0), "=&v"(x1), "=&v"(x2), "=&v"(x3), "=&v"(x4)
                     : "v"(hs[0]), "v"(hs[1]), "v"(hs[2]), "v"(selA), "v"(selB), "v"(sel), "n"(9 * PRE + 1) : "memory");
        STAMPX(2, 1);
        f32x4 q = (x0 * x0 + x1 * x1) + (x2 * x2 + x3 * x3) + x4 * x4;
        asm volatile("s_nop 2" : "+v"(sum), "+v"(q));
        char* sc = smem + B_TAB + 6144;
        *(f32x2*)(sc + (n & 7) * 256 + (w * 8 + hg * 2 + (n >> 3)) * 8) = (f32x2){(sum[0] + sum[1]) + (sum[2] + sum[3]), (q[0] + q[1]) + (q[2] + q[3])};
        asm volatile("s_waitcnt lgkmcnt(0)\n\ts_barrier" ::: "memory");
        if (w == 0) {
            const f32x4 p0 = *(const f32x4*)(sc + lane * 32), p1 = *(const f32x4*)(sc + lane * 32 + 16);
            f32x2 v = {(p0[0] + p0[2]) + (p1[0] + p1[2]), (p0[1] + p0[3]) + (p1[1] + p1[3])};
            v = dpp_add2<0xb1>(v);
            v = dpp_add2<0x4e>(v);
            v = dpp_add2<0x141>(v);
            const float mean = v.x * (1.0f / D_);
            const float var = fmaxf(__builtin_fmaf(-mean, mean, v.y * (1.0f / D_)), 0.f);
            const float m1 = rb(mean), d1 = rb(__builtin_amdgcn_sqrtf(rb(rb(var) + 1e-5f)));
            const float i1 = __builtin_amdgcn_rcpf(d1);
            const float m2 = rb((mean - m1) * i1), d2 = rb(__builtin_amdgcn_sqrtf(rb(rb(var * i1 * i1) + 1e-5f)));
            const float al = rb(i1 * __builtin_amdgcn_rcpf(d2));
            const float be = -__builtin_fmaf(m2, d1, m1) * al;
            asm volatile("buffer_store_dwordx2 %0, %1, %2, %3 offen sc1"
                         :: "v"((u32x2){__float_as_uint(be), __float_as_uint(al) | 1u}), "v"((lane & 7) * (L_ * 8) + gran_pos(lane >> 3)), "s"(srdG), "s"(gran_pos(blockIdx.x * 8)) : "memory");
            STAMP(2, 2);
        }
    }
    f32x4 acc[4][5];
    u32x4 wf[2][4];
    unsigned f, bq;
    const unsigned fo = (lane < (w == 3 ? 6 : 7) ? lane : 0) * 128;
    const char* fp = ws + WS_FL + (dt2 * 32 + 7 * w) * 128;
    if (g1) {
        g1_groups<PRE, PPOS>(c);
        u32x4 g[4];
        const unsigned gvo = lane * 16;
        asm volatile("s_waitcnt vmcnt(59)\n\t"
                     "buffer_load_dwordx4 %0, %4, %5, %6 offen sc1\n\tbuffer_load_dwordx4 %1, %4, %5, %6 offen offset:1024 sc1\n\t"
                     "buffer_load_dwordx4 %2, %4, %5, %6 offen offset:2048 sc1\n\tbuffer_load_dwordx4 %3, %4, %5, %6 offen offset:3072 sc1"
                     : "=&v"(g[0]), "=&v"(g[1]), "=&v"(g[2]), "=&v"(g[3]) : "v"(gvo), "s"(srdG), "s"(w * 4096) : "memory");
        g1_groups<PPOS, NP1>(c);
        STAMPX(2, 3);
        asm volatile("s_waitcnt vmcnt(%5)" : "+v"(g[0]), "+v"(g[1]), "+v"(g[2]), "+v"(g[3]), "+v"(bb) : "n"(9 * (NP1 - PPOS)) : "memory");
        STAMPX(2, 4);
        unsigned spins = 0;
#pragma unroll 1
        for (; spins < (1u << 16); ++spins) {
            if (__all(g[0].y & g[0].w & g[1].y & g[1].w & g[2].y & g[2].w & g[3].y & g[3].w & 1u)) break;
            asm volatile("buffer_load_dwordx4 %0, %4, %5, %6 offen sc1\n\tbuffer_load_dwordx4 %1, %4, %5, %6 offen offset:1024 sc1\n\t"
                         "buffer_load_dwordx4 %2, %4, %5, %6 offen offset:2048 sc1\n\tbuffer_load_dwordx4 %3, %4, %5, %6 offen offset:3072 sc1\n\t"
                         "s_waitcnt vmcnt(0)"
                         : "=&v"(g[0]), "=&v"(g[1]), "=&v"(g[2]), "=&v"(g[3]) : "v"(gvo), "s"(srdG), "s"(w * 4096) : "memory");
        }
        STAMPX(2, 5);
        {
            char* tw = ring + B_TAB + lane * 64;
#pragma unroll
            for (int k = 0; k < 4; ++k) {
                const unsigned lo = g[k].y >> 16, hi = g[k].w & 0xffff0000u;
                *(u32x4*)(tw + k * 16) = (k & 1) ? (u32x4){0u, lo, 0u, hi} : (u32x4){lo, 0u, hi, 0u};
            }
            *(u32x4*)(ring + B_TAB + 4096 + lane * 32) = (u32x4){g[0].x, g[0].z, g[1].x, g[1].z};
            *(u32x4*)(ring + B_TAB + 4096 + lane * 32 + 16) = (u32x4){g[2].x, g[2].z, g[3].x, g[3].z};
            asm volatile("" ::: "memory");
        }
        Nrm t;
        u32x4 nf[2][5];
        nrm_read<0>(t, c, 0); nrm_read<1>(t, c, 0); nrm_read<2>(t, c, 0);
        SB();
        g1_tail<0, TQ1>(c);
        w_read<0>(wf[0], c, 0); w_read<1>(wf[0], c, 0); w_read<2>(wf[0], c, 0); w_read<3>(wf[0], c, 0);
        w_read<4>(wf[0], c, 0); w_read<5>(wf[0], c, 0); w_read<6>(wf[0], c, 0); w_read<7>(wf[0], c, 0);
        SB();
        g1_tail<TQ1, TQ2>(c);
        {
            c.bias = (f32x4){bflo(bb.x), bfhi(bb.x), bflo(bb.y), bfhi(bb.y)};
            asm volatile("" : "+v"(c.bias));
            f32x4 d[10];
            SB();
            g1_mid0<0>(d, t, c);
            nrm_read<0>(t, c, 1); nrm_read<1>(t, c, 1); nrm_read<2>(t, c, 1);
            asm volatile("s_nop 4");
            SB();
#pragma unroll
            for (int k = 0; k < 20; ++k) nf[0][k >> 2][k & 3] = pk2(d[k >> 1][(k & 1) * 2], d[k >> 1][(k & 1) * 2 + 1]);
            SB();
            g1_tail<TQ2 + 5, 27>(c);
        }
        STAMP(3, 1);
        g1_step<0>(acc, nf, wf, t, c);
        STAMP(3, 2);
        f32x4 r[5];
        kb_reduce(r, acc, smem, w, lane);
        STAMPX(3, 3);
        const unsigned go = WS_GB + ((mt1 * 2 + (w >> 1)) * 40 + dt1 * 5) * 1024;
        const unsigned gv = (((w & 1) * 2 + (hg >> 1)) * 16 + n) * 16 + (hg & 1) * 8;
        if (!PK23) {
            vm_wait<8>();
            w_park8<2>(c);
            w_park8<3>(c);
        }
#pragma unroll
        for (int j = 0; j < 5; ++j) {
            if (j == 2) asm volatile("s_waitcnt lgkmcnt(0)" ::: "memory");
            if (j < EPW) w_issue(c.srdW2, c.wv2, w2_off(c, 6 + j), c.ring + j * 4096);
            SB();
            u32x2 o;
            o.x = pk2(gelu_tanh(r[j][0]), gelu_tanh(r[j][1]));
            o.y = pk2(gelu_tanh(r[j][2]), gelu_tanh(r[j][3]));
            __builtin_amdgcn_raw_buffer_store_b64(o, c.srdA, gv + j * 1024, go, 16);
            SB();
        }
        STAMPX(3, 4);
        asm volatile("s_waitcnt vmcnt(0)" ::: "memory");
        if (PK23) {
            w_park8<2>(c);
            w_park8<3>(c);
        }
        asm volatile("s_barrier" ::: "memory");
        if (tid == 0) __hip_atomic_store((gu32*)(ws + WS_FL + (dt1 * 32 + mt1) * 128), 1u, __ATOMIC_RELAXED, __HIP_MEMORY_SCOPE_AGENT);
        STAMP(3, 5);
        asm volatile("s_waitcnt lgkmcnt(0)" ::: "memory");
#pragma unroll
        for (int s = 6 + EPW; s < 14; ++s) {
            if (s == 14 - PLATE)
                asm volatile("global_load_ushort %1, %4, %5\n\tglobal_load_dword %0, %2, %3 sc1"
                             : "=&v"(f), "=&v"(bq) : "v"(fo), "s"(fp), "v"(n * 2), "s"(b2 + mt2 * 64 + 16 * w) : "memory");
            w_issue(s < 12 ? c.srdW2 : c.srdL, c.wv2, w2_off(c, s), c.ring + g2_slot(s) * 4096);
        }
        asm volatile("s_waitcnt vmcnt(%2)" : "+v"(f), "+v"(bq) : "n"(4 * PLATE) : "memory");
    } else {
        const unsigned t0 = (unsigned)__builtin_amdgcn_s_memrealtime();
#pragma unroll
        for (int s = 0; s < 6; ++s) w_issue(c.srdW2, c.wv2, w2_off(c, s), c.ring + s * 4096);
        vm_wait<0>();
        w_park8<0>(c); w_park8<1>(c); w_park8<2>(c); w_park8<3>(c);
        asm volatile("s_waitcnt lgkmcnt(0)" ::: "memory");
#pragma unroll
        for (int s = 6; s < 13; ++s) w_issue(s < 12 ? c.srdW2 : c.srdL, c.wv2, w2_off(c, s), c.ring + g2_slot(s) * 4096);
        if ((CW & 16) && idx == 31) touch_kb(w, c.ring + 9 * 4096, lane);
        fetch_w2(w2, W2N + ((idx - 27) * 4 + w) * 8 + xcd, 864, 160, c.ring + 9 * 4096, lane);
        pause_until(t0, KT);
        warm_w<1>(w2, b2, xcd, (idx - 27) * 4 + w, 20, c.ring + 9 * 4096, lane);
        vm_wait<0>();
        w_issue(c.srdL, c.wv2, w2_off(c, 13), c.ring + g2_slot(13) * 4096);
        asm volatile("global_load_ushort %1, %4, %5\n\tglobal_load_dword %0, %2, %3 sc1\n\ts_waitcnt vmcnt(0)"
                     : "=&v"(f), "=&v"(bq) : "v"(fo), "s"(fp), "v"(n * 2), "s"(b2 + mt2 * 64 + 16 * w) : "memory");
        STAMP(3, 6);
    }
    {
        unsigned spins = 0;
#pragma unroll 1
        for (; spins < (1u << 16); ++spins) {
            if (__all(f != 0)) break;
            asm volatile("global_load_dword %0, %1, %2 sc1\n\ts_waitcnt vmcnt(0)" : "=&v"(f) : "v"(fo), "s"(fp) : "memory");
        }
    }
    STAMP(4, 0);
    g2_first<0>(c);
    {
        const float b = bflo(bq);
        c.bias = (f32x4){b, b, b, b};
        asm volatile("" : "+v"(c.bias));
    }
    SB();
    g2_step<0>(acc, wf, c);
    STAMP(4, 2);
    f32x4 r[5];
    kb_reduce(r, acc, smem, w, lane);
    STAMPX(4, 3);
    {
        u16* op = out + (size_t)(mt2 * 64 + 16 * w + n) * D_ + dt2 * 80 + 20 * hg;
        typedef unsigned u32x4u __attribute__((ext_vector_type(4), aligned(8)));
        u32x4u o0 = {pk2(r[0][0], r[0][1]), pk2(r[0][2], r[0][3]), pk2(r[1][0], r[1][1]), pk2(r[1][2], r[1][3])};
        u32x4u o1 = {pk2(r[2][0], r[2][1]), pk2(r[2][2], r[2][3]), pk2(r[3][0], r[3][1]), pk2(r[3][2], r[3][3])};
        u32x2 o2 = {pk2(r[4][0], r[4][1]), pk2(r[4][2], r[4][3])};
        *(u32x4u*)op = o0;
        *(u32x4u*)(op + 8) = o1;
        *(u32x2*)(op + 16) = o2;
    }
    STAMP(4, 4);
    asm volatile(".Lkb_mlp_last:");
}

extern "C" void kernel_launch(void* const* d_in, const int* in_sizes, int n_in, void* d_out, int out_size,
                              void* d_ws, size_t ws_size, hipStream_t stream) {
    const u16* x = (const u16*)d_in[0];
    const u16* logA = (const u16*)d_in[1];
    const u16* Aim = (const u16*)d_in[2];
    const u16* Cre = (const u16*)d_in[3];
    const u16* Cim = (const u16*)d_in[4];
    const u16* logdt = (const u16*)d_in[5];
    const u16* dskip = (const u16*)d_in[6];
    const u16* w1 = (const u16*)d_in[8];
    const u16* b1 = (const u16*)d_in[9];
    const u16* w2 = (const u16*)d_in[10];
    const u16* b2 = (const u16*)d_in[11];
    char* ws = (char*)d_ws;
    ka_conv<<<256, 512, 0, stream>>>(x, logA, Aim, Cre, Cim, logdt, dskip, ws, w1, b1, w2, b2);
    kb_mlp<<<256, 256, 0, stream>>>(w1, b1, w2, b2, ws, (u16*)d_out);
}
```
